```python
import torch

def _softmax(x, axis=-1):
    return torch.softmax(x, dim=axis)

def _gelu(x):
    return torch.nn.functional.gelu(x, approximate='tanh')

def _silu(x):
    return torch.nn.functional.silu(x)

def _sigmoid(x):
    return torch.sigmoid(x)

def _layernorm(x, axis=-1, eps=1e-05):
    m = x.mean(dim=axis, keepdim=True)
    v = x.var(dim=axis, keepdim=True, unbiased=False)
    return (x - m) / torch.sqrt(v + eps)

def _scatter_add(tgt, idx, src, axis=0):
    return tgt.clone().index_add_(axis, idx, src)

def _segment_sum(src, seg, n):
    out = torch.zeros((n,) + src.shape[1:], dtype=src.dtype)
    return out.index_add_(0, seg, src)

def _pad(x, pw):
    flat = tuple((v for pair in reversed(pw) for v in pair))
    return torch.nn.functional.pad(x, flat)
kernel_name = 'recurrent_scan__memory_aug_recurrent_mem_tr__dense_activation_squared_relu__prompt'

def setup_inputs():

    _w = lambda a: (lambda t: t.to(torch.bfloat16) if t.is_floating_point() else t)(torch.as_tensor(a))

    def _A_setup():
        B, S, Dm = (1, 2048, 512)
        H, Dh = (32, 128)
        M, seg = (8, 32)
        seg = min(seg, S)
        S = S // seg * seg
        return {'x': _w((lambda *s: torch.randn(*s, dtype=torch.bfloat16) * 0.05)(B, S, Dm)), 'mem0': _w((lambda *s: torch.randn(*s, dtype=torch.bfloat16) * 0.2)(B, M, Dm)), 'Wq': _w((lambda *s: torch.randn(*s, dtype=torch.bfloat16) * 1.6)(Dm, H, Dh) * Dm ** (-0.5)), 'Wk': _w((lambda *s: torch.randn(*s, dtype=torch.bfloat16) * 1.6)(Dm, H, Dh) * Dm ** (-0.5)), 'Wv': _w((lambda *s: torch.randn(*s, dtype=torch.bfloat16) * 0.2)(Dm, H, Dh) * Dm ** (-0.5)), 'Wo': _w((lambda *s: torch.randn(*s, dtype=torch.bfloat16) * 0.2)(H, Dh, Dm) * (H * Dh) ** (-0.5)), 'seg_len': seg}
    _A_in = _A_setup()

    def _B_setup():
        B, S, D, F = (1, 2048, 512, 1376)
        return {'x': _w((lambda *s: torch.randn(*s, dtype=torch.bfloat16) * 0.05)(B, S, D)), 'w1': _w((lambda *s: torch.randn(*s, dtype=torch.bfloat16) * 0.1)(D, F)), 'b1': _w((lambda *s: torch.randn(*s, dtype=torch.bfloat16) * 0.8)(F)), 'w2': _w((lambda *s: torch.randn(*s, dtype=torch.bfloat16) * 0.1)(F, D)), 'b2': _w((lambda *s: torch.randn(*s, dtype=torch.bfloat16) * 12.8)(D))}
    _B_in = _B_setup()
    return {'A_x': _A_in['x'], 'A_mem0': _A_in['mem0'], 'A_Wq': _A_in['Wq'], 'A_Wk': _A_in['Wk'], 'A_Wv': _A_in['Wv'], 'A_Wo': _A_in['Wo'], 'A_seg_len': _A_in['seg_len'], 'B_x': _B_in['x'], 'B_w1': _B_in['w1'], 'B_b1': _B_in['b1'], 'B_w2': _B_in['w2'], 'B_b2': _B_in['b2']}

def reference(A_x, A_mem0, A_Wq, A_Wk, A_Wv, A_Wo, A_seg_len, B_x, B_w1, B_b1, B_w2, B_b2):

    def _A_ref(x, mem0, Wq, Wk, Wv, Wo, seg_len):
        B, S, Dm = x.shape
        H, Dh = (Wq.shape[1], Wq.shape[2])
        M = mem0.shape[1]
        scale = Dh ** (-0.5)
        n_seg = S // seg_len
        L = M + seg_len
        idx = torch.arange(L)
        mask = (idx[:, None] < M) | (idx[:, None] >= idx[None, :])
        mem = mem0
        outs = []
        for s in range(n_seg):
            xs = x[:, s * seg_len:(s + 1) * seg_len, :].clone()
            h = torch.concatenate([mem, xs], axis=1)
            q = torch.einsum('btd,dhe->bhte', h, Wq)
            k = torch.einsum('btd,dhe->bhte', h, Wk)
            v = torch.einsum('btd,dhe->bhte', h, Wv)
            scores = torch.einsum('bhqe,bhke->bhqk', q, k) * scale
            scores = torch.where(mask[None, None], scores, -1000000000.0)
            p = _softmax(scores, axis=-1)
            o = torch.einsum('bhqk,bhke->bhqe', p, v)
            o = torch.einsum('bhte,hed->btd', o, Wo)
            mem = o[:, :M, :].clone()
            outs.append(o[:, M:, :])
        return torch.concatenate(outs, axis=1)

    def _B_ref(x, w1, b1, w2, b2):
        h = torch.einsum('bsd,df->bsf', x, w1) + b1
        zero = torch.zeros_like(h)
        r = torch.where(h > zero, h, zero)
        h = r * r
        out = torch.einsum('bsf,fd->bsd', h, w2) + b2
        return out
    x = A_x
    xn = _layernorm(x, axis=-1)
    s = _A_ref(xn, A_mem0, A_Wq, A_Wk, A_Wv, A_Wo, A_seg_len)
    g = _silu(_B_ref(xn, B_w1, B_b1, B_w2, B_b2))
    return x + s * g
```

```cpp
#include <hip/hip_runtime.h>
#include <hip/hip_bf16.h>
typedef unsigned short u16;
__device__ __forceinline__ float b2f(u16 x) { return __uint_as_float((unsigned)x << 16); }
__device__ __forceinline__ u16 f2b(float f) { return __builtin_bit_cast(u16, (__bf16)f); }
__device__ __forceinline__ float rb(float f) { return b2f(f2b(f)); }
__global__ void ln_k(const u16* x, u16* xn) {
  const int row = blockIdx.x, t = threadIdx.x;
  float v[8], s = 0;
  for (int j = 0; j < 8; ++j) { v[j] = b2f(x[row * 512 + t * 8 + j]); s += v[j]; }
  for (int o = 32; o; o >>= 1) s += __shfl_xor(s, o);
  const float mf = s / 512.f; float q = 0;
  for (int j = 0; j < 8; ++j) q += (v[j] - mf) * (v[j] - mf);
  for (int o = 32; o; o >>= 1) q += __shfl_xor(q, o);
  const float m = rb(mf), var = rb(q / 512.f), sd = rb(sqrtf(rb(var + 1e-5f)));
  for (int j = 0; j < 8; ++j) xn[row * 512 + t * 8 + j] = f2b(rb(v[j] - m) / sd);
}
__global__ void gemm_k(const u16* A, int lda, const u16* B, int ldb, u16* C, int ldc, int M, int N, int K, const u16* bias, int mode) {
  const int n = blockIdx.x * 64 + threadIdx.x, m0 = blockIdx.y * 8;
  if (n >= N) return;
  float acc[8] = {0, 0, 0, 0, 0, 0, 0, 0};
  for (int k = 0; k < K; ++k) { const float b = b2f(B[(size_t)k * ldb + n]);
    for (int i = 0; i < 8; ++i) acc[i] += b2f(A[(size_t)(m0 + i) * lda + k]) * b; }
  for (int i = 0; i < 8; ++i) { float v = rb(acc[i]);
    if (mode) v = rb(v + b2f(bias[n]));
    if (mode == 1) { v = v > 0 ? v : 0; v = v * v; }
    if (mode == 2) v = v / (1.f + __expf(-v));
    C[(size_t)(m0 + i) * ldc + n] = f2b(v); }
}
__global__ void attn_k(const u16* q, const u16* km, const u16* vm, const u16* kx, const u16* vx, u16* o, int rowbase) {
  const int h = blockIdx.x, r = blockIdx.y, t = threadIdx.x;
  __shared__ float p[40];
  const int L = rowbase + r;
  if (t < 40) { const u16* kp = t < 8 ? km + t * 4096 : kx + (t - 8) * 4096; float s = 0;
    for (int e = 0; e < 128; ++e) s += b2f(q[r * 4096 + h * 128 + e]) * b2f(kp[h * 128 + e]);
    s = rb(rb(s) * 0.08838834764831845f);
    p[t] = (L < 8 || L >= t) ? s : -1e9f; }
  __syncthreads();
  float m = -1e30f, l = 0; for (int j = 0; j < 40; ++j) m = fmaxf(m, p[j]);
  for (int j = 0; j < 40; ++j) l += __expf(p[j] - m);
  float acc = 0;
  for (int j = 0; j < 40; ++j) { const u16* vp = j < 8 ? vm + j * 4096 : vx + (j - 8) * 4096; acc += rb(__expf(p[j] - m) / l) * b2f(vp[h * 128 + t]); }
  o[r * 4096 + h * 128 + t] = f2b(acc);
}
__global__ void fin_k(const u16* x, const u16* s, const u16* g, u16* out) { const int i = blockIdx.x * 256 + threadIdx.x; out[i] = f2b(b2f(x[i]) + rb(b2f(s[i]) * b2f(g[i]))); }
extern "C" void kernel_launch(void* const* d_in, const int* in_sizes, int n_in, void* d_out, int out_size, void* d_ws, size_t ws_size, hipStream_t st) {
  const u16 *x = (const u16*)d_in[0], *mem0 = (const u16*)d_in[1], *Wq = (const u16*)d_in[2], *Wk = (const u16*)d_in[3], *Wv = (const u16*)d_in[4], *Wo = (const u16*)d_in[5];
  const u16 *w1 = (const u16*)d_in[8], *b1 = (const u16*)d_in[9], *w2 = (const u16*)d_in[10], *b2 = (const u16*)d_in[11];
  u16* ws = (u16*)d_ws;
  u16 *xn = ws, *Q = xn + 2048 * 512, *K = Q + 2048 * 4096, *V = K + 2048 * 4096, *O = V + 2048 * 4096, *Hh = O + 2048 * 4096, *G = Hh + 2048 * 1376, *S = G + 2048 * 512;
  u16 *mem = S + 2048 * 512, *qm = mem + 65 * 4096, *km = qm + 8 * 4096, *vm = km + 8 * 4096, *om = vm + 8 * 4096;
  ln_k<<<2048, 64, 0, st>>>(x, xn);
  gemm_k<<<dim3(64, 256), 64, 0, st>>>(xn, 512, Wq, 4096, Q, 4096, 2048, 4096, 512, nullptr, 0);
  gemm_k<<<dim3(64, 256), 64, 0, st>>>(xn, 512, Wk, 4096, K, 4096, 2048, 4096, 512, nullptr, 0);
  gemm_k<<<dim3(64, 256), 64, 0, st>>>(xn, 512, Wv, 4096, V, 4096, 2048, 4096, 512, nullptr, 0);
  gemm_k<<<dim3(22, 256), 64, 0, st>>>(xn, 512, w1, 1376, Hh, 1376, 2048, 1376, 512, b1, 1);
  gemm_k<<<dim3(8, 256), 64, 0, st>>>(Hh, 1376, w2, 512, G, 512, 2048, 512, 1376, b2, 2);
  for (int s = 0; s < 64; ++s) {
    const u16* m = s == 0 ? mem0 : mem + s * 4096;
    gemm_k<<<dim3(64, 1), 64, 0, st>>>(m, 512, Wq, 4096, qm, 4096, 8, 4096, 512, nullptr, 0);
    gemm_k<<<dim3(64, 1), 64, 0, st>>>(m, 512, Wk, 4096, km, 4096, 8, 4096, 512, nullptr, 0);
    gemm_k<<<dim3(64, 1), 64, 0, st>>>(m, 512, Wv, 4096, vm, 4096, 8, 4096, 512, nullptr, 0);
    attn_k<<<dim3(32, 8), 128, 0, st>>>(qm, km, vm, K + s * 32 * 4096, V + s * 32 * 4096, om, 0);
    attn_k<<<dim3(32, 32), 128, 0, st>>>(Q + s * 32 * 4096, km, vm, K + s * 32 * 4096, V + s * 32 * 4096, O + s * 32 * 4096, 8);
    gemm_k<<<dim3(8, 1), 64, 0, st>>>(om, 4096, Wo, 512, mem + (s + 1) * 4096, 512, 8, 512, 4096, nullptr, 0);
  }
  gemm_k<<<dim3(8, 256), 64, 0, st>>>(O, 4096, Wo, 512, S, 512, 2048, 512, 4096, nullptr, 0);
  fin_k<<<4096, 256, 0, st>>>(x, S, G, (u16*)d_out);
}
```

```cpp
#include <hip/hip_runtime.h>
#include <hip/hip_bf16.h>
typedef unsigned short u16;
typedef unsigned u32x4 __attribute__((ext_vector_type(4)));
typedef float f32x2 __attribute__((ext_vector_type(2)));
typedef __bf16 bf16x2 __attribute__((ext_vector_type(2)));
#define LAS __attribute__((address_space(3)))
#define GAS __attribute__((address_space(1)))
#define DEV __device__ __forceinline__
#define RLX_AGENT __ATOMIC_RELAXED, __HIP_MEMORY_SCOPE_AGENT
#define ROLE_ASM \
"RSCAN_CODE_BEGIN:\n" \
"s_cmp_eq_u32 s4, 0\n" \
"s_cbranch_scc1 ROLE_R%=\n" \
"s_cmp_eq_u32 s4, 1\n" \
"s_cbranch_scc1 ROLE_A%=\n" \
"s_mov_b64 s[86:87], s[68:69]\n" \
"ds_read_b32 v1, v0 offset:0\n" \
"ds_read_b32 v2, v0 offset:1024\n" \
"ds_read_b32 v3, v0 offset:2048\n" \
"ds_read_b32 v4, v0 offset:3072\n" \
"ds_read_b32 v5, v0 offset:4096\n" \
"ds_read_b32 v6, v0 offset:5120\n" \
"ds_read_b32 v7, v0 offset:6144\n" \
"ds_read_b32 v8, v0 offset:7168\n" \
"ds_read_b32 v9, v0 offset:8192\n" \
"ds_read_b32 v10, v0 offset:9216\n" \
"ds_read_b32 v11, v0 offset:10240\n" \
"ds_read_b32 v12, v0 offset:11264\n" \
"ds_read_b32 v13, v0 offset:12288\n" \
"ds_read_b32 v14, v0 offset:13312\n" \
"ds_read_b32 v15, v0 offset:14336\n" \
"ds_read_b32 v16, v0 offset:15360\n" \
"ds_read_b32 v17, v0 offset:16384\n" \
"ds_read_b32 v18, v0 offset:17408\n" \
"ds_read_b32 v19, v0 offset:18432\n" \
"ds_read_b32 v0, v0 offset:19456\n" \
"s_waitcnt lgkmcnt(0)\n" \
"s_barrier \n" \
"v_mov_b32 v20, 1\n" \
"v_add_u32 v21, 16384, v1\n" \
"s_lshl_b32 s67, s64, 13\n" \
"v_add_u32 v22, s67, v1\n" \
"s_lshl_b32 s67, s64, 11\n" \
"v_add_u32 v23, s67, v1\n" \
"s_mul_i32 s68, s64, 28672\n" \
"s_add_u32 s68, s68, 49152\n" \
"s_lshl_b32 s69, s65, 17\n" \
"s_lshl_b32 s70, s64, 13\n" \
"s_add_u32 s70, s70, 16384\n" \
"s_mov_b32 s82, 32768\n" \
"s_mul_i32 s71, s65, 352256\n" \
"s_mov_b32 s76, 0\n" \
"s_mov_b32 s78, 0xbfb8aa3b\n" \
"s_add_u32 m0, s70, 0\n" \
"s_nop 0\n" \
"buffer_load_dwordx4 v2, s[40:43], s69 offen offset:0 lds\n" \
"buffer_load_dwordx4 v2, s[40:43], s69 offen offset:1024 lds\n" \
"buffer_load_dwordx4 v2, s[40:43], s69 offen offset:2048 lds\n" \
"buffer_load_dwordx4 v2, s[40:43], s69 offen offset:3072 lds\n" \
"s_add_u32 m0, s70, 4096\n" \
"s_add_u32 s67, s69, 4096\n" \
"buffer_load_dwordx4 v2, s[40:43], s67 offen offset:0 lds\n" \
"buffer_load_dwordx4 v2, s[40:43], s67 offen offset:1024 lds\n" \
"buffer_load_dwordx4 v2, s[40:43], s67 offen offset:2048 lds\n" \
"buffer_load_dwordx4 v2, s[40:43], s67 offen offset:3072 lds\n" \
"s_add_u32 s69, s69, 32768\n" \
"s_add_u32 s70, s70, s82\n" \
"s_sub_u32 s82, 0, s82\n" \
"buffer_load_dwordx4 v[24:27], v8, s[56:59], s96 offen\n" \
"buffer_load_dwordx4 v[28:31], v8, s[56:59], s97 offen\n" \
"buffer_load_dwordx4 v[32:35], v8, s[56:59], s98 offen\n" \
"s_waitcnt vmcnt(37)\n" \
"ds_read_b64_tr_b16 a[0:1], v4 offset:0\n" \
"ds_read_b64_tr_b16 a[2:3], v4 offset:256\n" \
"ds_read_b64_tr_b16 a[64:65], v4 offset:8\n" \
"ds_read_b64_tr_b16 a[66:67], v4 offset:264\n" \
"s_waitcnt vmcnt(35)\n" \
"ds_read_b64_tr_b16 a[4:5], v4 offset:2048\n" \
"ds_read_b64_tr_b16 a[6:7], v4 offset:2304\n" \
"ds_read_b64_tr_b16 a[68:69], v4 offset:2056\n" \
"ds_read_b64_tr_b16 a[70:71], v4 offset:2312\n" \
"s_waitcnt lgkmcnt(4)\n" \
"s_add_u32 m0, s68, 0\n" \
"s_add_u32 s67, s96, 1232896\n" \
"buffer_load_dwordx4 v3, s[44:47], s67 offen lds\n" \
"s_add_u32 m0, s68, 1024\n" \
"s_add_u32 s67, s96, 1276928\n" \
"buffer_load_dwordx4 v3, s[44:47], s67 offen lds\n" \
"s_waitcnt vmcnt(35)\n" \
"ds_read_b64_tr_b16 a[8:9], v4 offset:4096\n" \
"ds_read_b64_tr_b16 a[10:11], v4 offset:4352\n" \
"ds_read_b64_tr_b16 a[72:73], v4 offset:4104\n" \
"ds_read_b64_tr_b16 a[74:75], v4 offset:4360\n" \
"s_waitcnt lgkmcnt(4)\n" \
"s_add_u32 m0, s68, 2048\n" \
"s_add_u32 s67, s96, 1320960\n" \
"buffer_load_dwordx4 v3, s[44:47], s67 offen lds\n" \
"s_add_u32 m0, s68, 3072\n" \
"s_add_u32 s67, s96, 1364992\n" \
"buffer_load_dwordx4 v3, s[44:47], s67 offen lds\n" \
"s_waitcnt vmcnt(35)\n" \
"ds_read_b64_tr_b16 a[12:13], v4 offset:6144\n" \
"ds_read_b64_tr_b16 a[14:15], v4 offset:6400\n" \
"ds_read_b64_tr_b16 a[76:77], v4 offset:6152\n" \
"ds_read_b64_tr_b16 a[78:79], v4 offset:6408\n" \
"s_waitcnt lgkmcnt(4)\n" \
"s_add_u32 m0, s68, 4096\n" \
"s_add_u32 s67, s97, 0\n" \
"buffer_load_dwordx4 v3, s[44:47], s67 offen lds\n" \
"s_add_u32 m0, s68, 5120\n" \
"s_add_u32 s67, s97, 44032\n" \
"buffer_load_dwordx4 v3, s[44:47], s67 offen lds\n" \
"s_waitcnt vmcnt(35)\n" \
"ds_read_b64_tr_b16 a[16:17], v4 offset:8192\n" \
"ds_read_b64_tr_b16 a[18:19], v4 offset:8448\n" \
"ds_read_b64_tr_b16 a[80:81], v4 offset:8200\n" \
"ds_read_b64_tr_b16 a[82:83], v4 offset:8456\n" \
"s_waitcnt lgkmcnt(4)\n" \
"s_add_u32 m0, s68, 6144\n" \
"s_add_u32 s67, s97, 88064\n" \
"buffer_load_dwordx4 v3, s[44:47], s67 offen lds\n" \
"s_add_u32 m0, s68, 7168\n" \
"s_add_u32 s67, s97, 132096\n" \
"buffer_load_dwordx4 v3, s[44:47], s67 offen lds\n" \
"s_waitcnt vmcnt(35)\n" \
"ds_read_b64_tr_b16 a[20:21], v4 offset:10240\n" \
"ds_read_b64_tr_b16 a[22:23], v4 offset:10496\n" \
"ds_read_b64_tr_b16 a[84:85], v4 offset:10248\n" \
"ds_read_b64_tr_b16 a[86:87], v4 offset:10504\n" \
"s_waitcnt lgkmcnt(4)\n" \
"s_add_u32 m0, s68, 8192\n" \
"s_add_u32 s67, s97, 176128\n" \
"buffer_load_dwordx4 v3, s[44:47], s67 offen lds\n" \
"s_add_u32 m0, s68, 9216\n" \
"s_add_u32 s67, s97, 220160\n" \
"buffer_load_dwordx4 v3, s[44:47], s67 offen lds\n" \
"s_waitcnt vmcnt(35)\n" \
"ds_read_b64_tr_b16 a[24:25], v4 offset:12288\n" \
"ds_read_b64_tr_b16 a[26:27], v4 offset:12544\n" \
"ds_read_b64_tr_b16 a[88:89], v4 offset:12296\n" \
"ds_read_b64_tr_b16 a[90:91], v4 offset:12552\n" \
"s_waitcnt lgkmcnt(4)\n" \
"s_add_u32 m0, s68, 10240\n" \
"s_add_u32 s67, s97, 264192\n" \
"buffer_load_dwordx4 v3, s[44:47], s67 offen lds\n" \
"s_add_u32 m0, s68, 11264\n" \
"s_add_u32 s67, s97, 308224\n" \
"buffer_load_dwordx4 v3, s[44:47], s67 offen lds\n" \
"s_waitcnt vmcnt(35)\n" \
"ds_read_b64_tr_b16 a[28:29], v4 offset:14336\n" \
"ds_read_b64_tr_b16 a[30:31], v4 offset:14592\n" \
"ds_read_b64_tr_b16 a[92:93], v4 offset:14344\n" \
"ds_read_b64_tr_b16 a[94:95], v4 offset:14600\n" \
"s_waitcnt lgkmcnt(4)\n" \
"s_add_u32 m0, s68, 12288\n" \
"s_add_u32 s67, s97, 352256\n" \
"buffer_load_dwordx4 v3, s[44:47], s67 offen lds\n" \
"s_add_u32 m0, s68, 13312\n" \
"s_add_u32 s67, s97, 396288\n" \
"buffer_load_dwordx4 v3, s[44:47], s67 offen lds\n" \
"s_waitcnt vmcnt(35)\n" \
"ds_read_b64_tr_b16 a[32:33], v4 offset:16384\n" \
"ds_read_b64_tr_b16 a[34:35], v4 offset:16640\n" \
"ds_read_b64_tr_b16 a[96:97], v4 offset:16392\n" \
"ds_read_b64_tr_b16 a[98:99], v4 offset:16648\n" \
"s_waitcnt lgkmcnt(4)\n" \
"s_add_u32 m0, s68, 14336\n" \
"s_add_u32 s67, s97, 440320\n" \
"buffer_load_dwordx4 v3, s[44:47], s67 offen lds\n" \
"s_add_u32 m0, s68, 15360\n" \
"s_add_u32 s67, s97, 484352\n" \
"buffer_load_dwordx4 v3, s[44:47], s67 offen lds\n" \
"s_waitcnt vmcnt(35)\n" \
"ds_read_b64_tr_b16 a[36:37], v4 offset:18432\n" \
"ds_read_b64_tr_b16 a[38:39], v4 offset:18688\n" \
"ds_read_b64_tr_b16 a[100:101], v4 offset:18440\n" \
"ds_read_b64_tr_b16 a[102:103], v4 offset:18696\n" \
"s_waitcnt lgkmcnt(4)\n" \
"s_add_u32 m0, s68, 16384\n" \
"s_add_u32 s67, s97, 528384\n" \
"buffer_load_dwordx4 v3, s[44:47], s67 offen lds\n" \
"s_add_u32 m0, s68, 17408\n" \
"s_add_u32 s67, s97, 572416\n" \
"buffer_load_dwordx4 v3, s[44:47], s67 offen lds\n" \
"s_waitcnt vmcnt(35)\n" \
"ds_read_b64_tr_b16 a[40:41], v4 offset:20480\n" \
"ds_read_b64_tr_b16 a[42:43], v4 offset:20736\n" \
"ds_read_b64_tr_b16 a[104:105], v4 offset:20488\n" \
"ds_read_b64_tr_b16 a[106:107], v4 offset:20744\n" \
"s_waitcnt lgkmcnt(4)\n" \
"s_add_u32 m0, s68, 18432\n" \
"s_add_u32 s67, s97, 616448\n" \
"buffer_load_dwordx4 v3, s[44:47], s67 offen lds\n" \
"s_add_u32 m0, s68, 19456\n" \
"s_add_u32 s67, s97, 660480\n" \
"buffer_load_dwordx4 v3, s[44:47], s67 offen lds\n" \
"s_waitcnt vmcnt(35)\n" \
"ds_read_b64_tr_b16 a[44:45], v4 offset:22528\n" \
"ds_read_b64_tr_b16 a[46:47], v4 offset:22784\n" \
"ds_read_b64_tr_b16 a[108:109], v4 offset:22536\n" \
"ds_read_b64_tr_b16 a[110:111], v4 offset:22792\n" \
"s_waitcnt lgkmcnt(4)\n" \
"s_add_u32 m0, s68, 20480\n" \
"s_add_u32 s67, s97, 704512\n" \
"buffer_load_dwordx4 v3, s[44:47], s67 offen lds\n" \
"s_add_u32 m0, s68, 21504\n" \
"s_add_u32 s67, s97, 748544\n" \
"buffer_load_dwordx4 v3, s[44:47], s67 offen lds\n" \
"s_waitcnt vmcnt(35)\n" \
"ds_read_b64_tr_b16 a[48:49], v4 offset:24576\n" \
"ds_read_b64_tr_b16 a[50:51], v4 offset:24832\n" \
"ds_read_b64_tr_b16 a[112:113], v4 offset:24584\n" \
"ds_read_b64_tr_b16 a[114:115], v4 offset:24840\n" \
"s_waitcnt lgkmcnt(4)\n" \
"s_add_u32 m0, s68, 22528\n" \
"s_add_u32 s67, s97, 792576\n" \
"buffer_load_dwordx4 v3, s[44:47], s67 offen lds\n" \
"s_add_u32 m0, s68, 23552\n" \
"s_add_u32 s67, s97, 836608\n" \
"buffer_load_dwordx4 v3, s[44:47], s67 offen lds\n" \
"s_waitcnt vmcnt(35)\n" \
"ds_read_b64_tr_b16 a[52:53], v4 offset:26624\n" \
"ds_read_b64_tr_b16 a[54:55], v4 offset:26880\n" \
"ds_read_b64_tr_b16 a[116:117], v4 offset:26632\n" \
"ds_read_b64_tr_b16 a[118:119], v4 offset:26888\n" \
"s_waitcnt lgkmcnt(4)\n" \
"s_add_u32 m0, s68, 24576\n" \
"s_add_u32 s67, s97, 880640\n" \
"buffer_load_dwordx4 v3, s[44:47], s67 offen lds\n" \
"s_add_u32 m0, s68, 25600\n" \
"s_add_u32 s67, s97, 924672\n" \
"buffer_load_dwordx4 v3, s[44:47], s67 offen lds\n" \
"s_waitcnt vmcnt(24)\n" \
"ds_read_b64_tr_b16 a[56:57], v4 offset:0\n" \
"ds_read_b64_tr_b16 a[58:59], v4 offset:256\n" \
"ds_read_b64_tr_b16 a[120:121], v4 offset:8\n" \
"ds_read_b64_tr_b16 a[122:123], v4 offset:264\n" \
"s_waitcnt lgkmcnt(4)\n" \
"s_add_u32 m0, s68, 26624\n" \
"s_add_u32 s67, s97, 968704\n" \
"buffer_load_dwordx4 v3, s[44:47], s67 offen lds\n" \
"s_add_u32 m0, s68, 27648\n" \
"s_add_u32 s67, s97, 1012736\n" \
"buffer_load_dwordx4 v3, s[44:47], s67 offen lds\n" \
"s_waitcnt vmcnt(24)\n" \
"ds_read_b64_tr_b16 a[60:61], v4 offset:2048\n" \
"ds_read_b64_tr_b16 a[62:63], v4 offset:2304\n" \
"ds_read_b64_tr_b16 a[124:125], v4 offset:2056\n" \
"ds_read_b64_tr_b16 a[126:127], v4 offset:2312\n" \
"s_waitcnt lgkmcnt(4)\n" \
"s_add_u32 m0, s68, 0\n" \
"s_add_u32 s67, s97, 1056768\n" \
"buffer_load_dwordx4 v3, s[44:47], s67 offen lds\n" \
"s_add_u32 m0, s68, 1024\n" \
"s_add_u32 s67, s97, 1100800\n" \
"buffer_load_dwordx4 v3, s[44:47], s67 offen lds\n" \
"s_waitcnt vmcnt(24)\n" \
"ds_read_b64_tr_b16 a[128:129], v4 offset:4096\n" \
"ds_read_b64_tr_b16 a[130:131], v4 offset:4352\n" \
"ds_read_b64_tr_b16 a[192:193], v4 offset:4104\n" \
"ds_read_b64_tr_b16 a[194:195], v4 offset:4360\n" \
"s_waitcnt lgkmcnt(4)\n" \
"s_add_u32 m0, s68, 2048\n" \
"s_add_u32 s67, s97, 1144832\n" \
"buffer_load_dwordx4 v3, s[44:47], s67 offen lds\n" \
"s_add_u32 m0, s68, 3072\n" \
"s_add_u32 s67, s97, 1188864\n" \
"buffer_load_dwordx4 v3, s[44:47], s67 offen lds\n" \
"s_waitcnt vmcnt(24)\n" \
"ds_read_b64_tr_b16 a[132:133], v4 offset:6144\n" \
"ds_read_b64_tr_b16 a[134:135], v4 offset:6400\n" \
"ds_read_b64_tr_b16 a[196:197], v4 offset:6152\n" \
"ds_read_b64_tr_b16 a[198:199], v4 offset:6408\n" \
"s_waitcnt lgkmcnt(4)\n" \
"s_add_u32 m0, s68, 4096\n" \
"s_add_u32 s67, s97, 1232896\n" \
"buffer_load_dwordx4 v3, s[44:47], s67 offen lds\n" \
"s_add_u32 m0, s68, 5120\n" \
"s_add_u32 s67, s97, 1276928\n" \
"buffer_load_dwordx4 v3, s[44:47], s67 offen lds\n" \
"s_waitcnt vmcnt(24)\n" \
"ds_read_b64_tr_b16 a[136:137], v4 offset:8192\n" \
"ds_read_b64_tr_b16 a[138:139], v4 offset:8448\n" \
"ds_read_b64_tr_b16 a[200:201], v4 offset:8200\n" \
"ds_read_b64_tr_b16 a[202:203], v4 offset:8456\n" \
"s_waitcnt lgkmcnt(4)\n" \
"s_add_u32 m0, s68, 6144\n" \
"s_add_u32 s67, s97, 1320960\n" \
"buffer_load_dwordx4 v3, s[44:47], s67 offen lds\n" \
"s_add_u32 m0, s68, 7168\n" \
"s_add_u32 s67, s97, 1364992\n" \
"buffer_load_dwordx4 v3, s[44:47], s67 offen lds\n" \
"s_waitcnt vmcnt(24)\n" \
"ds_read_b64_tr_b16 a[140:141], v4 offset:10240\n" \
"ds_read_b64_tr_b16 a[142:143], v4 offset:10496\n" \
"ds_read_b64_tr_b16 a[204:205], v4 offset:10248\n" \
"ds_read_b64_tr_b16 a[206:207], v4 offset:10504\n" \
"s_waitcnt lgkmcnt(4)\n" \
"s_add_u32 m0, s68, 8192\n" \
"s_add_u32 s67, s98, 0\n" \
"buffer_load_dwordx4 v3, s[44:47], s67 offen lds\n" \
"s_add_u32 m0, s68, 9216\n" \
"s_add_u32 s67, s98, 44032\n" \
"buffer_load_dwordx4 v3, s[44:47], s67 offen lds\n" \
"s_waitcnt vmcnt(24)\n" \
"ds_read_b64_tr_b16 a[144:145], v4 offset:12288\n" \
"ds_read_b64_tr_b16 a[146:147], v4 offset:12544\n" \
"ds_read_b64_tr_b16 a[208:209], v4 offset:12296\n" \
"ds_read_b64_tr_b16 a[210:211], v4 offset:12552\n" \
"s_waitcnt lgkmcnt(4)\n" \
"s_add_u32 m0, s68, 10240\n" \
"s_add_u32 s67, s98, 88064\n" \
"buffer_load_dwordx4 v3, s[44:47], s67 offen lds\n" \
"s_add_u32 m0, s68, 11264\n" \
"s_add_u32 s67, s98, 132096\n" \
"buffer_load_dwordx4 v3, s[44:47], s67 offen lds\n" \
"s_waitcnt vmcnt(24)\n" \
"ds_read_b64_tr_b16 a[148:149], v4 offset:14336\n" \
"ds_read_b64_tr_b16 a[150:151], v4 offset:14592\n" \
"ds_read_b64_tr_b16 a[212:213], v4 offset:14344\n" \
"ds_read_b64_tr_b16 a[214:215], v4 offset:14600\n" \
"s_waitcnt lgkmcnt(4)\n" \
"s_add_u32 m0, s68, 12288\n" \
"s_add_u32 s67, s98, 176128\n" \
"buffer_load_dwordx4 v3, s[44:47], s67 offen lds\n" \
"s_add_u32 m0, s68, 13312\n" \
"s_add_u32 s67, s98, 220160\n" \
"buffer_load_dwordx4 v3, s[44:47], s67 offen lds\n" \
"s_waitcnt vmcnt(24)\n" \
"ds_read_b64_tr_b16 a[152:153], v4 offset:16384\n" \
"ds_read_b64_tr_b16 a[154:155], v4 offset:16640\n" \
"ds_read_b64_tr_b16 a[216:217], v4 offset:16392\n" \
"ds_read_b64_tr_b16 a[218:219], v4 offset:16648\n" \
"s_waitcnt lgkmcnt(4)\n" \
"s_add_u32 m0, s68, 14336\n" \
"s_add_u32 s67, s98, 264192\n" \
"buffer_load_dwordx4 v3, s[44:47], s67 offen lds\n" \
"s_add_u32 m0, s68, 15360\n" \
"s_add_u32 s67, s98, 308224\n" \
"buffer_load_dwordx4 v3, s[44:47], s67 offen lds\n" \
"s_waitcnt vmcnt(24)\n" \
"ds_read_b64_tr_b16 a[156:157], v4 offset:18432\n" \
"ds_read_b64_tr_b16 a[158:159], v4 offset:18688\n" \
"ds_read_b64_tr_b16 a[220:221], v4 offset:18440\n" \
"ds_read_b64_tr_b16 a[222:223], v4 offset:18696\n" \
"s_waitcnt lgkmcnt(4)\n" \
"s_add_u32 m0, s68, 16384\n" \
"s_add_u32 s67, s98, 352256\n" \
"buffer_load_dwordx4 v3, s[44:47], s67 offen lds\n" \
"s_add_u32 m0, s68, 17408\n" \
"s_add_u32 s67, s98, 396288\n" \
"buffer_load_dwordx4 v3, s[44:47], s67 offen lds\n" \
"s_waitcnt vmcnt(24)\n" \
"ds_read_b64_tr_b16 a[160:161], v4 offset:20480\n" \
"ds_read_b64_tr_b16 a[162:163], v4 offset:20736\n" \
"ds_read_b64_tr_b16 a[224:225], v4 offset:20488\n" \
"ds_read_b64_tr_b16 a[226:227], v4 offset:20744\n" \
"s_waitcnt lgkmcnt(4)\n" \
"s_add_u32 m0, s68, 18432\n" \
"s_add_u32 s67, s98, 440320\n" \
"buffer_load_dwordx4 v3, s[44:47], s67 offen lds\n" \
"s_add_u32 m0, s68, 19456\n" \
"s_add_u32 s67, s98, 484352\n" \
"buffer_load_dwordx4 v3, s[44:47], s67 offen lds\n" \
"s_waitcnt vmcnt(24)\n" \
"ds_read_b64_tr_b16 a[164:165], v4 offset:22528\n" \
"ds_read_b64_tr_b16 a[166:167], v4 offset:22784\n" \
"ds_read_b64_tr_b16 a[228:229], v4 offset:22536\n" \
"ds_read_b64_tr_b16 a[230:231], v4 offset:22792\n" \
"s_waitcnt lgkmcnt(4)\n" \
"s_add_u32 m0, s68, 20480\n" \
"s_add_u32 s67, s98, 528384\n" \
"buffer_load_dwordx4 v3, s[44:47], s67 offen lds\n" \
"s_add_u32 m0, s68, 21504\n" \
"s_add_u32 s67, s98, 572416\n" \
"buffer_load_dwordx4 v3, s[44:47], s67 offen lds\n" \
"s_waitcnt vmcnt(24)\n" \
"ds_read_b64_tr_b16 a[168:169], v4 offset:24576\n" \
"ds_read_b64_tr_b16 a[170:171], v4 offset:24832\n" \
"ds_read_b64_tr_b16 a[232:233], v4 offset:24584\n" \
"ds_read_b64_tr_b16 a[234:235], v4 offset:24840\n" \
"s_waitcnt lgkmcnt(4)\n" \
"s_add_u32 m0, s68, 22528\n" \
"s_add_u32 s67, s98, 616448\n" \
"buffer_load_dwordx4 v3, s[44:47], s67 offen lds\n" \
"s_add_u32 m0, s68, 23552\n" \
"s_add_u32 s67, s98, 660480\n" \
"buffer_load_dwordx4 v3, s[44:47], s67 offen lds\n" \
"s_waitcnt vmcnt(24)\n" \
"ds_read_b64_tr_b16 a[172:173], v4 offset:26624\n" \
"ds_read_b64_tr_b16 a[174:175], v4 offset:26880\n" \
"ds_read_b64_tr_b16 a[236:237], v4 offset:26632\n" \
"ds_read_b64_tr_b16 a[238:239], v4 offset:26888\n" \
"s_waitcnt lgkmcnt(4)\n" \
"s_add_u32 m0, s68, 24576\n" \
"s_add_u32 s67, s98, 704512\n" \
"buffer_load_dwordx4 v3, s[44:47], s67 offen lds\n" \
"s_add_u32 m0, s68, 25600\n" \
"s_add_u32 s67, s98, 748544\n" \
"buffer_load_dwordx4 v3, s[44:47], s67 offen lds\n" \
"s_waitcnt vmcnt(24)\n" \
"ds_read_b64_tr_b16 a[176:177], v4 offset:0\n" \
"ds_read_b64_tr_b16 a[178:179], v4 offset:256\n" \
"ds_read_b64_tr_b16 a[240:241], v4 offset:8\n" \
"ds_read_b64_tr_b16 a[242:243], v4 offset:264\n" \
"s_waitcnt lgkmcnt(4)\n" \
"s_add_u32 m0, s68, 26624\n" \
"s_add_u32 s67, s98, 792576\n" \
"buffer_load_dwordx4 v3, s[44:47], s67 offen lds\n" \
"s_add_u32 m0, s68, 27648\n" \
"s_add_u32 s67, s98, 836608\n" \
"buffer_load_dwordx4 v3, s[44:47], s67 offen lds\n" \
"s_waitcnt vmcnt(24)\n" \
"ds_read_b64_tr_b16 a[180:181], v4 offset:2048\n" \
"ds_read_b64_tr_b16 a[182:183], v4 offset:2304\n" \
"ds_read_b64_tr_b16 a[244:245], v4 offset:2056\n" \
"ds_read_b64_tr_b16 a[246:247], v4 offset:2312\n" \
"s_waitcnt lgkmcnt(4)\n" \
"s_add_u32 m0, s68, 0\n" \
"s_add_u32 s67, s98, 880640\n" \
"buffer_load_dwordx4 v3, s[44:47], s67 offen lds\n" \
"s_add_u32 m0, s68, 1024\n" \
"s_add_u32 s67, s98, 924672\n" \
"buffer_load_dwordx4 v3, s[44:47], s67 offen lds\n" \
"s_waitcnt vmcnt(24)\n" \
"ds_read_b64_tr_b16 a[184:185], v4 offset:4096\n" \
"ds_read_b64_tr_b16 a[186:187], v4 offset:4352\n" \
"ds_read_b64_tr_b16 a[248:249], v4 offset:4104\n" \
"ds_read_b64_tr_b16 a[250:251], v4 offset:4360\n" \
"s_waitcnt lgkmcnt(4)\n" \
"s_add_u32 m0, s68, 2048\n" \
"s_add_u32 s67, s98, 968704\n" \
"buffer_load_dwordx4 v3, s[44:47], s67 offen lds\n" \
"s_add_u32 m0, s68, 3072\n" \
"s_add_u32 s67, s98, 1012736\n" \
"buffer_load_dwordx4 v3, s[44:47], s67 offen lds\n" \
"s_waitcnt vmcnt(24)\n" \
"ds_read_b64_tr_b16 a[188:189], v4 offset:6144\n" \
"ds_read_b64_tr_b16 a[190:191], v4 offset:6400\n" \
"ds_read_b64_tr_b16 a[252:253], v4 offset:6152\n" \
"ds_read_b64_tr_b16 a[254:255], v4 offset:6408\n" \
"s_waitcnt lgkmcnt(4)\n" \
"s_add_u32 m0, s68, 4096\n" \
"s_add_u32 s67, s98, 1056768\n" \
"buffer_load_dwordx4 v3, s[44:47], s67 offen lds\n" \
"s_add_u32 m0, s68, 5120\n" \
"s_add_u32 s67, s98, 1100800\n" \
"buffer_load_dwordx4 v3, s[44:47], s67 offen lds\n" \
"s_waitcnt vmcnt(24)\n" \
"ds_read_b64_tr_b16 v[128:129], v4 offset:8192\n" \
"ds_read_b64_tr_b16 v[130:131], v4 offset:8448\n" \
"ds_read_b64_tr_b16 v[192:193], v4 offset:8200\n" \
"ds_read_b64_tr_b16 v[194:195], v4 offset:8456\n" \
"s_waitcnt lgkmcnt(4)\n" \
"s_add_u32 m0, s68, 6144\n" \
"s_add_u32 s67, s98, 1144832\n" \
"buffer_load_dwordx4 v3, s[44:47], s67 offen lds\n" \
"s_add_u32 m0, s68, 7168\n" \
"s_add_u32 s67, s98, 1188864\n" \
"buffer_load_dwordx4 v3, s[44:47], s67 offen lds\n" \
"s_waitcnt vmcnt(24)\n" \
"ds_read_b64_tr_b16 v[132:133], v4 offset:10240\n" \
"ds_read_b64_tr_b16 v[134:135], v4 offset:10496\n" \
"ds_read_b64_tr_b16 v[196:197], v4 offset:10248\n" \
"ds_read_b64_tr_b16 v[198:199], v4 offset:10504\n" \
"s_waitcnt lgkmcnt(4)\n" \
"s_add_u32 m0, s68, 8192\n" \
"s_add_u32 s67, s98, 1232896\n" \
"buffer_load_dwordx4 v3, s[44:47], s67 offen lds\n" \
"s_add_u32 m0, s68, 9216\n" \
"s_add_u32 s67, s98, 1276928\n" \
"buffer_load_dwordx4 v3, s[44:47], s67 offen lds\n" \
"s_waitcnt vmcnt(24)\n" \
"ds_read_b64_tr_b16 v[136:137], v4 offset:12288\n" \
"ds_read_b64_tr_b16 v[138:139], v4 offset:12544\n" \
"ds_read_b64_tr_b16 v[200:201], v4 offset:12296\n" \
"ds_read_b64_tr_b16 v[202:203], v4 offset:12552\n" \
"s_waitcnt lgkmcnt(4)\n" \
"s_add_u32 m0, s68, 10240\n" \
"s_add_u32 s67, s98, 1320960\n" \
"buffer_load_dwordx4 v3, s[44:47], s67 offen lds\n" \
"s_add_u32 m0, s68, 11264\n" \
"s_add_u32 s67, s98, 1364992\n" \
"buffer_load_dwordx4 v3, s[44:47], s67 offen lds\n" \
"s_waitcnt vmcnt(24)\n" \
"ds_read_b64_tr_b16 v[140:141], v4 offset:14336\n" \
"ds_read_b64_tr_b16 v[142:143], v4 offset:14592\n" \
"ds_read_b64_tr_b16 v[204:205], v4 offset:14344\n" \
"ds_read_b64_tr_b16 v[206:207], v4 offset:14600\n" \
"s_waitcnt vmcnt(22)\n" \
"ds_read_b64_tr_b16 v[144:145], v4 offset:16384\n" \
"ds_read_b64_tr_b16 v[146:147], v4 offset:16640\n" \
"ds_read_b64_tr_b16 v[208:209], v4 offset:16392\n" \
"ds_read_b64_tr_b16 v[210:211], v4 offset:16648\n" \
"s_waitcnt vmcnt(20)\n" \
"ds_read_b64_tr_b16 v[148:149], v4 offset:18432\n" \
"ds_read_b64_tr_b16 v[150:151], v4 offset:18688\n" \
"ds_read_b64_tr_b16 v[212:213], v4 offset:18440\n" \
"ds_read_b64_tr_b16 v[214:215], v4 offset:18696\n" \
"s_waitcnt vmcnt(18)\n" \
"ds_read_b64_tr_b16 v[152:153], v4 offset:20480\n" \
"ds_read_b64_tr_b16 v[154:155], v4 offset:20736\n" \
"ds_read_b64_tr_b16 v[216:217], v4 offset:20488\n" \
"ds_read_b64_tr_b16 v[218:219], v4 offset:20744\n" \
"s_waitcnt vmcnt(16)\n" \
"ds_read_b64_tr_b16 v[156:157], v4 offset:22528\n" \
"ds_read_b64_tr_b16 v[158:159], v4 offset:22784\n" \
"ds_read_b64_tr_b16 v[220:221], v4 offset:22536\n" \
"ds_read_b64_tr_b16 v[222:223], v4 offset:22792\n" \
"s_waitcnt vmcnt(14)\n" \
"ds_read_b64_tr_b16 v[160:161], v4 offset:24576\n" \
"ds_read_b64_tr_b16 v[162:163], v4 offset:24832\n" \
"ds_read_b64_tr_b16 v[224:225], v4 offset:24584\n" \
"ds_read_b64_tr_b16 v[226:227], v4 offset:24840\n" \
"s_waitcnt vmcnt(12)\n" \
"ds_read_b64_tr_b16 v[164:165], v4 offset:26624\n" \
"ds_read_b64_tr_b16 v[166:167], v4 offset:26880\n" \
"ds_read_b64_tr_b16 v[228:229], v4 offset:26632\n" \
"ds_read_b64_tr_b16 v[230:231], v4 offset:26888\n" \
"s_waitcnt vmcnt(10)\n" \
"ds_read_b64_tr_b16 v[168:169], v4 offset:0\n" \
"ds_read_b64_tr_b16 v[170:171], v4 offset:256\n" \
"ds_read_b64_tr_b16 v[232:233], v4 offset:8\n" \
"ds_read_b64_tr_b16 v[234:235], v4 offset:264\n" \
"s_waitcnt vmcnt(8)\n" \
"ds_read_b64_tr_b16 v[172:173], v4 offset:2048\n" \
"ds_read_b64_tr_b16 v[174:175], v4 offset:2304\n" \
"ds_read_b64_tr_b16 v[236:237], v4 offset:2056\n" \
"ds_read_b64_tr_b16 v[238:239], v4 offset:2312\n" \
"s_waitcnt vmcnt(6)\n" \
"ds_read_b64_tr_b16 v[176:177], v4 offset:4096\n" \
"ds_read_b64_tr_b16 v[178:179], v4 offset:4352\n" \
"ds_read_b64_tr_b16 v[240:241], v4 offset:4104\n" \
"ds_read_b64_tr_b16 v[242:243], v4 offset:4360\n" \
"s_waitcnt vmcnt(4)\n" \
"ds_read_b64_tr_b16 v[180:181], v4 offset:6144\n" \
"ds_read_b64_tr_b16 v[182:183], v4 offset:6400\n" \
"ds_read_b64_tr_b16 v[244:245], v4 offset:6152\n" \
"ds_read_b64_tr_b16 v[246:247], v4 offset:6408\n" \
"s_waitcnt vmcnt(2)\n" \
"ds_read_b64_tr_b16 v[184:185], v4 offset:8192\n" \
"ds_read_b64_tr_b16 v[186:187], v4 offset:8448\n" \
"ds_read_b64_tr_b16 v[248:249], v4 offset:8200\n" \
"ds_read_b64_tr_b16 v[250:251], v4 offset:8456\n" \
"s_waitcnt vmcnt(0)\n" \
"ds_read_b64_tr_b16 v[188:189], v4 offset:10240\n" \
"ds_read_b64_tr_b16 v[190:191], v4 offset:10496\n" \
"ds_read_b64_tr_b16 v[252:253], v4 offset:10248\n" \
"ds_read_b64_tr_b16 v[254:255], v4 offset:10504\n" \
"s_waitcnt lgkmcnt(0)\n" \
"s_waitcnt vmcnt(0)\n" \
"v_lshlrev_b32 v96, 16, v24\n" \
"v_and_b32 v97, 0xffff0000, v24\n" \
"v_lshlrev_b32 v98, 16, v25\n" \
"v_and_b32 v99, 0xffff0000, v25\n" \
"v_lshlrev_b32 v100, 16, v26\n" \
"v_and_b32 v101, 0xffff0000, v26\n" \
"v_lshlrev_b32 v102, 16, v27\n" \
"v_and_b32 v103, 0xffff0000, v27\n" \
"v_lshlrev_b32 v104, 16, v28\n" \
"v_and_b32 v105, 0xffff0000, v28\n" \
"v_lshlrev_b32 v106, 16, v29\n" \
"v_and_b32 v107, 0xffff0000, v29\n" \
"v_lshlrev_b32 v108, 16, v30\n" \
"v_and_b32 v109, 0xffff0000, v30\n" \
"v_lshlrev_b32 v110, 16, v31\n" \
"v_and_b32 v111, 0xffff0000, v31\n" \
"v_lshlrev_b32 v112, 16, v32\n" \
"v_and_b32 v113, 0xffff0000, v32\n" \
"v_lshlrev_b32 v114, 16, v33\n" \
"v_and_b32 v115, 0xffff0000, v33\n" \
"v_lshlrev_b32 v116, 16, v34\n" \
"v_and_b32 v117, 0xffff0000, v34\n" \
"v_lshlrev_b32 v118, 16, v35\n" \
"v_and_b32 v119, 0xffff0000, v35\n" \
"s_mov_b32 s66, 0\n" \
"s_nop 0\n" \
"s_barrier \n" \
"W_f1%=:\n" \
"ds_read_b128 v[72:75], v21 offset:0\n" \
"ds_read_b128 v[76:79], v21 offset:16384\n" \
"ds_read_b128 v[80:83], v21 offset:1024\n" \
"ds_read_b128 v[84:87], v21 offset:17408\n" \
"s_waitcnt lgkmcnt(3)\n" \
"v_mfma_f32_16x16x32_bf16 v[24:27], a[0:3], v[72:75], 0\n" \
"ds_read_b128 v[88:91], v21 offset:2048\n" \
"ds_read_b128 v[92:95], v21 offset:18432\n" \
"v_mfma_f32_16x16x32_bf16 v[28:31], a[64:67], v[72:75], 0\n" \
"v_mfma_f32_16x16x32_bf16 v[40:43], a[128:131], v[72:75], 0\n" \
"v_mfma_f32_16x16x32_bf16 v[44:47], a[192:195], v[72:75], 0\n" \
"v_mfma_f32_16x16x32_bf16 v[56:59], v[128:131], v[72:75], 0\n" \
"v_mfma_f32_16x16x32_bf16 v[60:63], v[192:195], v[72:75], 0\n" \
"s_add_u32 m0, s70, 0\n" \
"s_waitcnt lgkmcnt(4)\n" \
"v_mfma_f32_16x16x32_bf16 v[32:35], a[0:3], v[76:79], 0\n" \
"buffer_load_dwordx4 v2, s[40:43], s69 offen offset:0 lds\n" \
"v_mfma_f32_16x16x32_bf16 v[36:39], a[64:67], v[76:79], 0\n" \
"buffer_load_dwordx4 v2, s[40:43], s69 offen offset:1024 lds\n" \
"v_mfma_f32_16x16x32_bf16 v[48:51], a[128:131], v[76:79], 0\n" \
"buffer_load_dwordx4 v2, s[40:43], s69 offen offset:2048 lds\n" \
"v_mfma_f32_16x16x32_bf16 v[52:55], a[192:195], v[76:79], 0\n" \
"buffer_load_dwordx4 v2, s[40:43], s69 offen offset:3072 lds\n" \
"v_mfma_f32_16x16x32_bf16 v[64:67], v[128:131], v[76:79], 0\n" \
"s_add_u32 m0, s70, 4096\n" \
"v_mfma_f32_16x16x32_bf16 v[68:71], v[192:195], v[76:79], 0\n" \
"s_add_u32 s67, s69, 4096\n" \
"s_waitcnt lgkmcnt(3)\n" \
"v_mfma_f32_16x16x32_bf16 v[24:27], a[4:7], v[80:83], v[24:27]\n" \
"ds_read_b128 v[72:75], v21 offset:3072\n" \
"ds_read_b128 v[76:79], v21 offset:19456\n" \
"v_mfma_f32_16x16x32_bf16 v[28:31], a[68:71], v[80:83], v[28:31]\n" \
"buffer_load_dwordx4 v2, s[40:43], s67 offen offset:0 lds\n" \
"v_mfma_f32_16x16x32_bf16 v[40:43], a[132:135], v[80:83], v[40:43]\n" \
"buffer_load_dwordx4 v2, s[40:43], s67 offen offset:1024 lds\n" \
"v_mfma_f32_16x16x32_bf16 v[44:47], a[196:199], v[80:83], v[44:47]\n" \
"buffer_load_dwordx4 v2, s[40:43], s67 offen offset:2048 lds\n" \
"v_mfma_f32_16x16x32_bf16 v[56:59], v[132:135], v[80:83], v[56:59]\n" \
"buffer_load_dwordx4 v2, s[40:43], s67 offen offset:3072 lds\n" \
"v_mfma_f32_16x16x32_bf16 v[60:63], v[196:199], v[80:83], v[60:63]\n" \
"s_add_u32 s69, s69, 32768\n" \
"s_waitcnt lgkmcnt(4)\n" \
"v_mfma_f32_16x16x32_bf16 v[32:35], a[4:7], v[84:87], v[32:35]\n" \
"s_add_u32 s70, s70, s82\n" \
"v_mfma_f32_16x16x32_bf16 v[36:39], a[68:71], v[84:87], v[36:39]\n" \
"s_sub_u32 s82, 0, s82\n" \
"v_mfma_f32_16x16x32_bf16 v[48:51], a[132:135], v[84:87], v[48:51]\n" \
"v_mfma_f32_16x16x32_bf16 v[52:55], a[196:199], v[84:87], v[52:55]\n" \
"v_mfma_f32_16x16x32_bf16 v[64:67], v[132:135], v[84:87], v[64:67]\n" \
"v_mfma_f32_16x16x32_bf16 v[68:71], v[196:199], v[84:87], v[68:71]\n" \
"s_waitcnt lgkmcnt(3)\n" \
"v_mfma_f32_16x16x32_bf16 v[24:27], a[8:11], v[88:91], v[24:27]\n" \
"ds_read_b128 v[80:83], v21 offset:4096\n" \
"ds_read_b128 v[84:87], v21 offset:20480\n" \
"v_mfma_f32_16x16x32_bf16 v[28:31], a[72:75], v[88:91], v[28:31]\n" \
"v_mfma_f32_16x16x32_bf16 v[40:43], a[136:139], v[88:91], v[40:43]\n" \
"v_mfma_f32_16x16x32_bf16 v[44:47], a[200:203], v[88:91], v[44:47]\n" \
"v_mfma_f32_16x16x32_bf16 v[56:59], v[136:139], v[88:91], v[56:59]\n" \
"v_mfma_f32_16x16x32_bf16 v[60:63], v[200:203], v[88:91], v[60:63]\n" \
"s_waitcnt lgkmcnt(4)\n" \
"v_mfma_f32_16x16x32_bf16 v[32:35], a[8:11], v[92:95], v[32:35]\n" \
"v_mfma_f32_16x16x32_bf16 v[36:39], a[72:75], v[92:95], v[36:39]\n" \
"v_mfma_f32_16x16x32_bf16 v[48:51], a[136:139], v[92:95], v[48:51]\n" \
"v_mfma_f32_16x16x32_bf16 v[52:55], a[200:203], v[92:95], v[52:55]\n" \
"v_mfma_f32_16x16x32_bf16 v[64:67], v[136:139], v[92:95], v[64:67]\n" \
"v_mfma_f32_16x16x32_bf16 v[68:71], v[200:203], v[92:95], v[68:71]\n" \
"s_waitcnt lgkmcnt(3)\n" \
"v_mfma_f32_16x16x32_bf16 v[24:27], a[12:15], v[72:75], v[24:27]\n" \
"ds_read_b128 v[88:91], v21 offset:5120\n" \
"ds_read_b128 v[92:95], v21 offset:21504\n" \
"v_mfma_f32_16x16x32_bf16 v[28:31], a[76:79], v[72:75], v[28:31]\n" \
"v_mfma_f32_16x16x32_bf16 v[40:43], a[140:143], v[72:75], v[40:43]\n" \
"v_mfma_f32_16x16x32_bf16 v[44:47], a[204:207], v[72:75], v[44:47]\n" \
"v_mfma_f32_16x16x32_bf16 v[56:59], v[140:143], v[72:75], v[56:59]\n" \
"v_mfma_f32_16x16x32_bf16 v[60:63], v[204:207], v[72:75], v[60:63]\n" \
"s_waitcnt lgkmcnt(4)\n" \
"v_mfma_f32_16x16x32_bf16 v[32:35], a[12:15], v[76:79], v[32:35]\n" \
"v_mfma_f32_16x16x32_bf16 v[36:39], a[76:79], v[76:79], v[36:39]\n" \
"v_mfma_f32_16x16x32_bf16 v[48:51], a[140:143], v[76:79], v[48:51]\n" \
"v_mfma_f32_16x16x32_bf16 v[52:55], a[204:207], v[76:79], v[52:55]\n" \
"v_mfma_f32_16x16x32_bf16 v[64:67], v[140:143], v[76:79], v[64:67]\n" \
"v_mfma_f32_16x16x32_bf16 v[68:71], v[204:207], v[76:79], v[68:71]\n" \
"s_waitcnt lgkmcnt(3)\n" \
"v_mfma_f32_16x16x32_bf16 v[24:27], a[16:19], v[80:83], v[24:27]\n" \
"ds_read_b128 v[72:75], v21 offset:6144\n" \
"ds_read_b128 v[76:79], v21 offset:22528\n" \
"v_mfma_f32_16x16x32_bf16 v[28:31], a[80:83], v[80:83], v[28:31]\n" \
"v_mfma_f32_16x16x32_bf16 v[40:43], a[144:147], v[80:83], v[40:43]\n" \
"v_mfma_f32_16x16x32_bf16 v[44:47], a[208:211], v[80:83], v[44:47]\n" \
"v_mfma_f32_16x16x32_bf16 v[56:59], v[144:147], v[80:83], v[56:59]\n" \
"v_mfma_f32_16x16x32_bf16 v[60:63], v[208:211], v[80:83], v[60:63]\n" \
"s_waitcnt lgkmcnt(4)\n" \
"v_mfma_f32_16x16x32_bf16 v[32:35], a[16:19], v[84:87], v[32:35]\n" \
"v_mfma_f32_16x16x32_bf16 v[36:39], a[80:83], v[84:87], v[36:39]\n" \
"v_mfma_f32_16x16x32_bf16 v[48:51], a[144:147], v[84:87], v[48:51]\n" \
"v_mfma_f32_16x16x32_bf16 v[52:55], a[208:211], v[84:87], v[52:55]\n" \
"v_mfma_f32_16x16x32_bf16 v[64:67], v[144:147], v[84:87], v[64:67]\n" \
"v_mfma_f32_16x16x32_bf16 v[68:71], v[208:211], v[84:87], v[68:71]\n" \
"s_waitcnt lgkmcnt(3)\n" \
"v_mfma_f32_16x16x32_bf16 v[24:27], a[20:23], v[88:91], v[24:27]\n" \
"ds_read_b128 v[80:83], v21 offset:7168\n" \
"ds_read_b128 v[84:87], v21 offset:23552\n" \
"v_mfma_f32_16x16x32_bf16 v[28:31], a[84:87], v[88:91], v[28:31]\n" \
"v_mfma_f32_16x16x32_bf16 v[40:43], a[148:151], v[88:91], v[40:43]\n" \
"v_mfma_f32_16x16x32_bf16 v[44:47], a[212:215], v[88:91], v[44:47]\n" \
"v_mfma_f32_16x16x32_bf16 v[56:59], v[148:151], v[88:91], v[56:59]\n" \
"v_mfma_f32_16x16x32_bf16 v[60:63], v[212:215], v[88:91], v[60:63]\n" \
"s_waitcnt lgkmcnt(4)\n" \
"v_mfma_f32_16x16x32_bf16 v[32:35], a[20:23], v[92:95], v[32:35]\n" \
"v_mfma_f32_16x16x32_bf16 v[36:39], a[84:87], v[92:95], v[36:39]\n" \
"v_mfma_f32_16x16x32_bf16 v[48:51], a[148:151], v[92:95], v[48:51]\n" \
"v_mfma_f32_16x16x32_bf16 v[52:55], a[212:215], v[92:95], v[52:55]\n" \
"v_mfma_f32_16x16x32_bf16 v[64:67], v[148:151], v[92:95], v[64:67]\n" \
"v_mfma_f32_16x16x32_bf16 v[68:71], v[212:215], v[92:95], v[68:71]\n" \
"s_waitcnt lgkmcnt(3)\n" \
"v_mfma_f32_16x16x32_bf16 v[24:27], a[24:27], v[72:75], v[24:27]\n" \
"ds_read_b128 v[88:91], v21 offset:8192\n" \
"ds_read_b128 v[92:95], v21 offset:24576\n" \
"v_mfma_f32_16x16x32_bf16 v[28:31], a[88:91], v[72:75], v[28:31]\n" \
"v_mfma_f32_16x16x32_bf16 v[40:43], a[152:155], v[72:75], v[40:43]\n" \
"v_mfma_f32_16x16x32_bf16 v[44:47], a[216:219], v[72:75], v[44:47]\n" \
"v_mfma_f32_16x16x32_bf16 v[56:59], v[152:155], v[72:75], v[56:59]\n" \
"v_mfma_f32_16x16x32_bf16 v[60:63], v[216:219], v[72:75], v[60:63]\n" \
"s_waitcnt lgkmcnt(4)\n" \
"v_mfma_f32_16x16x32_bf16 v[32:35], a[24:27], v[76:79], v[32:35]\n" \
"v_mfma_f32_16x16x32_bf16 v[36:39], a[88:91], v[76:79], v[36:39]\n" \
"v_mfma_f32_16x16x32_bf16 v[48:51], a[152:155], v[76:79], v[48:51]\n" \
"v_mfma_f32_16x16x32_bf16 v[52:55], a[216:219], v[76:79], v[52:55]\n" \
"v_mfma_f32_16x16x32_bf16 v[64:67], v[152:155], v[76:79], v[64:67]\n" \
"v_mfma_f32_16x16x32_bf16 v[68:71], v[216:219], v[76:79], v[68:71]\n" \
"s_waitcnt lgkmcnt(3)\n" \
"v_mfma_f32_16x16x32_bf16 v[24:27], a[28:31], v[80:83], v[24:27]\n" \
"ds_read_b128 v[72:75], v21 offset:9216\n" \
"ds_read_b128 v[76:79], v21 offset:25600\n" \
"v_mfma_f32_16x16x32_bf16 v[28:31], a[92:95], v[80:83], v[28:31]\n" \
"v_mfma_f32_16x16x32_bf16 v[40:43], a[156:159], v[80:83], v[40:43]\n" \
"v_mfma_f32_16x16x32_bf16 v[44:47], a[220:223], v[80:83], v[44:47]\n" \
"v_mfma_f32_16x16x32_bf16 v[56:59], v[156:159], v[80:83], v[56:59]\n" \
"v_mfma_f32_16x16x32_bf16 v[60:63], v[220:223], v[80:83], v[60:63]\n" \
"s_waitcnt lgkmcnt(4)\n" \
"v_mfma_f32_16x16x32_bf16 v[32:35], a[28:31], v[84:87], v[32:35]\n" \
"v_mfma_f32_16x16x32_bf16 v[36:39], a[92:95], v[84:87], v[36:39]\n" \
"v_mfma_f32_16x16x32_bf16 v[48:51], a[156:159], v[84:87], v[48:51]\n" \
"v_mfma_f32_16x16x32_bf16 v[52:55], a[220:223], v[84:87], v[52:55]\n" \
"v_mfma_f32_16x16x32_bf16 v[64:67], v[156:159], v[84:87], v[64:67]\n" \
"v_mfma_f32_16x16x32_bf16 v[68:71], v[220:223], v[84:87], v[68:71]\n" \
"s_waitcnt lgkmcnt(3)\n" \
"v_mfma_f32_16x16x32_bf16 v[24:27], a[32:35], v[88:91], v[24:27]\n" \
"ds_read_b128 v[80:83], v21 offset:10240\n" \
"ds_read_b128 v[84:87], v21 offset:26624\n" \
"v_mfma_f32_16x16x32_bf16 v[28:31], a[96:99], v[88:91], v[28:31]\n" \
"v_mfma_f32_16x16x32_bf16 v[40:43], a[160:163], v[88:91], v[40:43]\n" \
"v_mfma_f32_16x16x32_bf16 v[44:47], a[224:227], v[88:91], v[44:47]\n" \
"v_mfma_f32_16x16x32_bf16 v[56:59], v[160:163], v[88:91], v[56:59]\n" \
"v_mfma_f32_16x16x32_bf16 v[60:63], v[224:227], v[88:91], v[60:63]\n" \
"s_waitcnt lgkmcnt(4)\n" \
"v_mfma_f32_16x16x32_bf16 v[32:35], a[32:35], v[92:95], v[32:35]\n" \
"v_mfma_f32_16x16x32_bf16 v[36:39], a[96:99], v[92:95], v[36:39]\n" \
"v_mfma_f32_16x16x32_bf16 v[48:51], a[160:163], v[92:95], v[48:51]\n" \
"v_mfma_f32_16x16x32_bf16 v[52:55], a[224:227], v[92:95], v[52:55]\n" \
"v_mfma_f32_16x16x32_bf16 v[64:67], v[160:163], v[92:95], v[64:67]\n" \
"v_mfma_f32_16x16x32_bf16 v[68:71], v[224:227], v[92:95], v[68:71]\n" \
"s_waitcnt lgkmcnt(3)\n" \
"v_mfma_f32_16x16x32_bf16 v[24:27], a[36:39], v[72:75], v[24:27]\n" \
"ds_read_b128 v[88:91], v21 offset:11264\n" \
"ds_read_b128 v[92:95], v21 offset:27648\n" \
"v_mfma_f32_16x16x32_bf16 v[28:31], a[100:103], v[72:75], v[28:31]\n" \
"v_mfma_f32_16x16x32_bf16 v[40:43], a[164:167], v[72:75], v[40:43]\n" \
"v_mfma_f32_16x16x32_bf16 v[44:47], a[228:231], v[72:75], v[44:47]\n" \
"v_mfma_f32_16x16x32_bf16 v[56:59], v[164:167], v[72:75], v[56:59]\n" \
"v_mfma_f32_16x16x32_bf16 v[60:63], v[228:231], v[72:75], v[60:63]\n" \
"s_waitcnt lgkmcnt(4)\n" \
"v_mfma_f32_16x16x32_bf16 v[32:35], a[36:39], v[76:79], v[32:35]\n" \
"v_mfma_f32_16x16x32_bf16 v[36:39], a[100:103], v[76:79], v[36:39]\n" \
"v_mfma_f32_16x16x32_bf16 v[48:51], a[164:167], v[76:79], v[48:51]\n" \
"v_mfma_f32_16x16x32_bf16 v[52:55], a[228:231], v[76:79], v[52:55]\n" \
"v_mfma_f32_16x16x32_bf16 v[64:67], v[164:167], v[76:79], v[64:67]\n" \
"v_mfma_f32_16x16x32_bf16 v[68:71], v[228:231], v[76:79], v[68:71]\n" \
"s_waitcnt lgkmcnt(3)\n" \
"v_mfma_f32_16x16x32_bf16 v[24:27], a[40:43], v[80:83], v[24:27]\n" \
"ds_read_b128 v[72:75], v21 offset:12288\n" \
"ds_read_b128 v[76:79], v21 offset:28672\n" \
"v_mfma_f32_16x16x32_bf16 v[28:31], a[104:107], v[80:83], v[28:31]\n" \
"v_mfma_f32_16x16x32_bf16 v[40:43], a[168:171], v[80:83], v[40:43]\n" \
"v_mfma_f32_16x16x32_bf16 v[44:47], a[232:235], v[80:83], v[44:47]\n" \
"v_mfma_f32_16x16x32_bf16 v[56:59], v[168:171], v[80:83], v[56:59]\n" \
"v_mfma_f32_16x16x32_bf16 v[60:63], v[232:235], v[80:83], v[60:63]\n" \
"s_waitcnt lgkmcnt(4)\n" \
"v_mfma_f32_16x16x32_bf16 v[32:35], a[40:43], v[84:87], v[32:35]\n" \
"v_mfma_f32_16x16x32_bf16 v[36:39], a[104:107], v[84:87], v[36:39]\n" \
"v_mfma_f32_16x16x32_bf16 v[48:51], a[168:171], v[84:87], v[48:51]\n" \
"v_mfma_f32_16x16x32_bf16 v[52:55], a[232:235], v[84:87], v[52:55]\n" \
"v_mfma_f32_16x16x32_bf16 v[64:67], v[168:171], v[84:87], v[64:67]\n" \
"v_mfma_f32_16x16x32_bf16 v[68:71], v[232:235], v[84:87], v[68:71]\n" \
"s_waitcnt lgkmcnt(3)\n" \
"v_mfma_f32_16x16x32_bf16 v[24:27], a[44:47], v[88:91], v[24:27]\n" \
"ds_read_b128 v[80:83], v21 offset:13312\n" \
"ds_read_b128 v[84:87], v21 offset:29696\n" \
"v_mfma_f32_16x16x32_bf16 v[28:31], a[108:111], v[88:91], v[28:31]\n" \
"v_mfma_f32_16x16x32_bf16 v[40:43], a[172:175], v[88:91], v[40:43]\n" \
"v_mfma_f32_16x16x32_bf16 v[44:47], a[236:239], v[88:91], v[44:47]\n" \
"v_mfma_f32_16x16x32_bf16 v[56:59], v[172:175], v[88:91], v[56:59]\n" \
"v_mfma_f32_16x16x32_bf16 v[60:63], v[236:239], v[88:91], v[60:63]\n" \
"s_waitcnt lgkmcnt(4)\n" \
"v_mfma_f32_16x16x32_bf16 v[32:35], a[44:47], v[92:95], v[32:35]\n" \
"v_mfma_f32_16x16x32_bf16 v[36:39], a[108:111], v[92:95], v[36:39]\n" \
"v_mfma_f32_16x16x32_bf16 v[48:51], a[172:175], v[92:95], v[48:51]\n" \
"v_mfma_f32_16x16x32_bf16 v[52:55], a[236:239], v[92:95], v[52:55]\n" \
"v_mfma_f32_16x16x32_bf16 v[64:67], v[172:175], v[92:95], v[64:67]\n" \
"v_mfma_f32_16x16x32_bf16 v[68:71], v[236:239], v[92:95], v[68:71]\n" \
"s_waitcnt lgkmcnt(3)\n" \
"v_mfma_f32_16x16x32_bf16 v[24:27], a[48:51], v[72:75], v[24:27]\n" \
"ds_read_b128 v[88:91], v21 offset:14336\n" \
"ds_read_b128 v[92:95], v21 offset:30720\n" \
"v_mfma_f32_16x16x32_bf16 v[28:31], a[112:115], v[72:75], v[28:31]\n" \
"v_mfma_f32_16x16x32_bf16 v[40:43], a[176:179], v[72:75], v[40:43]\n" \
"v_mfma_f32_16x16x32_bf16 v[44:47], a[240:243], v[72:75], v[44:47]\n" \
"v_mfma_f32_16x16x32_bf16 v[56:59], v[176:179], v[72:75], v[56:59]\n" \
"v_mfma_f32_16x16x32_bf16 v[60:63], v[240:243], v[72:75], v[60:63]\n" \
"s_waitcnt lgkmcnt(4)\n" \
"v_mfma_f32_16x16x32_bf16 v[32:35], a[48:51], v[76:79], v[32:35]\n" \
"v_mfma_f32_16x16x32_bf16 v[36:39], a[112:115], v[76:79], v[36:39]\n" \
"v_mfma_f32_16x16x32_bf16 v[48:51], a[176:179], v[76:79], v[48:51]\n" \
"v_mfma_f32_16x16x32_bf16 v[52:55], a[240:243], v[76:79], v[52:55]\n" \
"v_mfma_f32_16x16x32_bf16 v[64:67], v[176:179], v[76:79], v[64:67]\n" \
"v_mfma_f32_16x16x32_bf16 v[68:71], v[240:243], v[76:79], v[68:71]\n" \
"s_waitcnt lgkmcnt(3)\n" \
"v_mfma_f32_16x16x32_bf16 v[24:27], a[52:55], v[80:83], v[24:27]\n" \
"ds_read_b128 v[72:75], v21 offset:15360\n" \
"ds_read_b128 v[76:79], v21 offset:31744\n" \
"v_mfma_f32_16x16x32_bf16 v[28:31], a[116:119], v[80:83], v[28:31]\n" \
"v_mfma_f32_16x16x32_bf16 v[40:43], a[180:183], v[80:83], v[40:43]\n" \
"v_mfma_f32_16x16x32_bf16 v[44:47], a[244:247], v[80:83], v[44:47]\n" \
"v_mfma_f32_16x16x32_bf16 v[56:59], v[180:183], v[80:83], v[56:59]\n" \
"v_mfma_f32_16x16x32_bf16 v[60:63], v[244:247], v[80:83], v[60:63]\n" \
"s_waitcnt lgkmcnt(4)\n" \
"v_mfma_f32_16x16x32_bf16 v[32:35], a[52:55], v[84:87], v[32:35]\n" \
"v_mfma_f32_16x16x32_bf16 v[36:39], a[116:119], v[84:87], v[36:39]\n" \
"v_mfma_f32_16x16x32_bf16 v[48:51], a[180:183], v[84:87], v[48:51]\n" \
"v_mfma_f32_16x16x32_bf16 v[52:55], a[244:247], v[84:87], v[52:55]\n" \
"v_mfma_f32_16x16x32_bf16 v[64:67], v[180:183], v[84:87], v[64:67]\n" \
"v_mfma_f32_16x16x32_bf16 v[68:71], v[244:247], v[84:87], v[68:71]\n" \
"s_waitcnt lgkmcnt(3)\n" \
"v_mfma_f32_16x16x32_bf16 v[24:27], a[56:59], v[88:91], v[24:27]\n" \
"v_mfma_f32_16x16x32_bf16 v[28:31], a[120:123], v[88:91], v[28:31]\n" \
"v_mfma_f32_16x16x32_bf16 v[40:43], a[184:187], v[88:91], v[40:43]\n" \
"v_mfma_f32_16x16x32_bf16 v[44:47], a[248:251], v[88:91], v[44:47]\n" \
"v_mfma_f32_16x16x32_bf16 v[56:59], v[184:187], v[88:91], v[56:59]\n" \
"v_mfma_f32_16x16x32_bf16 v[60:63], v[248:251], v[88:91], v[60:63]\n" \
"s_waitcnt lgkmcnt(2)\n" \
"v_mfma_f32_16x16x32_bf16 v[32:35], a[56:59], v[92:95], v[32:35]\n" \
"v_mfma_f32_16x16x32_bf16 v[36:39], a[120:123], v[92:95], v[36:39]\n" \
"v_mfma_f32_16x16x32_bf16 v[48:51], a[184:187], v[92:95], v[48:51]\n" \
"v_mfma_f32_16x16x32_bf16 v[52:55], a[248:251], v[92:95], v[52:55]\n" \
"v_mfma_f32_16x16x32_bf16 v[64:67], v[184:187], v[92:95], v[64:67]\n" \
"v_mfma_f32_16x16x32_bf16 v[68:71], v[248:251], v[92:95], v[68:71]\n" \
"s_waitcnt lgkmcnt(1)\n" \
"v_mfma_f32_16x16x32_bf16 v[24:27], a[60:63], v[72:75], v[24:27]\n" \
"v_mfma_f32_16x16x32_bf16 v[28:31], a[124:127], v[72:75], v[28:31]\n" \
"v_mfma_f32_16x16x32_bf16 v[40:43], a[188:191], v[72:75], v[40:43]\n" \
"v_mfma_f32_16x16x32_bf16 v[44:47], a[252:255], v[72:75], v[44:47]\n" \
"v_mfma_f32_16x16x32_bf16 v[56:59], v[188:191], v[72:75], v[56:59]\n" \
"v_mfma_f32_16x16x32_bf16 v[60:63], v[252:255], v[72:75], v[60:63]\n" \
"s_waitcnt lgkmcnt(0)\n" \
"v_mfma_f32_16x16x32_bf16 v[32:35], a[60:63], v[76:79], v[32:35]\n" \
"v_mfma_f32_16x16x32_bf16 v[36:39], a[124:127], v[76:79], v[36:39]\n" \
"v_mfma_f32_16x16x32_bf16 v[48:51], a[188:191], v[76:79], v[48:51]\n" \
"v_mfma_f32_16x16x32_bf16 v[52:55], a[252:255], v[76:79], v[52:55]\n" \
"v_mfma_f32_16x16x32_bf16 v[64:67], v[188:191], v[76:79], v[64:67]\n" \
"v_mfma_f32_16x16x32_bf16 v[68:71], v[252:255], v[76:79], v[68:71]\n" \
"v_add_f32 v24, v24, v96\n" \
"v_add_f32 v25, v25, v97\n" \
"v_add_f32 v26, v26, v98\n" \
"v_add_f32 v27, v27, v99\n" \
"v_add_f32 v28, v28, v100\n" \
"v_add_f32 v29, v29, v101\n" \
"v_add_f32 v30, v30, v102\n" \
"v_add_f32 v31, v31, v103\n" \
"v_max_f32 v24, 0, v24\n" \
"v_max_f32 v25, 0, v25\n" \
"v_max_f32 v26, 0, v26\n" \
"v_max_f32 v27, 0, v27\n" \
"v_max_f32 v28, 0, v28\n" \
"v_max_f32 v29, 0, v29\n" \
"v_max_f32 v30, 0, v30\n" \
"v_max_f32 v31, 0, v31\n" \
"v_mul_f32 v24, v24, v24\n" \
"v_mul_f32 v25, v25, v25\n" \
"v_mul_f32 v26, v26, v26\n" \
"v_mul_f32 v27, v27, v27\n" \
"v_mul_f32 v28, v28, v28\n" \
"v_mul_f32 v29, v29, v29\n" \
"v_mul_f32 v30, v30, v30\n" \
"v_mul_f32 v31, v31, v31\n" \
"v_cvt_pk_bf16_f32 v120, v24, v25\n" \
"v_cvt_pk_bf16_f32 v121, v26, v27\n" \
"v_cvt_pk_bf16_f32 v122, v28, v29\n" \
"v_cvt_pk_bf16_f32 v123, v30, v31\n" \
"buffer_store_dwordx4 v[120:123], v5, s[40:43], s71 offen sc1\n" \
"v_add_f32 v32, v32, v96\n" \
"v_add_f32 v33, v33, v97\n" \
"v_add_f32 v34, v34, v98\n" \
"v_add_f32 v35, v35, v99\n" \
"v_add_f32 v36, v36, v100\n" \
"v_add_f32 v37, v37, v101\n" \
"v_add_f32 v38, v38, v102\n" \
"v_add_f32 v39, v39, v103\n" \
"v_max_f32 v32, 0, v32\n" \
"v_max_f32 v33, 0, v33\n" \
"v_max_f32 v34, 0, v34\n" \
"v_max_f32 v35, 0, v35\n" \
"v_max_f32 v36, 0, v36\n" \
"v_max_f32 v37, 0, v37\n" \
"v_max_f32 v38, 0, v38\n" \
"v_max_f32 v39, 0, v39\n" \
"v_mul_f32 v32, v32, v32\n" \
"v_mul_f32 v33, v33, v33\n" \
"v_mul_f32 v34, v34, v34\n" \
"v_mul_f32 v35, v35, v35\n" \
"v_mul_f32 v36, v36, v36\n" \
"v_mul_f32 v37, v37, v37\n" \
"v_mul_f32 v38, v38, v38\n" \
"v_mul_f32 v39, v39, v39\n" \
"v_cvt_pk_bf16_f32 v124, v32, v33\n" \
"v_cvt_pk_bf16_f32 v125, v34, v35\n" \
"v_cvt_pk_bf16_f32 v126, v36, v37\n" \
"v_cvt_pk_bf16_f32 v127, v38, v39\n" \
"s_add_u32 s67, s71, 44032\n" \
"buffer_store_dwordx4 v[124:127], v5, s[40:43], s67 offen sc1\n" \
"v_add_f32 v40, v40, v104\n" \
"v_add_f32 v41, v41, v105\n" \
"v_add_f32 v42, v42, v106\n" \
"v_add_f32 v43, v43, v107\n" \
"v_add_f32 v44, v44, v108\n" \
"v_add_f32 v45, v45, v109\n" \
"v_add_f32 v46, v46, v110\n" \
"v_add_f32 v47, v47, v111\n" \
"v_max_f32 v40, 0, v40\n" \
"v_max_f32 v41, 0, v41\n" \
"v_max_f32 v42, 0, v42\n" \
"v_max_f32 v43, 0, v43\n" \
"v_max_f32 v44, 0, v44\n" \
"v_max_f32 v45, 0, v45\n" \
"v_max_f32 v46, 0, v46\n" \
"v_max_f32 v47, 0, v47\n" \
"v_mul_f32 v40, v40, v40\n" \
"v_mul_f32 v41, v41, v41\n" \
"v_mul_f32 v42, v42, v42\n" \
"v_mul_f32 v43, v43, v43\n" \
"v_mul_f32 v44, v44, v44\n" \
"v_mul_f32 v45, v45, v45\n" \
"v_mul_f32 v46, v46, v46\n" \
"v_mul_f32 v47, v47, v47\n" \
"v_cvt_pk_bf16_f32 v120, v40, v41\n" \
"v_cvt_pk_bf16_f32 v121, v42, v43\n" \
"v_cvt_pk_bf16_f32 v122, v44, v45\n" \
"v_cvt_pk_bf16_f32 v123, v46, v47\n" \
"buffer_store_dwordx4 v[120:123], v6, s[40:43], s71 offen sc1\n" \
"v_add_f32 v48, v48, v104\n" \
"v_add_f32 v49, v49, v105\n" \
"v_add_f32 v50, v50, v106\n" \
"v_add_f32 v51, v51, v107\n" \
"v_add_f32 v52, v52, v108\n" \
"v_add_f32 v53, v53, v109\n" \
"v_add_f32 v54, v54, v110\n" \
"v_add_f32 v55, v55, v111\n" \
"v_max_f32 v48, 0, v48\n" \
"v_max_f32 v49, 0, v49\n" \
"v_max_f32 v50, 0, v50\n" \
"v_max_f32 v51, 0, v51\n" \
"v_max_f32 v52, 0, v52\n" \
"v_max_f32 v53, 0, v53\n" \
"v_max_f32 v54, 0, v54\n" \
"v_max_f32 v55, 0, v55\n" \
"v_mul_f32 v48, v48, v48\n" \
"v_mul_f32 v49, v49, v49\n" \
"v_mul_f32 v50, v50, v50\n" \
"v_mul_f32 v51, v51, v51\n" \
"v_mul_f32 v52, v52, v52\n" \
"v_mul_f32 v53, v53, v53\n" \
"v_mul_f32 v54, v54, v54\n" \
"v_mul_f32 v55, v55, v55\n" \
"v_cvt_pk_bf16_f32 v124, v48, v49\n" \
"v_cvt_pk_bf16_f32 v125, v50, v51\n" \
"v_cvt_pk_bf16_f32 v126, v52, v53\n" \
"v_cvt_pk_bf16_f32 v127, v54, v55\n" \
"s_add_u32 s67, s71, 44032\n" \
"buffer_store_dwordx4 v[124:127], v6, s[40:43], s67 offen sc1\n" \
"v_add_f32 v56, v56, v112\n" \
"v_add_f32 v57, v57, v113\n" \
"v_add_f32 v58, v58, v114\n" \
"v_add_f32 v59, v59, v115\n" \
"v_add_f32 v60, v60, v116\n" \
"v_add_f32 v61, v61, v117\n" \
"v_add_f32 v62, v62, v118\n" \
"v_add_f32 v63, v63, v119\n" \
"v_max_f32 v56, 0, v56\n" \
"v_max_f32 v57, 0, v57\n" \
"v_max_f32 v58, 0, v58\n" \
"v_max_f32 v59, 0, v59\n" \
"v_max_f32 v60, 0, v60\n" \
"v_max_f32 v61, 0, v61\n" \
"v_max_f32 v62, 0, v62\n" \
"v_max_f32 v63, 0, v63\n" \
"v_mul_f32 v56, v56, v56\n" \
"v_mul_f32 v57, v57, v57\n" \
"v_mul_f32 v58, v58, v58\n" \
"v_mul_f32 v59, v59, v59\n" \
"v_mul_f32 v60, v60, v60\n" \
"v_mul_f32 v61, v61, v61\n" \
"v_mul_f32 v62, v62, v62\n" \
"v_mul_f32 v63, v63, v63\n" \
"v_cvt_pk_bf16_f32 v120, v56, v57\n" \
"v_cvt_pk_bf16_f32 v121, v58, v59\n" \
"v_cvt_pk_bf16_f32 v122, v60, v61\n" \
"v_cvt_pk_bf16_f32 v123, v62, v63\n" \
"buffer_store_dwordx4 v[120:123], v7, s[40:43], s71 offen sc1\n" \
"v_add_f32 v64, v64, v112\n" \
"v_add_f32 v65, v65, v113\n" \
"v_add_f32 v66, v66, v114\n" \
"v_add_f32 v67, v67, v115\n" \
"v_add_f32 v68, v68, v116\n" \
"v_add_f32 v69, v69, v117\n" \
"v_add_f32 v70, v70, v118\n" \
"v_add_f32 v71, v71, v119\n" \
"v_max_f32 v64, 0, v64\n" \
"v_max_f32 v65, 0, v65\n" \
"v_max_f32 v66, 0, v66\n" \
"v_max_f32 v67, 0, v67\n" \
"v_max_f32 v68, 0, v68\n" \
"v_max_f32 v69, 0, v69\n" \
"v_max_f32 v70, 0, v70\n" \
"v_max_f32 v71, 0, v71\n" \
"v_mul_f32 v64, v64, v64\n" \
"v_mul_f32 v65, v65, v65\n" \
"v_mul_f32 v66, v66, v66\n" \
"v_mul_f32 v67, v67, v67\n" \
"v_mul_f32 v68, v68, v68\n" \
"v_mul_f32 v69, v69, v69\n" \
"v_mul_f32 v70, v70, v70\n" \
"v_mul_f32 v71, v71, v71\n" \
"v_cvt_pk_bf16_f32 v124, v64, v65\n" \
"v_cvt_pk_bf16_f32 v125, v66, v67\n" \
"v_cvt_pk_bf16_f32 v126, v68, v69\n" \
"v_cvt_pk_bf16_f32 v127, v70, v71\n" \
"s_add_u32 s67, s71, 44032\n" \
"buffer_store_dwordx4 v[124:127], v7, s[40:43], s67 offen sc1\n" \
"v_xor_b32 v21, 32768, v21\n" \
"s_add_u32 s71, s71, 88064\n" \
"s_waitcnt vmcnt(6)\n" \
"s_barrier \n" \
"s_add_u32 s66, s66, 1\n" \
"s_cmp_lt_u32 s66, 4\n" \
"s_cbranch_scc1 W_f1%=\n" \
"s_waitcnt vmcnt(0)\n" \
"s_barrier \n" \
"s_cmp_lg_u32 s64, 0\n" \
"s_cbranch_scc1 W_s1%=\n" \
"s_mov_b64 exec, 1\n" \
"buffer_atomic_add v20, v17, s[40:43], 0 offen\n" \
"s_mov_b64 exec, -1\n" \
"W_s1%=:\n" \
"buffer_load_dwordx4 v[112:115], v12, s[92:95], 0 offen\n" \
"s_add_u32 m0, s68, 0\n" \
"s_mov_b32 s67, 0\n" \
"buffer_load_dwordx4 v11, s[48:51], s67 offen lds\n" \
"s_add_u32 m0, s68, 1024\n" \
"s_mov_b32 s67, 16384\n" \
"buffer_load_dwordx4 v11, s[48:51], s67 offen lds\n" \
"s_add_u32 m0, s68, 2048\n" \
"s_mov_b32 s67, 64\n" \
"buffer_load_dwordx4 v11, s[48:51], s67 offen lds\n" \
"s_add_u32 m0, s68, 3072\n" \
"s_mov_b32 s67, 16448\n" \
"buffer_load_dwordx4 v11, s[48:51], s67 offen lds\n" \
"s_add_u32 m0, s68, 4096\n" \
"s_mov_b32 s67, 128\n" \
"buffer_load_dwordx4 v11, s[48:51], s67 offen lds\n" \
"s_add_u32 m0, s68, 5120\n" \
"s_mov_b32 s67, 16512\n" \
"buffer_load_dwordx4 v11, s[48:51], s67 offen lds\n" \
"s_add_u32 m0, s68, 6144\n" \
"s_mov_b32 s67, 192\n" \
"buffer_load_dwordx4 v11, s[48:51], s67 offen lds\n" \
"s_add_u32 m0, s68, 7168\n" \
"s_mov_b32 s67, 16576\n" \
"buffer_load_dwordx4 v11, s[48:51], s67 offen lds\n" \
"s_add_u32 m0, s68, 8192\n" \
"s_mov_b32 s67, 32768\n" \
"buffer_load_dwordx4 v11, s[48:51], s67 offen lds\n" \
"s_add_u32 m0, s68, 9216\n" \
"s_mov_b32 s67, 49152\n" \
"buffer_load_dwordx4 v11, s[48:51], s67 offen lds\n" \
"s_add_u32 m0, s68, 10240\n" \
"s_mov_b32 s67, 32832\n" \
"buffer_load_dwordx4 v11, s[48:51], s67 offen lds\n" \
"s_add_u32 m0, s68, 11264\n" \
"s_mov_b32 s67, 49216\n" \
"buffer_load_dwordx4 v11, s[48:51], s67 offen lds\n" \
"s_add_u32 m0, s68, 12288\n" \
"s_mov_b32 s67, 32896\n" \
"buffer_load_dwordx4 v11, s[48:51], s67 offen lds\n" \
"s_add_u32 m0, s68, 13312\n" \
"s_mov_b32 s67, 49280\n" \
"buffer_load_dwordx4 v11, s[48:51], s67 offen lds\n" \
"s_add_u32 m0, s68, 14336\n" \
"s_mov_b32 s67, 32960\n" \
"buffer_load_dwordx4 v11, s[48:51], s67 offen lds\n" \
"s_add_u32 m0, s68, 15360\n" \
"s_mov_b32 s67, 49344\n" \
"buffer_load_dwordx4 v11, s[48:51], s67 offen lds\n" \
"s_add_u32 m0, s68, 16384\n" \
"s_mov_b32 s67, 65536\n" \
"buffer_load_dwordx4 v11, s[48:51], s67 offen lds\n" \
"s_add_u32 m0, s68, 17408\n" \
"s_mov_b32 s67, 81920\n" \
"buffer_load_dwordx4 v11, s[48:51], s67 offen lds\n" \
"s_add_u32 m0, s68, 18432\n" \
"s_mov_b32 s67, 65600\n" \
"buffer_load_dwordx4 v11, s[48:51], s67 offen lds\n" \
"s_add_u32 m0, s68, 19456\n" \
"s_mov_b32 s67, 81984\n" \
"buffer_load_dwordx4 v11, s[48:51], s67 offen lds\n" \
"s_add_u32 m0, s68, 20480\n" \
"s_mov_b32 s67, 65664\n" \
"buffer_load_dwordx4 v11, s[48:51], s67 offen lds\n" \
"s_add_u32 m0, s68, 21504\n" \
"s_mov_b32 s67, 82048\n" \
"buffer_load_dwordx4 v11, s[48:51], s67 offen lds\n" \
"s_add_u32 m0, s68, 22528\n" \
"s_mov_b32 s67, 65728\n" \
"buffer_load_dwordx4 v11, s[48:51], s67 offen lds\n" \
"s_add_u32 m0, s68, 23552\n" \
"s_mov_b32 s67, 82112\n" \
"buffer_load_dwordx4 v11, s[48:51], s67 offen lds\n" \
"s_add_u32 m0, s68, 24576\n" \
"s_mov_b32 s67, 98304\n" \
"buffer_load_dwordx4 v11, s[48:51], s67 offen lds\n" \
"s_add_u32 m0, s68, 25600\n" \
"s_mov_b32 s67, 114688\n" \
"buffer_load_dwordx4 v11, s[48:51], s67 offen lds\n" \
"s_add_u32 m0, s68, 26624\n" \
"s_mov_b32 s67, 98368\n" \
"buffer_load_dwordx4 v11, s[48:51], s67 offen lds\n" \
"s_add_u32 m0, s68, 27648\n" \
"s_mov_b32 s67, 114752\n" \
"buffer_load_dwordx4 v11, s[48:51], s67 offen lds\n" \
"s_waitcnt vmcnt(26)\n" \
"ds_read_b64_tr_b16 a[0:1], v4 offset:0\n" \
"ds_read_b64_tr_b16 a[2:3], v4 offset:256\n" \
"ds_read_b64_tr_b16 a[44:45], v4 offset:8\n" \
"ds_read_b64_tr_b16 a[46:47], v4 offset:264\n" \
"s_waitcnt vmcnt(24)\n" \
"ds_read_b64_tr_b16 a[88:89], v4 offset:2048\n" \
"ds_read_b64_tr_b16 a[90:91], v4 offset:2304\n" \
"ds_read_b64_tr_b16 a[132:133], v4 offset:2056\n" \
"ds_read_b64_tr_b16 a[134:135], v4 offset:2312\n" \
"s_waitcnt lgkmcnt(4)\n" \
"s_add_u32 m0, s68, 0\n" \
"s_mov_b32 s67, 98432\n" \
"buffer_load_dwordx4 v11, s[48:51], s67 offen lds\n" \
"s_add_u32 m0, s68, 1024\n" \
"s_mov_b32 s67, 114816\n" \
"buffer_load_dwordx4 v11, s[48:51], s67 offen lds\n" \
"s_waitcnt vmcnt(24)\n" \
"ds_read_b64_tr_b16 a[176:177], v4 offset:4096\n" \
"ds_read_b64_tr_b16 a[178:179], v4 offset:4352\n" \
"ds_read_b64_tr_b16 a[220:221], v4 offset:4104\n" \
"ds_read_b64_tr_b16 a[222:223], v4 offset:4360\n" \
"s_waitcnt lgkmcnt(4)\n" \
"s_add_u32 m0, s68, 2048\n" \
"s_mov_b32 s67, 98496\n" \
"buffer_load_dwordx4 v11, s[48:51], s67 offen lds\n" \
"s_add_u32 m0, s68, 3072\n" \
"s_mov_b32 s67, 114880\n" \
"buffer_load_dwordx4 v11, s[48:51], s67 offen lds\n" \
"s_waitcnt vmcnt(24)\n" \
"ds_read_b64_tr_b16 v[168:169], v4 offset:6144\n" \
"ds_read_b64_tr_b16 v[170:171], v4 offset:6400\n" \
"ds_read_b64_tr_b16 v[212:213], v4 offset:6152\n" \
"ds_read_b64_tr_b16 v[214:215], v4 offset:6408\n" \
"s_waitcnt lgkmcnt(4)\n" \
"s_add_u32 m0, s68, 4096\n" \
"s_mov_b32 s67, 131072\n" \
"buffer_load_dwordx4 v11, s[48:51], s67 offen lds\n" \
"s_add_u32 m0, s68, 5120\n" \
"s_mov_b32 s67, 147456\n" \
"buffer_load_dwordx4 v11, s[48:51], s67 offen lds\n" \
"s_waitcnt vmcnt(24)\n" \
"ds_read_b64_tr_b16 a[4:5], v4 offset:8192\n" \
"ds_read_b64_tr_b16 a[6:7], v4 offset:8448\n" \
"ds_read_b64_tr_b16 a[48:49], v4 offset:8200\n" \
"ds_read_b64_tr_b16 a[50:51], v4 offset:8456\n" \
"s_waitcnt lgkmcnt(4)\n" \
"s_add_u32 m0, s68, 6144\n" \
"s_mov_b32 s67, 131136\n" \
"buffer_load_dwordx4 v11, s[48:51], s67 offen lds\n" \
"s_add_u32 m0, s68, 7168\n" \
"s_mov_b32 s67, 147520\n" \
"buffer_load_dwordx4 v11, s[48:51], s67 offen lds\n" \
"s_waitcnt vmcnt(24)\n" \
"ds_read_b64_tr_b16 a[92:93], v4 offset:10240\n" \
"ds_read_b64_tr_b16 a[94:95], v4 offset:10496\n" \
"ds_read_b64_tr_b16 a[136:137], v4 offset:10248\n" \
"ds_read_b64_tr_b16 a[138:139], v4 offset:10504\n" \
"s_waitcnt lgkmcnt(4)\n" \
"s_add_u32 m0, s68, 8192\n" \
"s_mov_b32 s67, 131200\n" \
"buffer_load_dwordx4 v11, s[48:51], s67 offen lds\n" \
"s_add_u32 m0, s68, 9216\n" \
"s_mov_b32 s67, 147584\n" \
"buffer_load_dwordx4 v11, s[48:51], s67 offen lds\n" \
"s_waitcnt vmcnt(24)\n" \
"ds_read_b64_tr_b16 a[180:181], v4 offset:12288\n" \
"ds_read_b64_tr_b16 a[182:183], v4 offset:12544\n" \
"ds_read_b64_tr_b16 a[224:225], v4 offset:12296\n" \
"ds_read_b64_tr_b16 a[226:227], v4 offset:12552\n" \
"s_waitcnt lgkmcnt(4)\n" \
"s_add_u32 m0, s68, 10240\n" \
"s_mov_b32 s67, 131264\n" \
"buffer_load_dwordx4 v11, s[48:51], s67 offen lds\n" \
"s_add_u32 m0, s68, 11264\n" \
"s_mov_b32 s67, 147648\n" \
"buffer_load_dwordx4 v11, s[48:51], s67 offen lds\n" \
"s_waitcnt vmcnt(24)\n" \
"ds_read_b64_tr_b16 v[172:173], v4 offset:14336\n" \
"ds_read_b64_tr_b16 v[174:175], v4 offset:14592\n" \
"ds_read_b64_tr_b16 v[216:217], v4 offset:14344\n" \
"ds_read_b64_tr_b16 v[218:219], v4 offset:14600\n" \
"s_waitcnt lgkmcnt(4)\n" \
"s_add_u32 m0, s68, 12288\n" \
"s_mov_b32 s67, 163840\n" \
"buffer_load_dwordx4 v11, s[48:51], s67 offen lds\n" \
"s_add_u32 m0, s68, 13312\n" \
"s_mov_b32 s67, 180224\n" \
"buffer_load_dwordx4 v11, s[48:51], s67 offen lds\n" \
"s_waitcnt vmcnt(24)\n" \
"ds_read_b64_tr_b16 a[8:9], v4 offset:16384\n" \
"ds_read_b64_tr_b16 a[10:11], v4 offset:16640\n" \
"ds_read_b64_tr_b16 a[52:53], v4 offset:16392\n" \
"ds_read_b64_tr_b16 a[54:55], v4 offset:16648\n" \
"s_waitcnt lgkmcnt(4)\n" \
"s_add_u32 m0, s68, 14336\n" \
"s_mov_b32 s67, 163904\n" \
"buffer_load_dwordx4 v11, s[48:51], s67 offen lds\n" \
"s_add_u32 m0, s68, 15360\n" \
"s_mov_b32 s67, 180288\n" \
"buffer_load_dwordx4 v11, s[48:51], s67 offen lds\n" \
"s_waitcnt vmcnt(24)\n" \
"ds_read_b64_tr_b16 a[96:97], v4 offset:18432\n" \
"ds_read_b64_tr_b16 a[98:99], v4 offset:18688\n" \
"ds_read_b64_tr_b16 a[140:141], v4 offset:18440\n" \
"ds_read_b64_tr_b16 a[142:143], v4 offset:18696\n" \
"s_waitcnt lgkmcnt(4)\n" \
"s_add_u32 m0, s68, 16384\n" \
"s_mov_b32 s67, 163968\n" \
"buffer_load_dwordx4 v11, s[48:51], s67 offen lds\n" \
"s_add_u32 m0, s68, 17408\n" \
"s_mov_b32 s67, 180352\n" \
"buffer_load_dwordx4 v11, s[48:51], s67 offen lds\n" \
"s_waitcnt vmcnt(24)\n" \
"ds_read_b64_tr_b16 a[184:185], v4 offset:20480\n" \
"ds_read_b64_tr_b16 a[186:187], v4 offset:20736\n" \
"ds_read_b64_tr_b16 a[228:229], v4 offset:20488\n" \
"ds_read_b64_tr_b16 a[230:231], v4 offset:20744\n" \
"s_waitcnt lgkmcnt(4)\n" \
"s_add_u32 m0, s68, 18432\n" \
"s_mov_b32 s67, 164032\n" \
"buffer_load_dwordx4 v11, s[48:51], s67 offen lds\n" \
"s_add_u32 m0, s68, 19456\n" \
"s_mov_b32 s67, 180416\n" \
"buffer_load_dwordx4 v11, s[48:51], s67 offen lds\n" \
"s_waitcnt vmcnt(24)\n" \
"ds_read_b64_tr_b16 v[176:177], v4 offset:22528\n" \
"ds_read_b64_tr_b16 v[178:179], v4 offset:22784\n" \
"ds_read_b64_tr_b16 v[220:221], v4 offset:22536\n" \
"ds_read_b64_tr_b16 v[222:223], v4 offset:22792\n" \
"s_waitcnt lgkmcnt(4)\n" \
"s_add_u32 m0, s68, 20480\n" \
"s_mov_b32 s67, 196608\n" \
"buffer_load_dwordx4 v11, s[48:51], s67 offen lds\n" \
"s_add_u32 m0, s68, 21504\n" \
"s_mov_b32 s67, 212992\n" \
"buffer_load_dwordx4 v11, s[48:51], s67 offen lds\n" \
"s_waitcnt vmcnt(24)\n" \
"ds_read_b64_tr_b16 a[12:13], v4 offset:24576\n" \
"ds_read_b64_tr_b16 a[14:15], v4 offset:24832\n" \
"ds_read_b64_tr_b16 a[56:57], v4 offset:24584\n" \
"ds_read_b64_tr_b16 a[58:59], v4 offset:24840\n" \
"s_waitcnt lgkmcnt(4)\n" \
"s_add_u32 m0, s68, 22528\n" \
"s_mov_b32 s67, 196672\n" \
"buffer_load_dwordx4 v11, s[48:51], s67 offen lds\n" \
"s_add_u32 m0, s68, 23552\n" \
"s_mov_b32 s67, 213056\n" \
"buffer_load_dwordx4 v11, s[48:51], s67 offen lds\n" \
"s_waitcnt vmcnt(24)\n" \
"ds_read_b64_tr_b16 a[100:101], v4 offset:26624\n" \
"ds_read_b64_tr_b16 a[102:103], v4 offset:26880\n" \
"ds_read_b64_tr_b16 a[144:145], v4 offset:26632\n" \
"ds_read_b64_tr_b16 a[146:147], v4 offset:26888\n" \
"s_waitcnt lgkmcnt(4)\n" \
"s_add_u32 m0, s68, 24576\n" \
"s_mov_b32 s67, 196736\n" \
"buffer_load_dwordx4 v11, s[48:51], s67 offen lds\n" \
"s_add_u32 m0, s68, 25600\n" \
"s_mov_b32 s67, 213120\n" \
"buffer_load_dwordx4 v11, s[48:51], s67 offen lds\n" \
"s_waitcnt vmcnt(24)\n" \
"ds_read_b64_tr_b16 a[188:189], v4 offset:0\n" \
"ds_read_b64_tr_b16 a[190:191], v4 offset:256\n" \
"ds_read_b64_tr_b16 a[232:233], v4 offset:8\n" \
"ds_read_b64_tr_b16 a[234:235], v4 offset:264\n" \
"s_waitcnt lgkmcnt(4)\n" \
"s_add_u32 m0, s68, 26624\n" \
"s_mov_b32 s67, 196800\n" \
"buffer_load_dwordx4 v11, s[48:51], s67 offen lds\n" \
"s_add_u32 m0, s68, 27648\n" \
"s_mov_b32 s67, 213184\n" \
"buffer_load_dwordx4 v11, s[48:51], s67 offen lds\n" \
"s_waitcnt vmcnt(24)\n" \
"ds_read_b64_tr_b16 v[180:181], v4 offset:2048\n" \
"ds_read_b64_tr_b16 v[182:183], v4 offset:2304\n" \
"ds_read_b64_tr_b16 v[224:225], v4 offset:2056\n" \
"ds_read_b64_tr_b16 v[226:227], v4 offset:2312\n" \
"s_waitcnt lgkmcnt(4)\n" \
"s_add_u32 m0, s68, 0\n" \
"s_mov_b32 s67, 229376\n" \
"buffer_load_dwordx4 v11, s[48:51], s67 offen lds\n" \
"s_add_u32 m0, s68, 1024\n" \
"s_mov_b32 s67, 245760\n" \
"buffer_load_dwordx4 v11, s[48:51], s67 offen lds\n" \
"s_waitcnt vmcnt(24)\n" \
"ds_read_b64_tr_b16 a[16:17], v4 offset:4096\n" \
"ds_read_b64_tr_b16 a[18:19], v4 offset:4352\n" \
"ds_read_b64_tr_b16 a[60:61], v4 offset:4104\n" \
"ds_read_b64_tr_b16 a[62:63], v4 offset:4360\n" \
"s_waitcnt lgkmcnt(4)\n" \
"s_add_u32 m0, s68, 2048\n" \
"s_mov_b32 s67, 229440\n" \
"buffer_load_dwordx4 v11, s[48:51], s67 offen lds\n" \
"s_add_u32 m0, s68, 3072\n" \
"s_mov_b32 s67, 245824\n" \
"buffer_load_dwordx4 v11, s[48:51], s67 offen lds\n" \
"s_waitcnt vmcnt(24)\n" \
"ds_read_b64_tr_b16 a[104:105], v4 offset:6144\n" \
"ds_read_b64_tr_b16 a[106:107], v4 offset:6400\n" \
"ds_read_b64_tr_b16 a[148:149], v4 offset:6152\n" \
"ds_read_b64_tr_b16 a[150:151], v4 offset:6408\n" \
"s_waitcnt lgkmcnt(4)\n" \
"s_add_u32 m0, s68, 4096\n" \
"s_mov_b32 s67, 229504\n" \
"buffer_load_dwordx4 v11, s[48:51], s67 offen lds\n" \
"s_add_u32 m0, s68, 5120\n" \
"s_mov_b32 s67, 245888\n" \
"buffer_load_dwordx4 v11, s[48:51], s67 offen lds\n" \
"s_waitcnt vmcnt(24)\n" \
"ds_read_b64_tr_b16 a[192:193], v4 offset:8192\n" \
"ds_read_b64_tr_b16 a[194:195], v4 offset:8448\n" \
"ds_read_b64_tr_b16 a[236:237], v4 offset:8200\n" \
"ds_read_b64_tr_b16 a[238:239], v4 offset:8456\n" \
"s_waitcnt lgkmcnt(4)\n" \
"s_add_u32 m0, s68, 6144\n" \
"s_mov_b32 s67, 229568\n" \
"buffer_load_dwordx4 v11, s[48:51], s67 offen lds\n" \
"s_add_u32 m0, s68, 7168\n" \
"s_mov_b32 s67, 245952\n" \
"buffer_load_dwordx4 v11, s[48:51], s67 offen lds\n" \
"s_waitcnt vmcnt(24)\n" \
"ds_read_b64_tr_b16 v[184:185], v4 offset:10240\n" \
"ds_read_b64_tr_b16 v[186:187], v4 offset:10496\n" \
"ds_read_b64_tr_b16 v[228:229], v4 offset:10248\n" \
"ds_read_b64_tr_b16 v[230:231], v4 offset:10504\n" \
"s_waitcnt lgkmcnt(4)\n" \
"s_add_u32 m0, s68, 8192\n" \
"s_mov_b32 s67, 262144\n" \
"buffer_load_dwordx4 v11, s[48:51], s67 offen lds\n" \
"s_add_u32 m0, s68, 9216\n" \
"s_mov_b32 s67, 278528\n" \
"buffer_load_dwordx4 v11, s[48:51], s67 offen lds\n" \
"s_waitcnt vmcnt(24)\n" \
"ds_read_b64_tr_b16 a[20:21], v4 offset:12288\n" \
"ds_read_b64_tr_b16 a[22:23], v4 offset:12544\n" \
"ds_read_b64_tr_b16 a[64:65], v4 offset:12296\n" \
"ds_read_b64_tr_b16 a[66:67], v4 offset:12552\n" \
"s_waitcnt lgkmcnt(4)\n" \
"s_add_u32 m0, s68, 10240\n" \
"s_mov_b32 s67, 262208\n" \
"buffer_load_dwordx4 v11, s[48:51], s67 offen lds\n" \
"s_add_u32 m0, s68, 11264\n" \
"s_mov_b32 s67, 278592\n" \
"buffer_load_dwordx4 v11, s[48:51], s67 offen lds\n" \
"s_waitcnt vmcnt(24)\n" \
"ds_read_b64_tr_b16 a[108:109], v4 offset:14336\n" \
"ds_read_b64_tr_b16 a[110:111], v4 offset:14592\n" \
"ds_read_b64_tr_b16 a[152:153], v4 offset:14344\n" \
"ds_read_b64_tr_b16 a[154:155], v4 offset:14600\n" \
"s_waitcnt lgkmcnt(4)\n" \
"s_add_u32 m0, s68, 12288\n" \
"s_mov_b32 s67, 262272\n" \
"buffer_load_dwordx4 v11, s[48:51], s67 offen lds\n" \
"s_add_u32 m0, s68, 13312\n" \
"s_mov_b32 s67, 278656\n" \
"buffer_load_dwordx4 v11, s[48:51], s67 offen lds\n" \
"s_waitcnt vmcnt(24)\n" \
"ds_read_b64_tr_b16 a[196:197], v4 offset:16384\n" \
"ds_read_b64_tr_b16 a[198:199], v4 offset:16640\n" \
"ds_read_b64_tr_b16 a[240:241], v4 offset:16392\n" \
"ds_read_b64_tr_b16 a[242:243], v4 offset:16648\n" \
"s_waitcnt lgkmcnt(4)\n" \
"s_add_u32 m0, s68, 14336\n" \
"s_mov_b32 s67, 262336\n" \
"buffer_load_dwordx4 v11, s[48:51], s67 offen lds\n" \
"s_add_u32 m0, s68, 15360\n" \
"s_mov_b32 s67, 278720\n" \
"buffer_load_dwordx4 v11, s[48:51], s67 offen lds\n" \
"s_waitcnt vmcnt(24)\n" \
"ds_read_b64_tr_b16 v[188:189], v4 offset:18432\n" \
"ds_read_b64_tr_b16 v[190:191], v4 offset:18688\n" \
"ds_read_b64_tr_b16 v[232:233], v4 offset:18440\n" \
"ds_read_b64_tr_b16 v[234:235], v4 offset:18696\n" \
"s_waitcnt lgkmcnt(4)\n" \
"s_add_u32 m0, s68, 16384\n" \
"s_mov_b32 s67, 294912\n" \
"buffer_load_dwordx4 v11, s[48:51], s67 offen lds\n" \
"s_add_u32 m0, s68, 17408\n" \
"s_mov_b32 s67, 311296\n" \
"buffer_load_dwordx4 v11, s[48:51], s67 offen lds\n" \
"s_waitcnt vmcnt(24)\n" \
"ds_read_b64_tr_b16 a[24:25], v4 offset:20480\n" \
"ds_read_b64_tr_b16 a[26:27], v4 offset:20736\n" \
"ds_read_b64_tr_b16 a[68:69], v4 offset:20488\n" \
"ds_read_b64_tr_b16 a[70:71], v4 offset:20744\n" \
"s_waitcnt lgkmcnt(4)\n" \
"s_add_u32 m0, s68, 18432\n" \
"s_mov_b32 s67, 294976\n" \
"buffer_load_dwordx4 v11, s[48:51], s67 offen lds\n" \
"s_add_u32 m0, s68, 19456\n" \
"s_mov_b32 s67, 311360\n" \
"buffer_load_dwordx4 v11, s[48:51], s67 offen lds\n" \
"s_waitcnt vmcnt(24)\n" \
"ds_read_b64_tr_b16 a[112:113], v4 offset:22528\n" \
"ds_read_b64_tr_b16 a[114:115], v4 offset:22784\n" \
"ds_read_b64_tr_b16 a[156:157], v4 offset:22536\n" \
"ds_read_b64_tr_b16 a[158:159], v4 offset:22792\n" \
"s_waitcnt lgkmcnt(4)\n" \
"s_add_u32 m0, s68, 20480\n" \
"s_mov_b32 s67, 295040\n" \
"buffer_load_dwordx4 v11, s[48:51], s67 offen lds\n" \
"s_add_u32 m0, s68, 21504\n" \
"s_mov_b32 s67, 311424\n" \
"buffer_load_dwordx4 v11, s[48:51], s67 offen lds\n" \
"s_waitcnt vmcnt(24)\n" \
"ds_read_b64_tr_b16 a[200:201], v4 offset:24576\n" \
"ds_read_b64_tr_b16 a[202:203], v4 offset:24832\n" \
"ds_read_b64_tr_b16 a[244:245], v4 offset:24584\n" \
"ds_read_b64_tr_b16 a[246:247], v4 offset:24840\n" \
"s_waitcnt lgkmcnt(4)\n" \
"s_add_u32 m0, s68, 22528\n" \
"s_mov_b32 s67, 295104\n" \
"buffer_load_dwordx4 v11, s[48:51], s67 offen lds\n" \
"s_add_u32 m0, s68, 23552\n" \
"s_mov_b32 s67, 311488\n" \
"buffer_load_dwordx4 v11, s[48:51], s67 offen lds\n" \
"s_waitcnt vmcnt(24)\n" \
"ds_read_b64_tr_b16 v[192:193], v4 offset:26624\n" \
"ds_read_b64_tr_b16 v[194:195], v4 offset:26880\n" \
"ds_read_b64_tr_b16 v[236:237], v4 offset:26632\n" \
"ds_read_b64_tr_b16 v[238:239], v4 offset:26888\n" \
"s_waitcnt lgkmcnt(4)\n" \
"s_add_u32 m0, s68, 24576\n" \
"s_mov_b32 s67, 327680\n" \
"buffer_load_dwordx4 v11, s[48:51], s67 offen lds\n" \
"s_add_u32 m0, s68, 25600\n" \
"s_mov_b32 s67, 344064\n" \
"buffer_load_dwordx4 v11, s[48:51], s67 offen lds\n" \
"s_waitcnt vmcnt(24)\n" \
"ds_read_b64_tr_b16 a[28:29], v4 offset:0\n" \
"ds_read_b64_tr_b16 a[30:31], v4 offset:256\n" \
"ds_read_b64_tr_b16 a[72:73], v4 offset:8\n" \
"ds_read_b64_tr_b16 a[74:75], v4 offset:264\n" \
"s_waitcnt lgkmcnt(4)\n" \
"s_add_u32 m0, s68, 26624\n" \
"s_mov_b32 s67, 327744\n" \
"buffer_load_dwordx4 v11, s[48:51], s67 offen lds\n" \
"s_add_u32 m0, s68, 27648\n" \
"s_mov_b32 s67, 344128\n" \
"buffer_load_dwordx4 v11, s[48:51], s67 offen lds\n" \
"s_waitcnt vmcnt(24)\n" \
"ds_read_b64_tr_b16 a[116:117], v4 offset:2048\n" \
"ds_read_b64_tr_b16 a[118:119], v4 offset:2304\n" \
"ds_read_b64_tr_b16 a[160:161], v4 offset:2056\n" \
"ds_read_b64_tr_b16 a[162:163], v4 offset:2312\n" \
"s_waitcnt lgkmcnt(4)\n" \
"s_add_u32 m0, s68, 0\n" \
"s_mov_b32 s67, 327808\n" \
"buffer_load_dwordx4 v11, s[48:51], s67 offen lds\n" \
"s_add_u32 m0, s68, 1024\n" \
"s_mov_b32 s67, 344192\n" \
"buffer_load_dwordx4 v11, s[48:51], s67 offen lds\n" \
"s_waitcnt vmcnt(24)\n" \
"ds_read_b64_tr_b16 a[204:205], v4 offset:4096\n" \
"ds_read_b64_tr_b16 a[206:207], v4 offset:4352\n" \
"ds_read_b64_tr_b16 a[248:249], v4 offset:4104\n" \
"ds_read_b64_tr_b16 a[250:251], v4 offset:4360\n" \
"s_waitcnt lgkmcnt(4)\n" \
"s_add_u32 m0, s68, 2048\n" \
"s_mov_b32 s67, 327872\n" \
"buffer_load_dwordx4 v11, s[48:51], s67 offen lds\n" \
"s_add_u32 m0, s68, 3072\n" \
"s_mov_b32 s67, 344256\n" \
"buffer_load_dwordx4 v11, s[48:51], s67 offen lds\n" \
"s_waitcnt vmcnt(24)\n" \
"ds_read_b64_tr_b16 v[196:197], v4 offset:6144\n" \
"ds_read_b64_tr_b16 v[198:199], v4 offset:6400\n" \
"ds_read_b64_tr_b16 v[240:241], v4 offset:6152\n" \
"ds_read_b64_tr_b16 v[242:243], v4 offset:6408\n" \
"s_waitcnt vmcnt(22)\n" \
"ds_read_b64_tr_b16 a[32:33], v4 offset:8192\n" \
"ds_read_b64_tr_b16 a[34:35], v4 offset:8448\n" \
"ds_read_b64_tr_b16 a[76:77], v4 offset:8200\n" \
"ds_read_b64_tr_b16 a[78:79], v4 offset:8456\n" \
"s_waitcnt vmcnt(20)\n" \
"ds_read_b64_tr_b16 a[120:121], v4 offset:10240\n" \
"ds_read_b64_tr_b16 a[122:123], v4 offset:10496\n" \
"ds_read_b64_tr_b16 a[164:165], v4 offset:10248\n" \
"ds_read_b64_tr_b16 a[166:167], v4 offset:10504\n" \
"s_waitcnt vmcnt(18)\n" \
"ds_read_b64_tr_b16 a[208:209], v4 offset:12288\n" \
"ds_read_b64_tr_b16 a[210:211], v4 offset:12544\n" \
"ds_read_b64_tr_b16 a[252:253], v4 offset:12296\n" \
"ds_read_b64_tr_b16 a[254:255], v4 offset:12552\n" \
"s_waitcnt vmcnt(16)\n" \
"ds_read_b64_tr_b16 v[200:201], v4 offset:14336\n" \
"ds_read_b64_tr_b16 v[202:203], v4 offset:14592\n" \
"ds_read_b64_tr_b16 v[244:245], v4 offset:14344\n" \
"ds_read_b64_tr_b16 v[246:247], v4 offset:14600\n" \
"s_waitcnt vmcnt(14)\n" \
"ds_read_b64_tr_b16 a[36:37], v4 offset:16384\n" \
"ds_read_b64_tr_b16 a[38:39], v4 offset:16640\n" \
"ds_read_b64_tr_b16 a[80:81], v4 offset:16392\n" \
"ds_read_b64_tr_b16 a[82:83], v4 offset:16648\n" \
"s_waitcnt vmcnt(12)\n" \
"ds_read_b64_tr_b16 a[124:125], v4 offset:18432\n" \
"ds_read_b64_tr_b16 a[126:127], v4 offset:18688\n" \
"ds_read_b64_tr_b16 a[168:169], v4 offset:18440\n" \
"ds_read_b64_tr_b16 a[170:171], v4 offset:18696\n" \
"s_waitcnt vmcnt(10)\n" \
"ds_read_b64_tr_b16 a[212:213], v4 offset:20480\n" \
"ds_read_b64_tr_b16 a[214:215], v4 offset:20736\n" \
"ds_read_b64_tr_b16 v[160:161], v4 offset:20488\n" \
"ds_read_b64_tr_b16 v[162:163], v4 offset:20744\n" \
"s_waitcnt vmcnt(8)\n" \
"ds_read_b64_tr_b16 v[204:205], v4 offset:22528\n" \
"ds_read_b64_tr_b16 v[206:207], v4 offset:22784\n" \
"ds_read_b64_tr_b16 v[248:249], v4 offset:22536\n" \
"ds_read_b64_tr_b16 v[250:251], v4 offset:22792\n" \
"s_waitcnt vmcnt(6)\n" \
"ds_read_b64_tr_b16 a[40:41], v4 offset:24576\n" \
"ds_read_b64_tr_b16 a[42:43], v4 offset:24832\n" \
"ds_read_b64_tr_b16 a[84:85], v4 offset:24584\n" \
"ds_read_b64_tr_b16 a[86:87], v4 offset:24840\n" \
"s_waitcnt vmcnt(4)\n" \
"ds_read_b64_tr_b16 a[128:129], v4 offset:26624\n" \
"ds_read_b64_tr_b16 a[130:131], v4 offset:26880\n" \
"ds_read_b64_tr_b16 a[172:173], v4 offset:26632\n" \
"ds_read_b64_tr_b16 a[174:175], v4 offset:26888\n" \
"s_waitcnt vmcnt(2)\n" \
"ds_read_b64_tr_b16 a[216:217], v4 offset:0\n" \
"ds_read_b64_tr_b16 a[218:219], v4 offset:256\n" \
"ds_read_b64_tr_b16 v[164:165], v4 offset:8\n" \
"ds_read_b64_tr_b16 v[166:167], v4 offset:264\n" \
"s_waitcnt vmcnt(0)\n" \
"ds_read_b64_tr_b16 v[208:209], v4 offset:2048\n" \
"ds_read_b64_tr_b16 v[210:211], v4 offset:2304\n" \
"ds_read_b64_tr_b16 v[252:253], v4 offset:2056\n" \
"ds_read_b64_tr_b16 v[254:255], v4 offset:2312\n" \
"s_waitcnt lgkmcnt(0)\n" \
"v_lshlrev_b32 v144, 16, v112\n" \
"v_and_b32 v145, 0xffff0000, v112\n" \
"v_lshlrev_b32 v146, 16, v113\n" \
"v_and_b32 v147, 0xffff0000, v113\n" \
"v_lshlrev_b32 v148, 16, v114\n" \
"v_and_b32 v149, 0xffff0000, v114\n" \
"v_lshlrev_b32 v150, 16, v115\n" \
"v_and_b32 v151, 0xffff0000, v115\n" \
"s_lshl_b32 s67, s65, 7\n" \
"s_add_u32 s75, s67, 8192\n" \
"s_cmp_lg_u32 s64, 0\n" \
"s_cbranch_scc1 W_p1x%=\n" \
"s_mov_b32 s77, 0\n" \
"W_p1%=:\n" \
"buffer_load_dword v24, v19, s[40:43], s75 offen sc1\n" \
"s_waitcnt vmcnt(0)\n" \
"v_readfirstlane_b32 s67, v24\n" \
"s_nop 0\n" \
"s_cmp_ge_u32 s67, 4\n" \
"s_cbranch_scc1 W_p1x%=\n" \
"s_sleep 4\n" \
"s_add_u32 s77, s77, 1\n" \
"s_cmp_gt_u32 s77, 65536\n" \
"s_cselect_b32 s76, 1, s76\n" \
"s_cmp_eq_u32 s76, 0\n" \
"s_cbranch_scc1 W_p1%=\n" \
"W_p1x%=:\n" \
"s_barrier \n" \
"s_mul_i32 s71, s65, 352256\n" \
"s_lshl_b32 s72, s65, 17\n" \
"s_mov_b32 s66, 0\n" \
"buffer_load_dwordx4 v[24:27], v10, s[40:43], s71 offen offset:0 sc1\n" \
"buffer_load_dwordx4 v[28:31], v9, s[40:43], s71 offen offset:1024 sc1\n" \
"buffer_load_dwordx4 v[32:35], v9, s[40:43], s71 offen offset:2048 sc1\n" \
"buffer_load_dwordx4 v[36:39], v9, s[40:43], s71 offen offset:3072 sc1\n" \
"s_add_u32 s67, s71, 4096\n" \
"buffer_load_dwordx4 v[40:43], v9, s[40:43], s67 offen offset:0 sc1\n" \
"buffer_load_dwordx4 v[44:47], v9, s[40:43], s67 offen offset:1024 sc1\n" \
"buffer_load_dwordx4 v[48:51], v9, s[40:43], s67 offen offset:2048 sc1\n" \
"buffer_load_dwordx4 v[52:55], v9, s[40:43], s67 offen offset:3072 sc1\n" \
"s_add_u32 s67, s71, 8192\n" \
"buffer_load_dwordx4 v[56:59], v9, s[40:43], s67 offen offset:0 sc1\n" \
"buffer_load_dwordx4 v[60:63], v9, s[40:43], s67 offen offset:1024 sc1\n" \
"buffer_load_dwordx4 v[64:67], v9, s[40:43], s67 offen offset:2048 sc1\n" \
"s_add_u32 s71, s71, 44032\n" \
"W_f2%=:\n" \
"buffer_load_dwordx4 v[68:71], v10, s[40:43], s71 offen offset:0 sc1\n" \
"buffer_load_dwordx4 v[72:75], v9, s[40:43], s71 offen offset:1024 sc1\n" \
"buffer_load_dwordx4 v[76:79], v9, s[40:43], s71 offen offset:2048 sc1\n" \
"buffer_load_dwordx4 v[80:83], v9, s[40:43], s71 offen offset:3072 sc1\n" \
"s_add_u32 s67, s71, 4096\n" \
"buffer_load_dwordx4 v[84:87], v9, s[40:43], s67 offen offset:0 sc1\n" \
"buffer_load_dwordx4 v[88:91], v9, s[40:43], s67 offen offset:1024 sc1\n" \
"buffer_load_dwordx4 v[92:95], v9, s[40:43], s67 offen offset:2048 sc1\n" \
"buffer_load_dwordx4 v[96:99], v9, s[40:43], s67 offen offset:3072 sc1\n" \
"s_add_u32 s67, s71, 8192\n" \
"buffer_load_dwordx4 v[100:103], v9, s[40:43], s67 offen offset:0 sc1\n" \
"buffer_load_dwordx4 v[104:107], v9, s[40:43], s67 offen offset:1024 sc1\n" \
"buffer_load_dwordx4 v[108:111], v9, s[40:43], s67 offen offset:2048 sc1\n" \
"s_add_u32 s71, s71, 44032\n" \
"s_waitcnt vmcnt(21)\n" \
"v_mfma_f32_16x16x32_bf16 v[112:115], a[0:3], v[24:27], 0\n" \
"v_mfma_f32_16x16x32_bf16 v[116:119], a[44:47], v[24:27], 0\n" \
"v_mfma_f32_16x16x32_bf16 v[120:123], a[88:91], v[24:27], 0\n" \
"v_mfma_f32_16x16x32_bf16 v[124:127], a[132:135], v[24:27], 0\n" \
"v_mfma_f32_16x16x32_bf16 v[128:131], a[176:179], v[24:27], 0\n" \
"v_mfma_f32_16x16x32_bf16 v[132:135], a[220:223], v[24:27], 0\n" \
"v_mfma_f32_16x16x32_bf16 v[136:139], v[168:171], v[24:27], 0\n" \
"v_mfma_f32_16x16x32_bf16 v[140:143], v[212:215], v[24:27], 0\n" \
"s_waitcnt vmcnt(20)\n" \
"v_mfma_f32_16x16x32_bf16 v[112:115], a[4:7], v[28:31], v[112:115]\n" \
"v_mfma_f32_16x16x32_bf16 v[116:119], a[48:51], v[28:31], v[116:119]\n" \
"v_mfma_f32_16x16x32_bf16 v[120:123], a[92:95], v[28:31], v[120:123]\n" \
"v_mfma_f32_16x16x32_bf16 v[124:127], a[136:139], v[28:31], v[124:127]\n" \
"v_mfma_f32_16x16x32_bf16 v[128:131], a[180:183], v[28:31], v[128:131]\n" \
"v_mfma_f32_16x16x32_bf16 v[132:135], a[224:227], v[28:31], v[132:135]\n" \
"v_mfma_f32_16x16x32_bf16 v[136:139], v[172:175], v[28:31], v[136:139]\n" \
"v_mfma_f32_16x16x32_bf16 v[140:143], v[216:219], v[28:31], v[140:143]\n" \
"s_waitcnt vmcnt(19)\n" \
"v_mfma_f32_16x16x32_bf16 v[112:115], a[8:11], v[32:35], v[112:115]\n" \
"v_mfma_f32_16x16x32_bf16 v[116:119], a[52:55], v[32:35], v[116:119]\n" \
"v_mfma_f32_16x16x32_bf16 v[120:123], a[96:99], v[32:35], v[120:123]\n" \
"v_mfma_f32_16x16x32_bf16 v[124:127], a[140:143], v[32:35], v[124:127]\n" \
"v_mfma_f32_16x16x32_bf16 v[128:131], a[184:187], v[32:35], v[128:131]\n" \
"v_mfma_f32_16x16x32_bf16 v[132:135], a[228:231], v[32:35], v[132:135]\n" \
"v_mfma_f32_16x16x32_bf16 v[136:139], v[176:179], v[32:35], v[136:139]\n" \
"v_mfma_f32_16x16x32_bf16 v[140:143], v[220:223], v[32:35], v[140:143]\n" \
"s_waitcnt vmcnt(18)\n" \
"v_mfma_f32_16x16x32_bf16 v[112:115], a[12:15], v[36:39], v[112:115]\n" \
"v_mfma_f32_16x16x32_bf16 v[116:119], a[56:59], v[36:39], v[116:119]\n" \
"v_mfma_f32_16x16x32_bf16 v[120:123], a[100:103], v[36:39], v[120:123]\n" \
"v_mfma_f32_16x16x32_bf16 v[124:127], a[144:147], v[36:39], v[124:127]\n" \
"v_mfma_f32_16x16x32_bf16 v[128:131], a[188:191], v[36:39], v[128:131]\n" \
"v_mfma_f32_16x16x32_bf16 v[132:135], a[232:235], v[36:39], v[132:135]\n" \
"v_mfma_f32_16x16x32_bf16 v[136:139], v[180:183], v[36:39], v[136:139]\n" \
"v_mfma_f32_16x16x32_bf16 v[140:143], v[224:227], v[36:39], v[140:143]\n" \
"s_waitcnt vmcnt(17)\n" \
"v_mfma_f32_16x16x32_bf16 v[112:115], a[16:19], v[40:43], v[112:115]\n" \
"v_mfma_f32_16x16x32_bf16 v[116:119], a[60:63], v[40:43], v[116:119]\n" \
"v_mfma_f32_16x16x32_bf16 v[120:123], a[104:107], v[40:43], v[120:123]\n" \
"v_mfma_f32_16x16x32_bf16 v[124:127], a[148:151], v[40:43], v[124:127]\n" \
"v_mfma_f32_16x16x32_bf16 v[128:131], a[192:195], v[40:43], v[128:131]\n" \
"v_mfma_f32_16x16x32_bf16 v[132:135], a[236:239], v[40:43], v[132:135]\n" \
"v_mfma_f32_16x16x32_bf16 v[136:139], v[184:187], v[40:43], v[136:139]\n" \
"v_mfma_f32_16x16x32_bf16 v[140:143], v[228:231], v[40:43], v[140:143]\n" \
"s_waitcnt vmcnt(16)\n" \
"v_mfma_f32_16x16x32_bf16 v[112:115], a[20:23], v[44:47], v[112:115]\n" \
"v_mfma_f32_16x16x32_bf16 v[116:119], a[64:67], v[44:47], v[116:119]\n" \
"v_mfma_f32_16x16x32_bf16 v[120:123], a[108:111], v[44:47], v[120:123]\n" \
"v_mfma_f32_16x16x32_bf16 v[124:127], a[152:155], v[44:47], v[124:127]\n" \
"v_mfma_f32_16x16x32_bf16 v[128:131], a[196:199], v[44:47], v[128:131]\n" \
"v_mfma_f32_16x16x32_bf16 v[132:135], a[240:243], v[44:47], v[132:135]\n" \
"v_mfma_f32_16x16x32_bf16 v[136:139], v[188:191], v[44:47], v[136:139]\n" \
"v_mfma_f32_16x16x32_bf16 v[140:143], v[232:235], v[44:47], v[140:143]\n" \
"s_waitcnt vmcnt(15)\n" \
"v_mfma_f32_16x16x32_bf16 v[112:115], a[24:27], v[48:51], v[112:115]\n" \
"v_mfma_f32_16x16x32_bf16 v[116:119], a[68:71], v[48:51], v[116:119]\n" \
"v_mfma_f32_16x16x32_bf16 v[120:123], a[112:115], v[48:51], v[120:123]\n" \
"v_mfma_f32_16x16x32_bf16 v[124:127], a[156:159], v[48:51], v[124:127]\n" \
"v_mfma_f32_16x16x32_bf16 v[128:131], a[200:203], v[48:51], v[128:131]\n" \
"v_mfma_f32_16x16x32_bf16 v[132:135], a[244:247], v[48:51], v[132:135]\n" \
"v_mfma_f32_16x16x32_bf16 v[136:139], v[192:195], v[48:51], v[136:139]\n" \
"v_mfma_f32_16x16x32_bf16 v[140:143], v[236:239], v[48:51], v[140:143]\n" \
"s_waitcnt vmcnt(14)\n" \
"v_mfma_f32_16x16x32_bf16 v[112:115], a[28:31], v[52:55], v[112:115]\n" \
"v_mfma_f32_16x16x32_bf16 v[116:119], a[72:75], v[52:55], v[116:119]\n" \
"v_mfma_f32_16x16x32_bf16 v[120:123], a[116:119], v[52:55], v[120:123]\n" \
"v_mfma_f32_16x16x32_bf16 v[124:127], a[160:163], v[52:55], v[124:127]\n" \
"v_mfma_f32_16x16x32_bf16 v[128:131], a[204:207], v[52:55], v[128:131]\n" \
"v_mfma_f32_16x16x32_bf16 v[132:135], a[248:251], v[52:55], v[132:135]\n" \
"v_mfma_f32_16x16x32_bf16 v[136:139], v[196:199], v[52:55], v[136:139]\n" \
"v_mfma_f32_16x16x32_bf16 v[140:143], v[240:243], v[52:55], v[140:143]\n" \
"s_waitcnt vmcnt(13)\n" \
"v_mfma_f32_16x16x32_bf16 v[112:115], a[32:35], v[56:59], v[112:115]\n" \
"v_mfma_f32_16x16x32_bf16 v[116:119], a[76:79], v[56:59], v[116:119]\n" \
"v_mfma_f32_16x16x32_bf16 v[120:123], a[120:123], v[56:59], v[120:123]\n" \
"v_mfma_f32_16x16x32_bf16 v[124:127], a[164:167], v[56:59], v[124:127]\n" \
"v_mfma_f32_16x16x32_bf16 v[128:131], a[208:211], v[56:59], v[128:131]\n" \
"v_mfma_f32_16x16x32_bf16 v[132:135], a[252:255], v[56:59], v[132:135]\n" \
"v_mfma_f32_16x16x32_bf16 v[136:139], v[200:203], v[56:59], v[136:139]\n" \
"v_mfma_f32_16x16x32_bf16 v[140:143], v[244:247], v[56:59], v[140:143]\n" \
"s_waitcnt vmcnt(12)\n" \
"v_mfma_f32_16x16x32_bf16 v[112:115], a[36:39], v[60:63], v[112:115]\n" \
"v_mfma_f32_16x16x32_bf16 v[116:119], a[80:83], v[60:63], v[116:119]\n" \
"v_mfma_f32_16x16x32_bf16 v[120:123], a[124:127], v[60:63], v[120:123]\n" \
"v_mfma_f32_16x16x32_bf16 v[124:127], a[168:171], v[60:63], v[124:127]\n" \
"v_mfma_f32_16x16x32_bf16 v[128:131], a[212:215], v[60:63], v[128:131]\n" \
"v_mfma_f32_16x16x32_bf16 v[132:135], v[160:163], v[60:63], v[132:135]\n" \
"v_mfma_f32_16x16x32_bf16 v[136:139], v[204:207], v[60:63], v[136:139]\n" \
"v_mfma_f32_16x16x32_bf16 v[140:143], v[248:251], v[60:63], v[140:143]\n" \
"s_waitcnt vmcnt(11)\n" \
"v_mfma_f32_16x16x32_bf16 v[112:115], a[40:43], v[64:67], v[112:115]\n" \
"v_mfma_f32_16x16x32_bf16 v[116:119], a[84:87], v[64:67], v[116:119]\n" \
"v_mfma_f32_16x16x32_bf16 v[120:123], a[128:131], v[64:67], v[120:123]\n" \
"v_mfma_f32_16x16x32_bf16 v[124:127], a[172:175], v[64:67], v[124:127]\n" \
"v_mfma_f32_16x16x32_bf16 v[128:131], a[216:219], v[64:67], v[128:131]\n" \
"v_mfma_f32_16x16x32_bf16 v[132:135], v[164:167], v[64:67], v[132:135]\n" \
"v_mfma_f32_16x16x32_bf16 v[136:139], v[208:211], v[64:67], v[136:139]\n" \
"v_mfma_f32_16x16x32_bf16 v[140:143], v[252:255], v[64:67], v[140:143]\n" \
"ds_write_b128 v22, v[112:115] offset:0\n" \
"ds_write_b128 v22, v[116:119] offset:1024\n" \
"ds_write_b128 v22, v[120:123] offset:2048\n" \
"ds_write_b128 v22, v[124:127] offset:3072\n" \
"ds_write_b128 v22, v[128:131] offset:4096\n" \
"ds_write_b128 v22, v[132:135] offset:5120\n" \
"ds_write_b128 v22, v[136:139] offset:6144\n" \
"s_nop 0\n" \
"ds_write_b128 v22, v[140:143] offset:7168\n" \
"s_waitcnt lgkmcnt(0)\n" \
"s_barrier \n" \
"ds_read_b128 v[112:115], v23 offset:0\n" \
"ds_read_b128 v[116:119], v23 offset:1024\n" \
"ds_read_b128 v[120:123], v23 offset:8192\n" \
"ds_read_b128 v[124:127], v23 offset:9216\n" \
"ds_read_b128 v[128:131], v23 offset:16384\n" \
"ds_read_b128 v[132:135], v23 offset:17408\n" \
"ds_read_b128 v[136:139], v23 offset:24576\n" \
"ds_read_b128 v[140:143], v23 offset:25600\n" \
"s_waitcnt lgkmcnt(7)\n" \
"v_add_f32 v112, v112, v144\n" \
"v_add_f32 v113, v113, v145\n" \
"v_add_f32 v114, v114, v146\n" \
"v_add_f32 v115, v115, v147\n" \
"s_waitcnt lgkmcnt(6)\n" \
"v_add_f32 v116, v116, v148\n" \
"v_add_f32 v117, v117, v149\n" \
"v_add_f32 v118, v118, v150\n" \
"v_add_f32 v119, v119, v151\n" \
"s_waitcnt lgkmcnt(5)\n" \
"v_add_f32 v112, v112, v120\n" \
"v_add_f32 v113, v113, v121\n" \
"v_add_f32 v114, v114, v122\n" \
"v_add_f32 v115, v115, v123\n" \
"s_waitcnt lgkmcnt(4)\n" \
"v_add_f32 v116, v116, v124\n" \
"v_add_f32 v117, v117, v125\n" \
"v_add_f32 v118, v118, v126\n" \
"v_add_f32 v119, v119, v127\n" \
"s_waitcnt lgkmcnt(3)\n" \
"v_add_f32 v112, v112, v128\n" \
"v_add_f32 v113, v113, v129\n" \
"v_add_f32 v114, v114, v130\n" \
"v_add_f32 v115, v115, v131\n" \
"s_waitcnt lgkmcnt(2)\n" \
"v_add_f32 v116, v116, v132\n" \
"v_add_f32 v117, v117, v133\n" \
"v_add_f32 v118, v118, v134\n" \
"v_add_f32 v119, v119, v135\n" \
"s_waitcnt lgkmcnt(1)\n" \
"v_add_f32 v112, v112, v136\n" \
"v_add_f32 v113, v113, v137\n" \
"v_add_f32 v114, v114, v138\n" \
"v_add_f32 v115, v115, v139\n" \
"s_waitcnt lgkmcnt(0)\n" \
"v_add_f32 v116, v116, v140\n" \
"v_add_f32 v117, v117, v141\n" \
"v_add_f32 v118, v118, v142\n" \
"v_add_f32 v119, v119, v143\n" \
"v_mul_f32 v120, s78, v112\n" \
"v_mul_f32 v121, s78, v113\n" \
"v_mul_f32 v122, s78, v114\n" \
"v_mul_f32 v123, s78, v115\n" \
"v_mul_f32 v124, s78, v116\n" \
"v_mul_f32 v125, s78, v117\n" \
"v_mul_f32 v126, s78, v118\n" \
"v_mul_f32 v127, s78, v119\n" \
"v_exp_f32 v120, v120\n" \
"v_exp_f32 v121, v121\n" \
"v_exp_f32 v122, v122\n" \
"v_exp_f32 v123, v123\n" \
"v_exp_f32 v124, v124\n" \
"v_exp_f32 v125, v125\n" \
"v_exp_f32 v126, v126\n" \
"v_exp_f32 v127, v127\n" \
"v_add_f32 v120, 1.0, v120\n" \
"v_add_f32 v121, 1.0, v121\n" \
"v_add_f32 v122, 1.0, v122\n" \
"v_add_f32 v123, 1.0, v123\n" \
"v_add_f32 v124, 1.0, v124\n" \
"v_add_f32 v125, 1.0, v125\n" \
"v_add_f32 v126, 1.0, v126\n" \
"v_add_f32 v127, 1.0, v127\n" \
"v_rcp_f32 v120, v120\n" \
"v_rcp_f32 v121, v121\n" \
"v_rcp_f32 v122, v122\n" \
"v_rcp_f32 v123, v123\n" \
"v_rcp_f32 v124, v124\n" \
"v_rcp_f32 v125, v125\n" \
"v_rcp_f32 v126, v126\n" \
"v_rcp_f32 v127, v127\n" \
"v_mul_f32 v112, v112, v120\n" \
"v_mul_f32 v113, v113, v121\n" \
"v_mul_f32 v114, v114, v122\n" \
"v_mul_f32 v115, v115, v123\n" \
"v_mul_f32 v116, v116, v124\n" \
"v_mul_f32 v117, v117, v125\n" \
"v_mul_f32 v118, v118, v126\n" \
"v_mul_f32 v119, v119, v127\n" \
"v_cvt_pk_bf16_f32 v152, v112, v113\n" \
"v_cvt_pk_bf16_f32 v153, v114, v115\n" \
"v_cvt_pk_bf16_f32 v154, v116, v117\n" \
"v_cvt_pk_bf16_f32 v155, v118, v119\n" \
"buffer_store_dwordx4 v[152:155], v13, s[40:43], s72 offen sc1\n" \
"s_add_u32 s72, s72, 16384\n" \
"buffer_load_dwordx4 v[24:27], v10, s[40:43], s71 offen offset:0 sc1\n" \
"buffer_load_dwordx4 v[28:31], v9, s[40:43], s71 offen offset:1024 sc1\n" \
"buffer_load_dwordx4 v[32:35], v9, s[40:43], s71 offen offset:2048 sc1\n" \
"buffer_load_dwordx4 v[36:39], v9, s[40:43], s71 offen offset:3072 sc1\n" \
"s_add_u32 s67, s71, 4096\n" \
"buffer_load_dwordx4 v[40:43], v9, s[40:43], s67 offen offset:0 sc1\n" \
"buffer_load_dwordx4 v[44:47], v9, s[40:43], s67 offen offset:1024 sc1\n" \
"buffer_load_dwordx4 v[48:51], v9, s[40:43], s67 offen offset:2048 sc1\n" \
"buffer_load_dwordx4 v[52:55], v9, s[40:43], s67 offen offset:3072 sc1\n" \
"s_add_u32 s67, s71, 8192\n" \
"buffer_load_dwordx4 v[56:59], v9, s[40:43], s67 offen offset:0 sc1\n" \
"buffer_load_dwordx4 v[60:63], v9, s[40:43], s67 offen offset:1024 sc1\n" \
"buffer_load_dwordx4 v[64:67], v9, s[40:43], s67 offen offset:2048 sc1\n" \
"s_add_u32 s71, s71, 44032\n" \
"s_waitcnt vmcnt(22)\n" \
"v_mfma_f32_16x16x32_bf16 v[112:115], a[0:3], v[68:71], 0\n" \
"v_mfma_f32_16x16x32_bf16 v[116:119], a[44:47], v[68:71], 0\n" \
"v_mfma_f32_16x16x32_bf16 v[120:123], a[88:91], v[68:71], 0\n" \
"v_mfma_f32_16x16x32_bf16 v[124:127], a[132:135], v[68:71], 0\n" \
"v_mfma_f32_16x16x32_bf16 v[128:131], a[176:179], v[68:71], 0\n" \
"v_mfma_f32_16x16x32_bf16 v[132:135], a[220:223], v[68:71], 0\n" \
"v_mfma_f32_16x16x32_bf16 v[136:139], v[168:171], v[68:71], 0\n" \
"v_mfma_f32_16x16x32_bf16 v[140:143], v[212:215], v[68:71], 0\n" \
"s_waitcnt vmcnt(21)\n" \
"v_mfma_f32_16x16x32_bf16 v[112:115], a[4:7], v[72:75], v[112:115]\n" \
"v_mfma_f32_16x16x32_bf16 v[116:119], a[48:51], v[72:75], v[116:119]\n" \
"v_mfma_f32_16x16x32_bf16 v[120:123], a[92:95], v[72:75], v[120:123]\n" \
"v_mfma_f32_16x16x32_bf16 v[124:127], a[136:139], v[72:75], v[124:127]\n" \
"v_mfma_f32_16x16x32_bf16 v[128:131], a[180:183], v[72:75], v[128:131]\n" \
"v_mfma_f32_16x16x32_bf16 v[132:135], a[224:227], v[72:75], v[132:135]\n" \
"v_mfma_f32_16x16x32_bf16 v[136:139], v[172:175], v[72:75], v[136:139]\n" \
"v_mfma_f32_16x16x32_bf16 v[140:143], v[216:219], v[72:75], v[140:143]\n" \
"s_waitcnt vmcnt(20)\n" \
"v_mfma_f32_16x16x32_bf16 v[112:115], a[8:11], v[76:79], v[112:115]\n" \
"v_mfma_f32_16x16x32_bf16 v[116:119], a[52:55], v[76:79], v[116:119]\n" \
"v_mfma_f32_16x16x32_bf16 v[120:123], a[96:99], v[76:79], v[120:123]\n" \
"v_mfma_f32_16x16x32_bf16 v[124:127], a[140:143], v[76:79], v[124:127]\n" \
"v_mfma_f32_16x16x32_bf16 v[128:131], a[184:187], v[76:79], v[128:131]\n" \
"v_mfma_f32_16x16x32_bf16 v[132:135], a[228:231], v[76:79], v[132:135]\n" \
"v_mfma_f32_16x16x32_bf16 v[136:139], v[176:179], v[76:79], v[136:139]\n" \
"v_mfma_f32_16x16x32_bf16 v[140:143], v[220:223], v[76:79], v[140:143]\n" \
"s_waitcnt vmcnt(19)\n" \
"v_mfma_f32_16x16x32_bf16 v[112:115], a[12:15], v[80:83], v[112:115]\n" \
"v_mfma_f32_16x16x32_bf16 v[116:119], a[56:59], v[80:83], v[116:119]\n" \
"v_mfma_f32_16x16x32_bf16 v[120:123], a[100:103], v[80:83], v[120:123]\n" \
"v_mfma_f32_16x16x32_bf16 v[124:127], a[144:147], v[80:83], v[124:127]\n" \
"v_mfma_f32_16x16x32_bf16 v[128:131], a[188:191], v[80:83], v[128:131]\n" \
"v_mfma_f32_16x16x32_bf16 v[132:135], a[232:235], v[80:83], v[132:135]\n" \
"v_mfma_f32_16x16x32_bf16 v[136:139], v[180:183], v[80:83], v[136:139]\n" \
"v_mfma_f32_16x16x32_bf16 v[140:143], v[224:227], v[80:83], v[140:143]\n" \
"s_waitcnt vmcnt(18)\n" \
"v_mfma_f32_16x16x32_bf16 v[112:115], a[16:19], v[84:87], v[112:115]\n" \
"v_mfma_f32_16x16x32_bf16 v[116:119], a[60:63], v[84:87], v[116:119]\n" \
"v_mfma_f32_16x16x32_bf16 v[120:123], a[104:107], v[84:87], v[120:123]\n" \
"v_mfma_f32_16x16x32_bf16 v[124:127], a[148:151], v[84:87], v[124:127]\n" \
"v_mfma_f32_16x16x32_bf16 v[128:131], a[192:195], v[84:87], v[128:131]\n" \
"v_mfma_f32_16x16x32_bf16 v[132:135], a[236:239], v[84:87], v[132:135]\n" \
"v_mfma_f32_16x16x32_bf16 v[136:139], v[184:187], v[84:87], v[136:139]\n" \
"v_mfma_f32_16x16x32_bf16 v[140:143], v[228:231], v[84:87], v[140:143]\n" \
"s_waitcnt vmcnt(17)\n" \
"v_mfma_f32_16x16x32_bf16 v[112:115], a[20:23], v[88:91], v[112:115]\n" \
"v_mfma_f32_16x16x32_bf16 v[116:119], a[64:67], v[88:91], v[116:119]\n" \
"v_mfma_f32_16x16x32_bf16 v[120:123], a[108:111], v[88:91], v[120:123]\n" \
"v_mfma_f32_16x16x32_bf16 v[124:127], a[152:155], v[88:91], v[124:127]\n" \
"v_mfma_f32_16x16x32_bf16 v[128:131], a[196:199], v[88:91], v[128:131]\n" \
"v_mfma_f32_16x16x32_bf16 v[132:135], a[240:243], v[88:91], v[132:135]\n" \
"v_mfma_f32_16x16x32_bf16 v[136:139], v[188:191], v[88:91], v[136:139]\n" \
"v_mfma_f32_16x16x32_bf16 v[140:143], v[232:235], v[88:91], v[140:143]\n" \
"s_waitcnt vmcnt(16)\n" \
"v_mfma_f32_16x16x32_bf16 v[112:115], a[24:27], v[92:95], v[112:115]\n" \
"v_mfma_f32_16x16x32_bf16 v[116:119], a[68:71], v[92:95], v[116:119]\n" \
"v_mfma_f32_16x16x32_bf16 v[120:123], a[112:115], v[92:95], v[120:123]\n" \
"v_mfma_f32_16x16x32_bf16 v[124:127], a[156:159], v[92:95], v[124:127]\n" \
"v_mfma_f32_16x16x32_bf16 v[128:131], a[200:203], v[92:95], v[128:131]\n" \
"v_mfma_f32_16x16x32_bf16 v[132:135], a[244:247], v[92:95], v[132:135]\n" \
"v_mfma_f32_16x16x32_bf16 v[136:139], v[192:195], v[92:95], v[136:139]\n" \
"v_mfma_f32_16x16x32_bf16 v[140:143], v[236:239], v[92:95], v[140:143]\n" \
"s_waitcnt vmcnt(15)\n" \
"v_mfma_f32_16x16x32_bf16 v[112:115], a[28:31], v[96:99], v[112:115]\n" \
"v_mfma_f32_16x16x32_bf16 v[116:119], a[72:75], v[96:99], v[116:119]\n" \
"v_mfma_f32_16x16x32_bf16 v[120:123], a[116:119], v[96:99], v[120:123]\n" \
"v_mfma_f32_16x16x32_bf16 v[124:127], a[160:163], v[96:99], v[124:127]\n" \
"v_mfma_f32_16x16x32_bf16 v[128:131], a[204:207], v[96:99], v[128:131]\n" \
"v_mfma_f32_16x16x32_bf16 v[132:135], a[248:251], v[96:99], v[132:135]\n" \
"v_mfma_f32_16x16x32_bf16 v[136:139], v[196:199], v[96:99], v[136:139]\n" \
"v_mfma_f32_16x16x32_bf16 v[140:143], v[240:243], v[96:99], v[140:143]\n" \
"s_waitcnt vmcnt(14)\n" \
"v_mfma_f32_16x16x32_bf16 v[112:115], a[32:35], v[100:103], v[112:115]\n" \
"v_mfma_f32_16x16x32_bf16 v[116:119], a[76:79], v[100:103], v[116:119]\n" \
"v_mfma_f32_16x16x32_bf16 v[120:123], a[120:123], v[100:103], v[120:123]\n" \
"v_mfma_f32_16x16x32_bf16 v[124:127], a[164:167], v[100:103], v[124:127]\n" \
"v_mfma_f32_16x16x32_bf16 v[128:131], a[208:211], v[100:103], v[128:131]\n" \
"v_mfma_f32_16x16x32_bf16 v[132:135], a[252:255], v[100:103], v[132:135]\n" \
"v_mfma_f32_16x16x32_bf16 v[136:139], v[200:203], v[100:103], v[136:139]\n" \
"v_mfma_f32_16x16x32_bf16 v[140:143], v[244:247], v[100:103], v[140:143]\n" \
"s_waitcnt vmcnt(13)\n" \
"v_mfma_f32_16x16x32_bf16 v[112:115], a[36:39], v[104:107], v[112:115]\n" \
"v_mfma_f32_16x16x32_bf16 v[116:119], a[80:83], v[104:107], v[116:119]\n" \
"v_mfma_f32_16x16x32_bf16 v[120:123], a[124:127], v[104:107], v[120:123]\n" \
"v_mfma_f32_16x16x32_bf16 v[124:127], a[168:171], v[104:107], v[124:127]\n" \
"v_mfma_f32_16x16x32_bf16 v[128:131], a[212:215], v[104:107], v[128:131]\n" \
"v_mfma_f32_16x16x32_bf16 v[132:135], v[160:163], v[104:107], v[132:135]\n" \
"v_mfma_f32_16x16x32_bf16 v[136:139], v[204:207], v[104:107], v[136:139]\n" \
"v_mfma_f32_16x16x32_bf16 v[140:143], v[248:251], v[104:107], v[140:143]\n" \
"s_waitcnt vmcnt(12)\n" \
"v_mfma_f32_16x16x32_bf16 v[112:115], a[40:43], v[108:111], v[112:115]\n" \
"v_mfma_f32_16x16x32_bf16 v[116:119], a[84:87], v[108:111], v[116:119]\n" \
"v_mfma_f32_16x16x32_bf16 v[120:123], a[128:131], v[108:111], v[120:123]\n" \
"v_mfma_f32_16x16x32_bf16 v[124:127], a[172:175], v[108:111], v[124:127]\n" \
"v_mfma_f32_16x16x32_bf16 v[128:131], a[216:219], v[108:111], v[128:131]\n" \
"v_mfma_f32_16x16x32_bf16 v[132:135], v[164:167], v[108:111], v[132:135]\n" \
"v_mfma_f32_16x16x32_bf16 v[136:139], v[208:211], v[108:111], v[136:139]\n" \
"v_mfma_f32_16x16x32_bf16 v[140:143], v[252:255], v[108:111], v[140:143]\n" \
"ds_write_b128 v22, v[112:115] offset:32768\n" \
"ds_write_b128 v22, v[116:119] offset:33792\n" \
"ds_write_b128 v22, v[120:123] offset:34816\n" \
"ds_write_b128 v22, v[124:127] offset:35840\n" \
"ds_write_b128 v22, v[128:131] offset:36864\n" \
"ds_write_b128 v22, v[132:135] offset:37888\n" \
"ds_write_b128 v22, v[136:139] offset:38912\n" \
"s_nop 0\n" \
"ds_write_b128 v22, v[140:143] offset:39936\n" \
"s_waitcnt lgkmcnt(0)\n" \
"s_barrier \n" \
"ds_read_b128 v[112:115], v23 offset:32768\n" \
"ds_read_b128 v[116:119], v23 offset:33792\n" \
"ds_read_b128 v[120:123], v23 offset:40960\n" \
"ds_read_b128 v[124:127], v23 offset:41984\n" \
"ds_read_b128 v[128:131], v23 offset:49152\n" \
"ds_read_b128 v[132:135], v23 offset:50176\n" \
"ds_read_b128 v[136:139], v23 offset:57344\n" \
"ds_read_b128 v[140:143], v23 offset:58368\n" \
"s_waitcnt lgkmcnt(7)\n" \
"v_add_f32 v112, v112, v144\n" \
"v_add_f32 v113, v113, v145\n" \
"v_add_f32 v114, v114, v146\n" \
"v_add_f32 v115, v115, v147\n" \
"s_waitcnt lgkmcnt(6)\n" \
"v_add_f32 v116, v116, v148\n" \
"v_add_f32 v117, v117, v149\n" \
"v_add_f32 v118, v118, v150\n" \
"v_add_f32 v119, v119, v151\n" \
"s_waitcnt lgkmcnt(5)\n" \
"v_add_f32 v112, v112, v120\n" \
"v_add_f32 v113, v113, v121\n" \
"v_add_f32 v114, v114, v122\n" \
"v_add_f32 v115, v115, v123\n" \
"s_waitcnt lgkmcnt(4)\n" \
"v_add_f32 v116, v116, v124\n" \
"v_add_f32 v117, v117, v125\n" \
"v_add_f32 v118, v118, v126\n" \
"v_add_f32 v119, v119, v127\n" \
"s_waitcnt lgkmcnt(3)\n" \
"v_add_f32 v112, v112, v128\n" \
"v_add_f32 v113, v113, v129\n" \
"v_add_f32 v114, v114, v130\n" \
"v_add_f32 v115, v115, v131\n" \
"s_waitcnt lgkmcnt(2)\n" \
"v_add_f32 v116, v116, v132\n" \
"v_add_f32 v117, v117, v133\n" \
"v_add_f32 v118, v118, v134\n" \
"v_add_f32 v119, v119, v135\n" \
"s_waitcnt lgkmcnt(1)\n" \
"v_add_f32 v112, v112, v136\n" \
"v_add_f32 v113, v113, v137\n" \
"v_add_f32 v114, v114, v138\n" \
"v_add_f32 v115, v115, v139\n" \
"s_waitcnt lgkmcnt(0)\n" \
"v_add_f32 v116, v116, v140\n" \
"v_add_f32 v117, v117, v141\n" \
"v_add_f32 v118, v118, v142\n" \
"v_add_f32 v119, v119, v143\n" \
"v_mul_f32 v120, s78, v112\n" \
"v_mul_f32 v121, s78, v113\n" \
"v_mul_f32 v122, s78, v114\n" \
"v_mul_f32 v123, s78, v115\n" \
"v_mul_f32 v124, s78, v116\n" \
"v_mul_f32 v125, s78, v117\n" \
"v_mul_f32 v126, s78, v118\n" \
"v_mul_f32 v127, s78, v119\n" \
"v_exp_f32 v120, v120\n" \
"v_exp_f32 v121, v121\n" \
"v_exp_f32 v122, v122\n" \
"v_exp_f32 v123, v123\n" \
"v_exp_f32 v124, v124\n" \
"v_exp_f32 v125, v125\n" \
"v_exp_f32 v126, v126\n" \
"v_exp_f32 v127, v127\n" \
"v_add_f32 v120, 1.0, v120\n" \
"v_add_f32 v121, 1.0, v121\n" \
"v_add_f32 v122, 1.0, v122\n" \
"v_add_f32 v123, 1.0, v123\n" \
"v_add_f32 v124, 1.0, v124\n" \
"v_add_f32 v125, 1.0, v125\n" \
"v_add_f32 v126, 1.0, v126\n" \
"v_add_f32 v127, 1.0, v127\n" \
"v_rcp_f32 v120, v120\n" \
"v_rcp_f32 v121, v121\n" \
"v_rcp_f32 v122, v122\n" \
"v_rcp_f32 v123, v123\n" \
"v_rcp_f32 v124, v124\n" \
"v_rcp_f32 v125, v125\n" \
"v_rcp_f32 v126, v126\n" \
"v_rcp_f32 v127, v127\n" \
"v_mul_f32 v112, v112, v120\n" \
"v_mul_f32 v113, v113, v121\n" \
"v_mul_f32 v114, v114, v122\n" \
"v_mul_f32 v115, v115, v123\n" \
"v_mul_f32 v116, v116, v124\n" \
"v_mul_f32 v117, v117, v125\n" \
"v_mul_f32 v118, v118, v126\n" \
"v_mul_f32 v119, v119, v127\n" \
"v_cvt_pk_bf16_f32 v152, v112, v113\n" \
"v_cvt_pk_bf16_f32 v153, v114, v115\n" \
"v_cvt_pk_bf16_f32 v154, v116, v117\n" \
"v_cvt_pk_bf16_f32 v155, v118, v119\n" \
"buffer_store_dwordx4 v[152:155], v13, s[40:43], s72 offen sc1\n" \
"s_add_u32 s72, s72, 16384\n" \
"s_waitcnt vmcnt(1)\n" \
"s_add_u32 s66, s66, 1\n" \
"s_cmp_lt_u32 s66, 4\n" \
"s_cbranch_scc1 W_f2%=\n" \
"s_waitcnt vmcnt(0)\n" \
"s_barrier \n" \
"s_cmp_lg_u32 s64, 0\n" \
"s_cbranch_scc1 W_s2%=\n" \
"s_mov_b64 exec, 1\n" \
"buffer_atomic_add v20, v18, s[40:43], 0 offen\n" \
"s_mov_b64 exec, -1\n" \
"W_s2%=:\n" \
"s_add_u32 m0, s68, 0\n" \
"s_mov_b32 s67, 0\n" \
"buffer_load_dwordx4 v14, s[52:55], s67 offen lds\n" \
"s_add_u32 m0, s68, 1024\n" \
"s_mov_b32 s67, 16384\n" \
"buffer_load_dwordx4 v14, s[52:55], s67 offen lds\n" \
"s_add_u32 m0, s68, 2048\n" \
"s_mov_b32 s67, 32768\n" \
"buffer_load_dwordx4 v14, s[52:55], s67 offen lds\n" \
"s_add_u32 m0, s68, 3072\n" \
"s_mov_b32 s67, 49152\n" \
"buffer_load_dwordx4 v14, s[52:55], s67 offen lds\n" \
"s_add_u32 m0, s68, 4096\n" \
"s_mov_b32 s67, 65536\n" \
"buffer_load_dwordx4 v14, s[52:55], s67 offen lds\n" \
"s_add_u32 m0, s68, 5120\n" \
"s_mov_b32 s67, 81920\n" \
"buffer_load_dwordx4 v14, s[52:55], s67 offen lds\n" \
"s_add_u32 m0, s68, 6144\n" \
"s_mov_b32 s67, 98304\n" \
"buffer_load_dwordx4 v14, s[52:55], s67 offen lds\n" \
"s_add_u32 m0, s68, 7168\n" \
"s_mov_b32 s67, 114688\n" \
"buffer_load_dwordx4 v14, s[52:55], s67 offen lds\n" \
"s_add_u32 m0, s68, 8192\n" \
"s_mov_b32 s67, 131072\n" \
"buffer_load_dwordx4 v14, s[52:55], s67 offen lds\n" \
"s_add_u32 m0, s68, 9216\n" \
"s_mov_b32 s67, 147456\n" \
"buffer_load_dwordx4 v14, s[52:55], s67 offen lds\n" \
"s_add_u32 m0, s68, 10240\n" \
"s_mov_b32 s67, 163840\n" \
"buffer_load_dwordx4 v14, s[52:55], s67 offen lds\n" \
"s_add_u32 m0, s68, 11264\n" \
"s_mov_b32 s67, 180224\n" \
"buffer_load_dwordx4 v14, s[52:55], s67 offen lds\n" \
"s_add_u32 m0, s68, 12288\n" \
"s_mov_b32 s67, 196608\n" \
"buffer_load_dwordx4 v14, s[52:55], s67 offen lds\n" \
"s_add_u32 m0, s68, 13312\n" \
"s_mov_b32 s67, 212992\n" \
"buffer_load_dwordx4 v14, s[52:55], s67 offen lds\n" \
"s_add_u32 m0, s68, 14336\n" \
"s_mov_b32 s67, 229376\n" \
"buffer_load_dwordx4 v14, s[52:55], s67 offen lds\n" \
"s_add_u32 m0, s68, 15360\n" \
"s_mov_b32 s67, 245760\n" \
"buffer_load_dwordx4 v14, s[52:55], s67 offen lds\n" \
"s_add_u32 m0, s68, 16384\n" \
"s_mov_b32 s67, 262144\n" \
"buffer_load_dwordx4 v14, s[52:55], s67 offen lds\n" \
"s_add_u32 m0, s68, 17408\n" \
"s_mov_b32 s67, 278528\n" \
"buffer_load_dwordx4 v14, s[52:55], s67 offen lds\n" \
"s_add_u32 m0, s68, 18432\n" \
"s_mov_b32 s67, 294912\n" \
"buffer_load_dwordx4 v14, s[52:55], s67 offen lds\n" \
"s_add_u32 m0, s68, 19456\n" \
"s_mov_b32 s67, 311296\n" \
"buffer_load_dwordx4 v14, s[52:55], s67 offen lds\n" \
"s_add_u32 m0, s68, 20480\n" \
"s_mov_b32 s67, 327680\n" \
"buffer_load_dwordx4 v14, s[52:55], s67 offen lds\n" \
"s_add_u32 m0, s68, 21504\n" \
"s_mov_b32 s67, 344064\n" \
"buffer_load_dwordx4 v14, s[52:55], s67 offen lds\n" \
"s_add_u32 m0, s68, 22528\n" \
"s_mov_b32 s67, 360448\n" \
"buffer_load_dwordx4 v14, s[52:55], s67 offen lds\n" \
"s_add_u32 m0, s68, 23552\n" \
"s_mov_b32 s67, 376832\n" \
"buffer_load_dwordx4 v14, s[52:55], s67 offen lds\n" \
"s_add_u32 m0, s68, 24576\n" \
"s_mov_b32 s67, 393216\n" \
"buffer_load_dwordx4 v14, s[52:55], s67 offen lds\n" \
"s_add_u32 m0, s68, 25600\n" \
"s_mov_b32 s67, 409600\n" \
"buffer_load_dwordx4 v14, s[52:55], s67 offen lds\n" \
"s_add_u32 m0, s68, 26624\n" \
"s_mov_b32 s67, 425984\n" \
"buffer_load_dwordx4 v14, s[52:55], s67 offen lds\n" \
"s_add_u32 m0, s68, 27648\n" \
"s_mov_b32 s67, 442368\n" \
"buffer_load_dwordx4 v14, s[52:55], s67 offen lds\n" \
"s_waitcnt vmcnt(26)\n" \
"ds_read_b64_tr_b16 a[0:1], v4 offset:0\n" \
"ds_read_b64_tr_b16 a[2:3], v4 offset:256\n" \
"ds_read_b64_tr_b16 a[128:129], v4 offset:8\n" \
"ds_read_b64_tr_b16 a[130:131], v4 offset:264\n" \
"s_waitcnt vmcnt(24)\n" \
"ds_read_b64_tr_b16 a[4:5], v4 offset:2048\n" \
"ds_read_b64_tr_b16 a[6:7], v4 offset:2304\n" \
"ds_read_b64_tr_b16 a[132:133], v4 offset:2056\n" \
"ds_read_b64_tr_b16 a[134:135], v4 offset:2312\n" \
"s_waitcnt lgkmcnt(4)\n" \
"s_add_u32 m0, s68, 0\n" \
"s_mov_b32 s67, 458752\n" \
"buffer_load_dwordx4 v14, s[52:55], s67 offen lds\n" \
"s_add_u32 m0, s68, 1024\n" \
"s_mov_b32 s67, 475136\n" \
"buffer_load_dwordx4 v14, s[52:55], s67 offen lds\n" \
"s_waitcnt vmcnt(24)\n" \
"ds_read_b64_tr_b16 a[8:9], v4 offset:4096\n" \
"ds_read_b64_tr_b16 a[10:11], v4 offset:4352\n" \
"ds_read_b64_tr_b16 a[136:137], v4 offset:4104\n" \
"ds_read_b64_tr_b16 a[138:139], v4 offset:4360\n" \
"s_waitcnt lgkmcnt(4)\n" \
"s_add_u32 m0, s68, 2048\n" \
"s_mov_b32 s67, 491520\n" \
"buffer_load_dwordx4 v14, s[52:55], s67 offen lds\n" \
"s_add_u32 m0, s68, 3072\n" \
"s_mov_b32 s67, 507904\n" \
"buffer_load_dwordx4 v14, s[52:55], s67 offen lds\n" \
"s_waitcnt vmcnt(24)\n" \
"ds_read_b64_tr_b16 a[12:13], v4 offset:6144\n" \
"ds_read_b64_tr_b16 a[14:15], v4 offset:6400\n" \
"ds_read_b64_tr_b16 a[140:141], v4 offset:6152\n" \
"ds_read_b64_tr_b16 a[142:143], v4 offset:6408\n" \
"s_waitcnt lgkmcnt(4)\n" \
"s_add_u32 m0, s68, 4096\n" \
"s_mov_b32 s67, 524288\n" \
"buffer_load_dwordx4 v14, s[52:55], s67 offen lds\n" \
"s_add_u32 m0, s68, 5120\n" \
"s_mov_b32 s67, 540672\n" \
"buffer_load_dwordx4 v14, s[52:55], s67 offen lds\n" \
"s_waitcnt vmcnt(24)\n" \
"ds_read_b64_tr_b16 a[16:17], v4 offset:8192\n" \
"ds_read_b64_tr_b16 a[18:19], v4 offset:8448\n" \
"ds_read_b64_tr_b16 a[144:145], v4 offset:8200\n" \
"ds_read_b64_tr_b16 a[146:147], v4 offset:8456\n" \
"s_waitcnt lgkmcnt(4)\n" \
"s_add_u32 m0, s68, 6144\n" \
"s_mov_b32 s67, 557056\n" \
"buffer_load_dwordx4 v14, s[52:55], s67 offen lds\n" \
"s_add_u32 m0, s68, 7168\n" \
"s_mov_b32 s67, 573440\n" \
"buffer_load_dwordx4 v14, s[52:55], s67 offen lds\n" \
"s_waitcnt vmcnt(24)\n" \
"ds_read_b64_tr_b16 a[20:21], v4 offset:10240\n" \
"ds_read_b64_tr_b16 a[22:23], v4 offset:10496\n" \
"ds_read_b64_tr_b16 a[148:149], v4 offset:10248\n" \
"ds_read_b64_tr_b16 a[150:151], v4 offset:10504\n" \
"s_waitcnt lgkmcnt(4)\n" \
"s_add_u32 m0, s68, 8192\n" \
"s_mov_b32 s67, 589824\n" \
"buffer_load_dwordx4 v14, s[52:55], s67 offen lds\n" \
"s_add_u32 m0, s68, 9216\n" \
"s_mov_b32 s67, 606208\n" \
"buffer_load_dwordx4 v14, s[52:55], s67 offen lds\n" \
"s_waitcnt vmcnt(24)\n" \
"ds_read_b64_tr_b16 a[24:25], v4 offset:12288\n" \
"ds_read_b64_tr_b16 a[26:27], v4 offset:12544\n" \
"ds_read_b64_tr_b16 a[152:153], v4 offset:12296\n" \
"ds_read_b64_tr_b16 a[154:155], v4 offset:12552\n" \
"s_waitcnt lgkmcnt(4)\n" \
"s_add_u32 m0, s68, 10240\n" \
"s_mov_b32 s67, 622592\n" \
"buffer_load_dwordx4 v14, s[52:55], s67 offen lds\n" \
"s_add_u32 m0, s68, 11264\n" \
"s_mov_b32 s67, 638976\n" \
"buffer_load_dwordx4 v14, s[52:55], s67 offen lds\n" \
"s_waitcnt vmcnt(24)\n" \
"ds_read_b64_tr_b16 a[28:29], v4 offset:14336\n" \
"ds_read_b64_tr_b16 a[30:31], v4 offset:14592\n" \
"ds_read_b64_tr_b16 a[156:157], v4 offset:14344\n" \
"ds_read_b64_tr_b16 a[158:159], v4 offset:14600\n" \
"s_waitcnt lgkmcnt(4)\n" \
"s_add_u32 m0, s68, 12288\n" \
"s_mov_b32 s67, 655360\n" \
"buffer_load_dwordx4 v14, s[52:55], s67 offen lds\n" \
"s_add_u32 m0, s68, 13312\n" \
"s_mov_b32 s67, 671744\n" \
"buffer_load_dwordx4 v14, s[52:55], s67 offen lds\n" \
"s_waitcnt vmcnt(24)\n" \
"ds_read_b64_tr_b16 a[32:33], v4 offset:16384\n" \
"ds_read_b64_tr_b16 a[34:35], v4 offset:16640\n" \
"ds_read_b64_tr_b16 a[160:161], v4 offset:16392\n" \
"ds_read_b64_tr_b16 a[162:163], v4 offset:16648\n" \
"s_waitcnt lgkmcnt(4)\n" \
"s_add_u32 m0, s68, 14336\n" \
"s_mov_b32 s67, 688128\n" \
"buffer_load_dwordx4 v14, s[52:55], s67 offen lds\n" \
"s_add_u32 m0, s68, 15360\n" \
"s_mov_b32 s67, 704512\n" \
"buffer_load_dwordx4 v14, s[52:55], s67 offen lds\n" \
"s_waitcnt vmcnt(24)\n" \
"ds_read_b64_tr_b16 a[36:37], v4 offset:18432\n" \
"ds_read_b64_tr_b16 a[38:39], v4 offset:18688\n" \
"ds_read_b64_tr_b16 a[164:165], v4 offset:18440\n" \
"ds_read_b64_tr_b16 a[166:167], v4 offset:18696\n" \
"s_waitcnt lgkmcnt(4)\n" \
"s_add_u32 m0, s68, 16384\n" \
"s_mov_b32 s67, 720896\n" \
"buffer_load_dwordx4 v14, s[52:55], s67 offen lds\n" \
"s_add_u32 m0, s68, 17408\n" \
"s_mov_b32 s67, 737280\n" \
"buffer_load_dwordx4 v14, s[52:55], s67 offen lds\n" \
"s_waitcnt vmcnt(24)\n" \
"ds_read_b64_tr_b16 a[40:41], v4 offset:20480\n" \
"ds_read_b64_tr_b16 a[42:43], v4 offset:20736\n" \
"ds_read_b64_tr_b16 a[168:169], v4 offset:20488\n" \
"ds_read_b64_tr_b16 a[170:171], v4 offset:20744\n" \
"s_waitcnt lgkmcnt(4)\n" \
"s_add_u32 m0, s68, 18432\n" \
"s_mov_b32 s67, 753664\n" \
"buffer_load_dwordx4 v14, s[52:55], s67 offen lds\n" \
"s_add_u32 m0, s68, 19456\n" \
"s_mov_b32 s67, 770048\n" \
"buffer_load_dwordx4 v14, s[52:55], s67 offen lds\n" \
"s_waitcnt vmcnt(24)\n" \
"ds_read_b64_tr_b16 a[44:45], v4 offset:22528\n" \
"ds_read_b64_tr_b16 a[46:47], v4 offset:22784\n" \
"ds_read_b64_tr_b16 a[172:173], v4 offset:22536\n" \
"ds_read_b64_tr_b16 a[174:175], v4 offset:22792\n" \
"s_waitcnt lgkmcnt(4)\n" \
"s_add_u32 m0, s68, 20480\n" \
"s_mov_b32 s67, 786432\n" \
"buffer_load_dwordx4 v14, s[52:55], s67 offen lds\n" \
"s_add_u32 m0, s68, 21504\n" \
"s_mov_b32 s67, 802816\n" \
"buffer_load_dwordx4 v14, s[52:55], s67 offen lds\n" \
"s_waitcnt vmcnt(24)\n" \
"ds_read_b64_tr_b16 a[48:49], v4 offset:24576\n" \
"ds_read_b64_tr_b16 a[50:51], v4 offset:24832\n" \
"ds_read_b64_tr_b16 a[176:177], v4 offset:24584\n" \
"ds_read_b64_tr_b16 a[178:179], v4 offset:24840\n" \
"s_waitcnt lgkmcnt(4)\n" \
"s_add_u32 m0, s68, 22528\n" \
"s_mov_b32 s67, 819200\n" \
"buffer_load_dwordx4 v14, s[52:55], s67 offen lds\n" \
"s_add_u32 m0, s68, 23552\n" \
"s_mov_b32 s67, 835584\n" \
"buffer_load_dwordx4 v14, s[52:55], s67 offen lds\n" \
"s_waitcnt vmcnt(24)\n" \
"ds_read_b64_tr_b16 a[52:53], v4 offset:26624\n" \
"ds_read_b64_tr_b16 a[54:55], v4 offset:26880\n" \
"ds_read_b64_tr_b16 a[180:181], v4 offset:26632\n" \
"ds_read_b64_tr_b16 a[182:183], v4 offset:26888\n" \
"s_waitcnt lgkmcnt(4)\n" \
"s_add_u32 m0, s68, 24576\n" \
"s_mov_b32 s67, 851968\n" \
"buffer_load_dwordx4 v14, s[52:55], s67 offen lds\n" \
"s_add_u32 m0, s68, 25600\n" \
"s_mov_b32 s67, 868352\n" \
"buffer_load_dwordx4 v14, s[52:55], s67 offen lds\n" \
"s_waitcnt vmcnt(24)\n" \
"ds_read_b64_tr_b16 a[56:57], v4 offset:0\n" \
"ds_read_b64_tr_b16 a[58:59], v4 offset:256\n" \
"ds_read_b64_tr_b16 a[184:185], v4 offset:8\n" \
"ds_read_b64_tr_b16 a[186:187], v4 offset:264\n" \
"s_waitcnt lgkmcnt(4)\n" \
"s_add_u32 m0, s68, 26624\n" \
"s_mov_b32 s67, 884736\n" \
"buffer_load_dwordx4 v14, s[52:55], s67 offen lds\n" \
"s_add_u32 m0, s68, 27648\n" \
"s_mov_b32 s67, 901120\n" \
"buffer_load_dwordx4 v14, s[52:55], s67 offen lds\n" \
"s_waitcnt vmcnt(24)\n" \
"ds_read_b64_tr_b16 a[60:61], v4 offset:2048\n" \
"ds_read_b64_tr_b16 a[62:63], v4 offset:2304\n" \
"ds_read_b64_tr_b16 a[188:189], v4 offset:2056\n" \
"ds_read_b64_tr_b16 a[190:191], v4 offset:2312\n" \
"s_waitcnt lgkmcnt(4)\n" \
"s_add_u32 m0, s68, 0\n" \
"s_mov_b32 s67, 917504\n" \
"buffer_load_dwordx4 v14, s[52:55], s67 offen lds\n" \
"s_add_u32 m0, s68, 1024\n" \
"s_mov_b32 s67, 933888\n" \
"buffer_load_dwordx4 v14, s[52:55], s67 offen lds\n" \
"s_waitcnt vmcnt(24)\n" \
"ds_read_b64_tr_b16 a[64:65], v4 offset:4096\n" \
"ds_read_b64_tr_b16 a[66:67], v4 offset:4352\n" \
"ds_read_b64_tr_b16 a[192:193], v4 offset:4104\n" \
"ds_read_b64_tr_b16 a[194:195], v4 offset:4360\n" \
"s_waitcnt lgkmcnt(4)\n" \
"s_add_u32 m0, s68, 2048\n" \
"s_mov_b32 s67, 950272\n" \
"buffer_load_dwordx4 v14, s[52:55], s67 offen lds\n" \
"s_add_u32 m0, s68, 3072\n" \
"s_mov_b32 s67, 966656\n" \
"buffer_load_dwordx4 v14, s[52:55], s67 offen lds\n" \
"s_waitcnt vmcnt(24)\n" \
"ds_read_b64_tr_b16 a[68:69], v4 offset:6144\n" \
"ds_read_b64_tr_b16 a[70:71], v4 offset:6400\n" \
"ds_read_b64_tr_b16 a[196:197], v4 offset:6152\n" \
"ds_read_b64_tr_b16 a[198:199], v4 offset:6408\n" \
"s_waitcnt lgkmcnt(4)\n" \
"s_add_u32 m0, s68, 4096\n" \
"s_mov_b32 s67, 983040\n" \
"buffer_load_dwordx4 v14, s[52:55], s67 offen lds\n" \
"s_add_u32 m0, s68, 5120\n" \
"s_mov_b32 s67, 999424\n" \
"buffer_load_dwordx4 v14, s[52:55], s67 offen lds\n" \
"s_waitcnt vmcnt(24)\n" \
"ds_read_b64_tr_b16 a[72:73], v4 offset:8192\n" \
"ds_read_b64_tr_b16 a[74:75], v4 offset:8448\n" \
"ds_read_b64_tr_b16 a[200:201], v4 offset:8200\n" \
"ds_read_b64_tr_b16 a[202:203], v4 offset:8456\n" \
"s_waitcnt lgkmcnt(4)\n" \
"s_add_u32 m0, s68, 6144\n" \
"s_mov_b32 s67, 1015808\n" \
"buffer_load_dwordx4 v14, s[52:55], s67 offen lds\n" \
"s_add_u32 m0, s68, 7168\n" \
"s_mov_b32 s67, 1032192\n" \
"buffer_load_dwordx4 v14, s[52:55], s67 offen lds\n" \
"s_waitcnt vmcnt(24)\n" \
"ds_read_b64_tr_b16 a[76:77], v4 offset:10240\n" \
"ds_read_b64_tr_b16 a[78:79], v4 offset:10496\n" \
"ds_read_b64_tr_b16 a[204:205], v4 offset:10248\n" \
"ds_read_b64_tr_b16 a[206:207], v4 offset:10504\n" \
"s_waitcnt vmcnt(22)\n" \
"ds_read_b64_tr_b16 a[80:81], v4 offset:12288\n" \
"ds_read_b64_tr_b16 a[82:83], v4 offset:12544\n" \
"ds_read_b64_tr_b16 a[208:209], v4 offset:12296\n" \
"ds_read_b64_tr_b16 a[210:211], v4 offset:12552\n" \
"s_waitcnt vmcnt(20)\n" \
"ds_read_b64_tr_b16 a[84:85], v4 offset:14336\n" \
"ds_read_b64_tr_b16 a[86:87], v4 offset:14592\n" \
"ds_read_b64_tr_b16 a[212:213], v4 offset:14344\n" \
"ds_read_b64_tr_b16 a[214:215], v4 offset:14600\n" \
"s_waitcnt vmcnt(18)\n" \
"ds_read_b64_tr_b16 a[88:89], v4 offset:16384\n" \
"ds_read_b64_tr_b16 a[90:91], v4 offset:16640\n" \
"ds_read_b64_tr_b16 a[216:217], v4 offset:16392\n" \
"ds_read_b64_tr_b16 a[218:219], v4 offset:16648\n" \
"s_waitcnt vmcnt(16)\n" \
"ds_read_b64_tr_b16 a[92:93], v4 offset:18432\n" \
"ds_read_b64_tr_b16 a[94:95], v4 offset:18688\n" \
"ds_read_b64_tr_b16 a[220:221], v4 offset:18440\n" \
"ds_read_b64_tr_b16 a[222:223], v4 offset:18696\n" \
"s_waitcnt vmcnt(14)\n" \
"ds_read_b64_tr_b16 a[96:97], v4 offset:20480\n" \
"ds_read_b64_tr_b16 a[98:99], v4 offset:20736\n" \
"ds_read_b64_tr_b16 a[224:225], v4 offset:20488\n" \
"ds_read_b64_tr_b16 a[226:227], v4 offset:20744\n" \
"s_waitcnt vmcnt(12)\n" \
"ds_read_b64_tr_b16 a[100:101], v4 offset:22528\n" \
"ds_read_b64_tr_b16 a[102:103], v4 offset:22784\n" \
"ds_read_b64_tr_b16 a[228:229], v4 offset:22536\n" \
"ds_read_b64_tr_b16 a[230:231], v4 offset:22792\n" \
"s_waitcnt vmcnt(10)\n" \
"ds_read_b64_tr_b16 a[104:105], v4 offset:24576\n" \
"ds_read_b64_tr_b16 a[106:107], v4 offset:24832\n" \
"ds_read_b64_tr_b16 a[232:233], v4 offset:24584\n" \
"ds_read_b64_tr_b16 a[234:235], v4 offset:24840\n" \
"s_waitcnt vmcnt(8)\n" \
"ds_read_b64_tr_b16 a[108:109], v4 offset:26624\n" \
"ds_read_b64_tr_b16 a[110:111], v4 offset:26880\n" \
"ds_read_b64_tr_b16 a[236:237], v4 offset:26632\n" \
"ds_read_b64_tr_b16 a[238:239], v4 offset:26888\n" \
"s_waitcnt vmcnt(6)\n" \
"ds_read_b64_tr_b16 a[112:113], v4 offset:0\n" \
"ds_read_b64_tr_b16 a[114:115], v4 offset:256\n" \
"ds_read_b64_tr_b16 a[240:241], v4 offset:8\n" \
"ds_read_b64_tr_b16 a[242:243], v4 offset:264\n" \
"s_waitcnt vmcnt(4)\n" \
"ds_read_b64_tr_b16 a[116:117], v4 offset:2048\n" \
"ds_read_b64_tr_b16 a[118:119], v4 offset:2304\n" \
"ds_read_b64_tr_b16 a[244:245], v4 offset:2056\n" \
"ds_read_b64_tr_b16 a[246:247], v4 offset:2312\n" \
"s_waitcnt vmcnt(2)\n" \
"ds_read_b64_tr_b16 a[120:121], v4 offset:4096\n" \
"ds_read_b64_tr_b16 a[122:123], v4 offset:4352\n" \
"ds_read_b64_tr_b16 a[248:249], v4 offset:4104\n" \
"ds_read_b64_tr_b16 a[250:251], v4 offset:4360\n" \
"s_waitcnt vmcnt(0)\n" \
"ds_read_b64_tr_b16 a[124:125], v4 offset:6144\n" \
"ds_read_b64_tr_b16 a[126:127], v4 offset:6400\n" \
"ds_read_b64_tr_b16 a[252:253], v4 offset:6152\n" \
"ds_read_b64_tr_b16 a[254:255], v4 offset:6408\n" \
"s_waitcnt lgkmcnt(0)\n" \
"s_mov_b32 s75, 12288\n" \
"s_waitcnt vmcnt(0)\n" \
"s_barrier \n" \
"v_mov_b32 v21, 65536\n" \
"v_mov_b32 v61, 0\n" \
"ds_write_b32 v21, v61\n" \
"s_waitcnt lgkmcnt(0)\n" \
"s_cmp_lg_u32 s64, 0\n" \
"s_cbranch_scc1 W_p2x%=\n" \
"s_mov_b32 s77, 0\n" \
"W_p2%=:\n" \
"buffer_load_dword v24, v19, s[40:43], s75 offen sc1\n" \
"s_waitcnt vmcnt(0)\n" \
"v_readfirstlane_b32 s67, v24\n" \
"s_nop 0\n" \
"s_cmp_ge_u32 s67, 64\n" \
"s_cbranch_scc1 W_p2x%=\n" \
"s_sleep 4\n" \
"s_add_u32 s77, s77, 1\n" \
"s_cmp_gt_u32 s77, 65536\n" \
"s_cselect_b32 s76, 1, s76\n" \
"s_cmp_eq_u32 s76, 0\n" \
"s_cbranch_scc1 W_p2%=\n" \
"W_p2x%=:\n" \
"s_barrier \n" \
"s_mov_b32 s73, 0\n" \
"s_mov_b32 s74, 0\n" \
"s_mov_b32 s75, 0\n" \
"s_mov_b32 s66, 0\n" \
"s_lshl_b32 s67, s99, 17\n" \
"s_lshl_b32 s68, s64, 15\n" \
"s_add_u32 s67, s67, s68\n" \
"s_add_u32 s67, s67, 44040192\n" \
"v_add_u32 v5, s67, v1\n" \
"v_mov_b32 v6, v0\n" \
"v_lshlrev_b32 v8, 2, v1\n" \
"v_and_b32 v8, 2047, v8\n" \
"v_add_u32 v8, 69206048, v8\n" \
"s_lshr_b32 s67, s99, 1\n" \
"s_lshl_b32 s67, s67, 11\n" \
"v_add_u32 v8, s67, v8\n" \
"Wa_wo%=:\n" \
"s_add_u32 s68, s66, 1\n" \
"s_mov_b32 s77, 0\n" \
"s_cmp_lg_u32 s64, 0\n" \
"s_cbranch_scc1 Wa_p4%=\n" \
"Wa_p3%=:\n" \
"buffer_load_dword v60, v8, s[40:43], s74 offen sc1\n" \
"s_waitcnt vmcnt(0)\n" \
"v_cmp_eq_u32 vcc, 0, v60\n" \
"s_cbranch_vccz Wa_p3y%=\n" \
"s_sleep 3\n" \
"s_add_u32 s77, s77, 1\n" \
"s_cmp_gt_u32 s77, 65536\n" \
"s_cselect_b32 s76, 1, s76\n" \
"s_cmp_eq_u32 s76, 0\n" \
"s_cbranch_scc1 Wa_p3%=\n" \
"Wa_p3y%=:\n" \
"v_mov_b32 v60, s68\n" \
"ds_write_b32 v21, v60\n" \
"s_branch Wa_p3x%=\n" \
"Wa_p4%=:\n" \
"ds_read_b32 v60, v21\n" \
"s_waitcnt lgkmcnt(0)\n" \
"v_readfirstlane_b32 s67, v60\n" \
"s_nop 0\n" \
"s_cmp_ge_u32 s67, s68\n" \
"s_cbranch_scc1 Wa_p3x%=\n" \
"s_add_u32 s77, s77, 1\n" \
"s_cmp_lt_u32 s77, 2097152\n" \
"s_cbranch_scc1 Wa_p4%=\n" \
"Wa_p3x%=:\n" \
"buffer_load_dwordx2 v[40:41], v6, s[60:63], s75 offen\n" \
"s_add_u32 s68, s75, 67108864\n" \
"buffer_load_dwordx2 v[42:43], v6, s[40:43], s68 offen sc1\n" \
"s_add_u32 s68, s74, 4096\n" \
"buffer_load_dword v60, v8, s[40:43], s68 offen sc1\n" \
"s_add_u32 s67, s73, 0\n" \
"buffer_load_dwordx4 v[64:67], v5, s[40:43], s67 offen offset:0 sc1\n" \
"buffer_load_dwordx4 v[68:71], v5, s[40:43], s67 offen offset:1024 sc1\n" \
"buffer_load_dwordx4 v[72:75], v5, s[40:43], s67 offen offset:2048 sc1\n" \
"buffer_load_dwordx4 v[76:79], v5, s[40:43], s67 offen offset:3072 sc1\n" \
"s_add_u32 s67, s73, 4096\n" \
"buffer_load_dwordx4 v[80:83], v5, s[40:43], s67 offen offset:0 sc1\n" \
"buffer_load_dwordx4 v[84:87], v5, s[40:43], s67 offen offset:1024 sc1\n" \
"buffer_load_dwordx4 v[88:91], v5, s[40:43], s67 offen offset:2048 sc1\n" \
"buffer_load_dwordx4 v[92:95], v5, s[40:43], s67 offen offset:3072 sc1\n" \
"s_add_u32 s67, s73, 8192\n" \
"buffer_load_dwordx4 v[96:99], v5, s[40:43], s67 offen offset:0 sc1\n" \
"buffer_load_dwordx4 v[100:103], v5, s[40:43], s67 offen offset:1024 sc1\n" \
"buffer_load_dwordx4 v[104:107], v5, s[40:43], s67 offen offset:2048 sc1\n" \
"buffer_load_dwordx4 v[108:111], v5, s[40:43], s67 offen offset:3072 sc1\n" \
"s_add_u32 s67, s73, 12288\n" \
"buffer_load_dwordx4 v[112:115], v5, s[40:43], s67 offen offset:0 sc1\n" \
"buffer_load_dwordx4 v[116:119], v5, s[40:43], s67 offen offset:1024 sc1\n" \
"buffer_load_dwordx4 v[120:123], v5, s[40:43], s67 offen offset:2048 sc1\n" \
"buffer_load_dwordx4 v[124:127], v5, s[40:43], s67 offen offset:3072 sc1\n" \
"s_add_u32 s67, s73, 16384\n" \
"buffer_load_dwordx4 v[128:131], v5, s[40:43], s67 offen offset:0 sc1\n" \
"buffer_load_dwordx4 v[132:135], v5, s[40:43], s67 offen offset:1024 sc1\n" \
"buffer_load_dwordx4 v[136:139], v5, s[40:43], s67 offen offset:2048 sc1\n" \
"buffer_load_dwordx4 v[140:143], v5, s[40:43], s67 offen offset:3072 sc1\n" \
"s_add_u32 s67, s73, 20480\n" \
"buffer_load_dwordx4 v[144:147], v5, s[40:43], s67 offen offset:0 sc1\n" \
"buffer_load_dwordx4 v[148:151], v5, s[40:43], s67 offen offset:1024 sc1\n" \
"buffer_load_dwordx4 v[152:155], v5, s[40:43], s67 offen offset:2048 sc1\n" \
"buffer_load_dwordx4 v[156:159], v5, s[40:43], s67 offen offset:3072 sc1\n" \
"s_add_u32 s67, s73, 24576\n" \
"buffer_load_dwordx4 v[160:163], v5, s[40:43], s67 offen offset:0 sc1\n" \
"buffer_load_dwordx4 v[164:167], v5, s[40:43], s67 offen offset:1024 sc1\n" \
"buffer_load_dwordx4 v[168:171], v5, s[40:43], s67 offen offset:2048 sc1\n" \
"buffer_load_dwordx4 v[172:175], v5, s[40:43], s67 offen offset:3072 sc1\n" \
"s_add_u32 s67, s73, 28672\n" \
"buffer_load_dwordx4 v[176:179], v5, s[40:43], s67 offen offset:0 sc1\n" \
"buffer_load_dwordx4 v[180:183], v5, s[40:43], s67 offen offset:1024 sc1\n" \
"buffer_load_dwordx4 v[184:187], v5, s[40:43], s67 offen offset:2048 sc1\n" \
"buffer_load_dwordx4 v[188:191], v5, s[40:43], s67 offen offset:3072 sc1\n" \
"Wa_hot%=:\n" \
"s_add_u32 s73, s73, 524288\n" \
"s_add_u32 s74, s74, 4096\n" \
"s_add_u32 s66, s66, 2\n" \
"s_waitcnt vmcnt(32)\n" \
"v_cmp_eq_u32 vcc, 0, v60\n" \
"v_lshlrev_b32 v48, 16, v40\n" \
"v_and_b32 v49, 0xffff0000, v40\n" \
"v_lshlrev_b32 v50, 16, v41\n" \
"v_and_b32 v51, 0xffff0000, v41\n" \
"v_lshlrev_b32 v52, 16, v42\n" \
"v_and_b32 v53, 0xffff0000, v42\n" \
"v_lshlrev_b32 v54, 16, v43\n" \
"v_and_b32 v55, 0xffff0000, v43\n" \
"s_mov_b32 s72, s75\n" \
"s_add_u32 s75, s75, 65536\n" \
"s_cmp_eq_u64 vcc, 0\n" \
"s_cselect_b32 s71, 1, 0\n" \
"s_cmp_ge_u32 s66, 62\n" \
"s_cselect_b32 s71, 0, s71\n" \
"s_cmp_lg_u32 s71, 0\n" \
"s_cbranch_scc1 Wa_more%=\n" \
"s_waitcnt vmcnt(31)\n" \
"v_mfma_f32_16x16x32_bf16 v[208:211], a[0:3], v[64:67], 0\n" \
"v_mfma_f32_16x16x32_bf16 v[212:215], a[128:131], v[64:67], 0\n" \
"s_waitcnt vmcnt(30)\n" \
"v_mfma_f32_16x16x32_bf16 v[208:211], a[4:7], v[68:71], v[208:211]\n" \
"v_mfma_f32_16x16x32_bf16 v[212:215], a[132:135], v[68:71], v[212:215]\n" \
"s_waitcnt vmcnt(29)\n" \
"v_mfma_f32_16x16x32_bf16 v[208:211], a[8:11], v[72:75], v[208:211]\n" \
"v_mfma_f32_16x16x32_bf16 v[212:215], a[136:139], v[72:75], v[212:215]\n" \
"s_waitcnt vmcnt(28)\n" \
"v_mfma_f32_16x16x32_bf16 v[208:211], a[12:15], v[76:79], v[208:211]\n" \
"v_mfma_f32_16x16x32_bf16 v[212:215], a[140:143], v[76:79], v[212:215]\n" \
"s_waitcnt vmcnt(27)\n" \
"v_mfma_f32_16x16x32_bf16 v[208:211], a[16:19], v[80:83], v[208:211]\n" \
"v_mfma_f32_16x16x32_bf16 v[212:215], a[144:147], v[80:83], v[212:215]\n" \
"s_waitcnt vmcnt(26)\n" \
"v_mfma_f32_16x16x32_bf16 v[208:211], a[20:23], v[84:87], v[208:211]\n" \
"v_mfma_f32_16x16x32_bf16 v[212:215], a[148:151], v[84:87], v[212:215]\n" \
"s_waitcnt vmcnt(25)\n" \
"v_mfma_f32_16x16x32_bf16 v[208:211], a[24:27], v[88:91], v[208:211]\n" \
"v_mfma_f32_16x16x32_bf16 v[212:215], a[152:155], v[88:91], v[212:215]\n" \
"s_waitcnt vmcnt(24)\n" \
"v_mfma_f32_16x16x32_bf16 v[208:211], a[28:31], v[92:95], v[208:211]\n" \
"v_mfma_f32_16x16x32_bf16 v[212:215], a[156:159], v[92:95], v[212:215]\n" \
"s_waitcnt vmcnt(23)\n" \
"v_mfma_f32_16x16x32_bf16 v[208:211], a[32:35], v[96:99], v[208:211]\n" \
"v_mfma_f32_16x16x32_bf16 v[212:215], a[160:163], v[96:99], v[212:215]\n" \
"s_waitcnt vmcnt(22)\n" \
"v_mfma_f32_16x16x32_bf16 v[208:211], a[36:39], v[100:103], v[208:211]\n" \
"v_mfma_f32_16x16x32_bf16 v[212:215], a[164:167], v[100:103], v[212:215]\n" \
"s_waitcnt vmcnt(21)\n" \
"v_mfma_f32_16x16x32_bf16 v[208:211], a[40:43], v[104:107], v[208:211]\n" \
"v_mfma_f32_16x16x32_bf16 v[212:215], a[168:171], v[104:107], v[212:215]\n" \
"s_waitcnt vmcnt(20)\n" \
"v_mfma_f32_16x16x32_bf16 v[208:211], a[44:47], v[108:111], v[208:211]\n" \
"v_mfma_f32_16x16x32_bf16 v[212:215], a[172:175], v[108:111], v[212:215]\n" \
"s_waitcnt vmcnt(19)\n" \
"v_mfma_f32_16x16x32_bf16 v[208:211], a[48:51], v[112:115], v[208:211]\n" \
"v_mfma_f32_16x16x32_bf16 v[212:215], a[176:179], v[112:115], v[212:215]\n" \
"s_waitcnt vmcnt(18)\n" \
"v_mfma_f32_16x16x32_bf16 v[208:211], a[52:55], v[116:119], v[208:211]\n" \
"v_mfma_f32_16x16x32_bf16 v[212:215], a[180:183], v[116:119], v[212:215]\n" \
"s_waitcnt vmcnt(17)\n" \
"v_mfma_f32_16x16x32_bf16 v[208:211], a[56:59], v[120:123], v[208:211]\n" \
"v_mfma_f32_16x16x32_bf16 v[212:215], a[184:187], v[120:123], v[212:215]\n" \
"s_waitcnt vmcnt(16)\n" \
"v_mfma_f32_16x16x32_bf16 v[208:211], a[60:63], v[124:127], v[208:211]\n" \
"v_mfma_f32_16x16x32_bf16 v[212:215], a[188:191], v[124:127], v[212:215]\n" \
"s_waitcnt vmcnt(15)\n" \
"v_mfma_f32_16x16x32_bf16 v[208:211], a[64:67], v[128:131], v[208:211]\n" \
"v_mfma_f32_16x16x32_bf16 v[212:215], a[192:195], v[128:131], v[212:215]\n" \
"s_waitcnt vmcnt(14)\n" \
"v_mfma_f32_16x16x32_bf16 v[208:211], a[68:71], v[132:135], v[208:211]\n" \
"v_mfma_f32_16x16x32_bf16 v[212:215], a[196:199], v[132:135], v[212:215]\n" \
"s_waitcnt vmcnt(13)\n" \
"v_mfma_f32_16x16x32_bf16 v[208:211], a[72:75], v[136:139], v[208:211]\n" \
"v_mfma_f32_16x16x32_bf16 v[212:215], a[200:203], v[136:139], v[212:215]\n" \
"s_waitcnt vmcnt(12)\n" \
"v_mfma_f32_16x16x32_bf16 v[208:211], a[76:79], v[140:143], v[208:211]\n" \
"v_mfma_f32_16x16x32_bf16 v[212:215], a[204:207], v[140:143], v[212:215]\n" \
"s_waitcnt vmcnt(11)\n" \
"v_mfma_f32_16x16x32_bf16 v[208:211], a[80:83], v[144:147], v[208:211]\n" \
"v_mfma_f32_16x16x32_bf16 v[212:215], a[208:211], v[144:147], v[212:215]\n" \
"s_waitcnt vmcnt(10)\n" \
"v_mfma_f32_16x16x32_bf16 v[208:211], a[84:87], v[148:151], v[208:211]\n" \
"v_mfma_f32_16x16x32_bf16 v[212:215], a[212:215], v[148:151], v[212:215]\n" \
"s_waitcnt vmcnt(9)\n" \
"v_mfma_f32_16x16x32_bf16 v[208:211], a[88:91], v[152:155], v[208:211]\n" \
"v_mfma_f32_16x16x32_bf16 v[212:215], a[216:219], v[152:155], v[212:215]\n" \
"s_waitcnt vmcnt(8)\n" \
"v_mfma_f32_16x16x32_bf16 v[208:211], a[92:95], v[156:159], v[208:211]\n" \
"v_mfma_f32_16x16x32_bf16 v[212:215], a[220:223], v[156:159], v[212:215]\n" \
"s_waitcnt vmcnt(7)\n" \
"v_mfma_f32_16x16x32_bf16 v[208:211], a[96:99], v[160:163], v[208:211]\n" \
"v_mfma_f32_16x16x32_bf16 v[212:215], a[224:227], v[160:163], v[212:215]\n" \
"s_waitcnt vmcnt(6)\n" \
"v_mfma_f32_16x16x32_bf16 v[208:211], a[100:103], v[164:167], v[208:211]\n" \
"v_mfma_f32_16x16x32_bf16 v[212:215], a[228:231], v[164:167], v[212:215]\n" \
"s_waitcnt vmcnt(5)\n" \
"v_mfma_f32_16x16x32_bf16 v[208:211], a[104:107], v[168:171], v[208:211]\n" \
"v_mfma_f32_16x16x32_bf16 v[212:215], a[232:235], v[168:171], v[212:215]\n" \
"s_waitcnt vmcnt(4)\n" \
"v_mfma_f32_16x16x32_bf16 v[208:211], a[108:111], v[172:175], v[208:211]\n" \
"v_mfma_f32_16x16x32_bf16 v[212:215], a[236:239], v[172:175], v[212:215]\n" \
"s_waitcnt vmcnt(3)\n" \
"v_mfma_f32_16x16x32_bf16 v[208:211], a[112:115], v[176:179], v[208:211]\n" \
"v_mfma_f32_16x16x32_bf16 v[212:215], a[240:243], v[176:179], v[212:215]\n" \
"s_waitcnt vmcnt(2)\n" \
"v_mfma_f32_16x16x32_bf16 v[208:211], a[116:119], v[180:183], v[208:211]\n" \
"v_mfma_f32_16x16x32_bf16 v[212:215], a[244:247], v[180:183], v[212:215]\n" \
"s_waitcnt vmcnt(1)\n" \
"v_mfma_f32_16x16x32_bf16 v[208:211], a[120:123], v[184:187], v[208:211]\n" \
"v_mfma_f32_16x16x32_bf16 v[212:215], a[248:251], v[184:187], v[212:215]\n" \
"s_waitcnt vmcnt(0)\n" \
"v_mfma_f32_16x16x32_bf16 v[208:211], a[124:127], v[188:191], v[208:211]\n" \
"v_mfma_f32_16x16x32_bf16 v[212:215], a[252:255], v[188:191], v[212:215]\n" \
"s_nop 7\n" \
"s_branch Wa_red%=\n" \
"Wa_more%=:\n" \
"s_cmp_lg_u32 s64, 0\n" \
"s_cbranch_scc1 Wa_mor2%=\n" \
"s_add_u32 s68, s66, 1\n" \
"v_mov_b32 v61, s68\n" \
"ds_write_b32 v21, v61\n" \
"Wa_mor2%=:\n" \
"buffer_load_dwordx2 v[40:41], v6, s[60:63], s75 offen\n" \
"s_add_u32 s68, s75, 67108864\n" \
"buffer_load_dwordx2 v[42:43], v6, s[40:43], s68 offen sc1\n" \
"s_add_u32 s68, s74, 4096\n" \
"buffer_load_dword v60, v8, s[40:43], s68 offen sc1\n" \
"s_waitcnt vmcnt(34)\n" \
"v_mfma_f32_16x16x32_bf16 v[208:211], a[0:3], v[64:67], 0\n" \
"v_mfma_f32_16x16x32_bf16 v[212:215], a[128:131], v[64:67], 0\n" \
"s_add_u32 s67, s73, 0\n" \
"buffer_load_dwordx4 v[64:67], v5, s[40:43], s67 offen offset:0 sc1\n" \
"s_waitcnt vmcnt(34)\n" \
"v_mfma_f32_16x16x32_bf16 v[208:211], a[4:7], v[68:71], v[208:211]\n" \
"v_mfma_f32_16x16x32_bf16 v[212:215], a[132:135], v[68:71], v[212:215]\n" \
"buffer_load_dwordx4 v[68:71], v5, s[40:43], s67 offen offset:1024 sc1\n" \
"s_waitcnt vmcnt(34)\n" \
"v_mfma_f32_16x16x32_bf16 v[208:211], a[8:11], v[72:75], v[208:211]\n" \
"v_mfma_f32_16x16x32_bf16 v[212:215], a[136:139], v[72:75], v[212:215]\n" \
"buffer_load_dwordx4 v[72:75], v5, s[40:43], s67 offen offset:2048 sc1\n" \
"s_waitcnt vmcnt(34)\n" \
"v_mfma_f32_16x16x32_bf16 v[208:211], a[12:15], v[76:79], v[208:211]\n" \
"v_mfma_f32_16x16x32_bf16 v[212:215], a[140:143], v[76:79], v[212:215]\n" \
"buffer_load_dwordx4 v[76:79], v5, s[40:43], s67 offen offset:3072 sc1\n" \
"s_waitcnt vmcnt(34)\n" \
"v_mfma_f32_16x16x32_bf16 v[208:211], a[16:19], v[80:83], v[208:211]\n" \
"v_mfma_f32_16x16x32_bf16 v[212:215], a[144:147], v[80:83], v[212:215]\n" \
"s_add_u32 s67, s73, 4096\n" \
"buffer_load_dwordx4 v[80:83], v5, s[40:43], s67 offen offset:0 sc1\n" \
"s_waitcnt vmcnt(34)\n" \
"v_mfma_f32_16x16x32_bf16 v[208:211], a[20:23], v[84:87], v[208:211]\n" \
"v_mfma_f32_16x16x32_bf16 v[212:215], a[148:151], v[84:87], v[212:215]\n" \
"buffer_load_dwordx4 v[84:87], v5, s[40:43], s67 offen offset:1024 sc1\n" \
"s_waitcnt vmcnt(34)\n" \
"v_mfma_f32_16x16x32_bf16 v[208:211], a[24:27], v[88:91], v[208:211]\n" \
"v_mfma_f32_16x16x32_bf16 v[212:215], a[152:155], v[88:91], v[212:215]\n" \
"buffer_load_dwordx4 v[88:91], v5, s[40:43], s67 offen offset:2048 sc1\n" \
"s_waitcnt vmcnt(34)\n" \
"v_mfma_f32_16x16x32_bf16 v[208:211], a[28:31], v[92:95], v[208:211]\n" \
"v_mfma_f32_16x16x32_bf16 v[212:215], a[156:159], v[92:95], v[212:215]\n" \
"buffer_load_dwordx4 v[92:95], v5, s[40:43], s67 offen offset:3072 sc1\n" \
"s_waitcnt vmcnt(34)\n" \
"v_mfma_f32_16x16x32_bf16 v[208:211], a[32:35], v[96:99], v[208:211]\n" \
"v_mfma_f32_16x16x32_bf16 v[212:215], a[160:163], v[96:99], v[212:215]\n" \
"s_add_u32 s67, s73, 8192\n" \
"buffer_load_dwordx4 v[96:99], v5, s[40:43], s67 offen offset:0 sc1\n" \
"s_waitcnt vmcnt(34)\n" \
"v_mfma_f32_16x16x32_bf16 v[208:211], a[36:39], v[100:103], v[208:211]\n" \
"v_mfma_f32_16x16x32_bf16 v[212:215], a[164:167], v[100:103], v[212:215]\n" \
"buffer_load_dwordx4 v[100:103], v5, s[40:43], s67 offen offset:1024 sc1\n" \
"s_waitcnt vmcnt(34)\n" \
"v_mfma_f32_16x16x32_bf16 v[208:211], a[40:43], v[104:107], v[208:211]\n" \
"v_mfma_f32_16x16x32_bf16 v[212:215], a[168:171], v[104:107], v[212:215]\n" \
"buffer_load_dwordx4 v[104:107], v5, s[40:43], s67 offen offset:2048 sc1\n" \
"s_waitcnt vmcnt(34)\n" \
"v_mfma_f32_16x16x32_bf16 v[208:211], a[44:47], v[108:111], v[208:211]\n" \
"v_mfma_f32_16x16x32_bf16 v[212:215], a[172:175], v[108:111], v[212:215]\n" \
"buffer_load_dwordx4 v[108:111], v5, s[40:43], s67 offen offset:3072 sc1\n" \
"s_waitcnt vmcnt(34)\n" \
"v_mfma_f32_16x16x32_bf16 v[208:211], a[48:51], v[112:115], v[208:211]\n" \
"v_mfma_f32_16x16x32_bf16 v[212:215], a[176:179], v[112:115], v[212:215]\n" \
"s_add_u32 s67, s73, 12288\n" \
"buffer_load_dwordx4 v[112:115], v5, s[40:43], s67 offen offset:0 sc1\n" \
"s_waitcnt vmcnt(34)\n" \
"v_mfma_f32_16x16x32_bf16 v[208:211], a[52:55], v[116:119], v[208:211]\n" \
"v_mfma_f32_16x16x32_bf16 v[212:215], a[180:183], v[116:119], v[212:215]\n" \
"buffer_load_dwordx4 v[116:119], v5, s[40:43], s67 offen offset:1024 sc1\n" \
"s_waitcnt vmcnt(34)\n" \
"v_mfma_f32_16x16x32_bf16 v[208:211], a[56:59], v[120:123], v[208:211]\n" \
"v_mfma_f32_16x16x32_bf16 v[212:215], a[184:187], v[120:123], v[212:215]\n" \
"buffer_load_dwordx4 v[120:123], v5, s[40:43], s67 offen offset:2048 sc1\n" \
"s_waitcnt vmcnt(34)\n" \
"v_mfma_f32_16x16x32_bf16 v[208:211], a[60:63], v[124:127], v[208:211]\n" \
"v_mfma_f32_16x16x32_bf16 v[212:215], a[188:191], v[124:127], v[212:215]\n" \
"buffer_load_dwordx4 v[124:127], v5, s[40:43], s67 offen offset:3072 sc1\n" \
"s_waitcnt vmcnt(34)\n" \
"v_mfma_f32_16x16x32_bf16 v[208:211], a[64:67], v[128:131], v[208:211]\n" \
"v_mfma_f32_16x16x32_bf16 v[212:215], a[192:195], v[128:131], v[212:215]\n" \
"s_add_u32 s67, s73, 16384\n" \
"buffer_load_dwordx4 v[128:131], v5, s[40:43], s67 offen offset:0 sc1\n" \
"s_waitcnt vmcnt(34)\n" \
"v_mfma_f32_16x16x32_bf16 v[208:211], a[68:71], v[132:135], v[208:211]\n" \
"v_mfma_f32_16x16x32_bf16 v[212:215], a[196:199], v[132:135], v[212:215]\n" \
"buffer_load_dwordx4 v[132:135], v5, s[40:43], s67 offen offset:1024 sc1\n" \
"s_waitcnt vmcnt(34)\n" \
"v_mfma_f32_16x16x32_bf16 v[208:211], a[72:75], v[136:139], v[208:211]\n" \
"v_mfma_f32_16x16x32_bf16 v[212:215], a[200:203], v[136:139], v[212:215]\n" \
"buffer_load_dwordx4 v[136:139], v5, s[40:43], s67 offen offset:2048 sc1\n" \
"s_waitcnt vmcnt(34)\n" \
"v_mfma_f32_16x16x32_bf16 v[208:211], a[76:79], v[140:143], v[208:211]\n" \
"v_mfma_f32_16x16x32_bf16 v[212:215], a[204:207], v[140:143], v[212:215]\n" \
"buffer_load_dwordx4 v[140:143], v5, s[40:43], s67 offen offset:3072 sc1\n" \
"s_waitcnt vmcnt(34)\n" \
"v_mfma_f32_16x16x32_bf16 v[208:211], a[80:83], v[144:147], v[208:211]\n" \
"v_mfma_f32_16x16x32_bf16 v[212:215], a[208:211], v[144:147], v[212:215]\n" \
"s_add_u32 s67, s73, 20480\n" \
"buffer_load_dwordx4 v[144:147], v5, s[40:43], s67 offen offset:0 sc1\n" \
"s_waitcnt vmcnt(34)\n" \
"v_mfma_f32_16x16x32_bf16 v[208:211], a[84:87], v[148:151], v[208:211]\n" \
"v_mfma_f32_16x16x32_bf16 v[212:215], a[212:215], v[148:151], v[212:215]\n" \
"buffer_load_dwordx4 v[148:151], v5, s[40:43], s67 offen offset:1024 sc1\n" \
"s_waitcnt vmcnt(34)\n" \
"v_mfma_f32_16x16x32_bf16 v[208:211], a[88:91], v[152:155], v[208:211]\n" \
"v_mfma_f32_16x16x32_bf16 v[212:215], a[216:219], v[152:155], v[212:215]\n" \
"buffer_load_dwordx4 v[152:155], v5, s[40:43], s67 offen offset:2048 sc1\n" \
"s_waitcnt vmcnt(34)\n" \
"v_mfma_f32_16x16x32_bf16 v[208:211], a[92:95], v[156:159], v[208:211]\n" \
"v_mfma_f32_16x16x32_bf16 v[212:215], a[220:223], v[156:159], v[212:215]\n" \
"buffer_load_dwordx4 v[156:159], v5, s[40:43], s67 offen offset:3072 sc1\n" \
"s_waitcnt vmcnt(34)\n" \
"v_mfma_f32_16x16x32_bf16 v[208:211], a[96:99], v[160:163], v[208:211]\n" \
"v_mfma_f32_16x16x32_bf16 v[212:215], a[224:227], v[160:163], v[212:215]\n" \
"s_add_u32 s67, s73, 24576\n" \
"buffer_load_dwordx4 v[160:163], v5, s[40:43], s67 offen offset:0 sc1\n" \
"s_waitcnt vmcnt(34)\n" \
"v_mfma_f32_16x16x32_bf16 v[208:211], a[100:103], v[164:167], v[208:211]\n" \
"v_mfma_f32_16x16x32_bf16 v[212:215], a[228:231], v[164:167], v[212:215]\n" \
"buffer_load_dwordx4 v[164:167], v5, s[40:43], s67 offen offset:1024 sc1\n" \
"s_waitcnt vmcnt(34)\n" \
"v_mfma_f32_16x16x32_bf16 v[208:211], a[104:107], v[168:171], v[208:211]\n" \
"v_mfma_f32_16x16x32_bf16 v[212:215], a[232:235], v[168:171], v[212:215]\n" \
"buffer_load_dwordx4 v[168:171], v5, s[40:43], s67 offen offset:2048 sc1\n" \
"s_waitcnt vmcnt(34)\n" \
"v_mfma_f32_16x16x32_bf16 v[208:211], a[108:111], v[172:175], v[208:211]\n" \
"v_mfma_f32_16x16x32_bf16 v[212:215], a[236:239], v[172:175], v[212:215]\n" \
"buffer_load_dwordx4 v[172:175], v5, s[40:43], s67 offen offset:3072 sc1\n" \
"s_waitcnt vmcnt(34)\n" \
"v_mfma_f32_16x16x32_bf16 v[208:211], a[112:115], v[176:179], v[208:211]\n" \
"v_mfma_f32_16x16x32_bf16 v[212:215], a[240:243], v[176:179], v[212:215]\n" \
"s_add_u32 s67, s73, 28672\n" \
"buffer_load_dwordx4 v[176:179], v5, s[40:43], s67 offen offset:0 sc1\n" \
"s_waitcnt vmcnt(34)\n" \
"v_mfma_f32_16x16x32_bf16 v[208:211], a[116:119], v[180:183], v[208:211]\n" \
"v_mfma_f32_16x16x32_bf16 v[212:215], a[244:247], v[180:183], v[212:215]\n" \
"buffer_load_dwordx4 v[180:183], v5, s[40:43], s67 offen offset:1024 sc1\n" \
"s_waitcnt vmcnt(34)\n" \
"v_mfma_f32_16x16x32_bf16 v[208:211], a[120:123], v[184:187], v[208:211]\n" \
"v_mfma_f32_16x16x32_bf16 v[212:215], a[248:251], v[184:187], v[212:215]\n" \
"buffer_load_dwordx4 v[184:187], v5, s[40:43], s67 offen offset:2048 sc1\n" \
"s_waitcnt vmcnt(34)\n" \
"v_mfma_f32_16x16x32_bf16 v[208:211], a[124:127], v[188:191], v[208:211]\n" \
"v_mfma_f32_16x16x32_bf16 v[212:215], a[252:255], v[188:191], v[212:215]\n" \
"buffer_load_dwordx4 v[188:191], v5, s[40:43], s67 offen offset:3072 sc1\n" \
"s_nop 6\n" \
"Wa_red%=:\n" \
"ds_write_b128 v22, v[208:211] offset:0\n" \
"ds_write_b128 v22, v[212:215] offset:2048\n" \
"v_xor_b32 v22, 32768, v22\n" \
"s_waitcnt lgkmcnt(0)\n" \
"s_barrier \n" \
"s_cmp_gt_u32 s64, 1\n" \
"s_cbranch_scc1 Wa_won%=\n" \
"ds_read_b128 v[192:195], v23 offset:0\n" \
"ds_read_b128 v[196:199], v23 offset:8192\n" \
"ds_read_b128 v[200:203], v23 offset:16384\n" \
"ds_read_b128 v[204:207], v23 offset:24576\n" \
"v_xor_b32 v23, 32768, v23\n" \
"s_waitcnt lgkmcnt(2)\n" \
"v_add_f32 v192, v192, v196\n" \
"v_add_f32 v193, v193, v197\n" \
"v_add_f32 v194, v194, v198\n" \
"v_add_f32 v195, v195, v199\n" \
"s_waitcnt lgkmcnt(1)\n" \
"v_add_f32 v192, v192, v200\n" \
"v_add_f32 v193, v193, v201\n" \
"v_add_f32 v194, v194, v202\n" \
"v_add_f32 v195, v195, v203\n" \
"s_waitcnt lgkmcnt(0)\n" \
"v_add_f32 v192, v192, v204\n" \
"v_add_f32 v193, v193, v205\n" \
"v_add_f32 v194, v194, v206\n" \
"v_add_f32 v195, v195, v207\n" \
"v_fma_f32 v192, v192, v52, v48\n" \
"v_fma_f32 v193, v193, v53, v49\n" \
"v_fma_f32 v194, v194, v54, v50\n" \
"v_fma_f32 v195, v195, v55, v51\n" \
"v_cvt_pk_bf16_f32 v56, v192, v193\n" \
"v_cvt_pk_bf16_f32 v57, v194, v195\n" \
"buffer_store_dwordx2 v[56:57], v6, s[88:91], s72 offen\n" \
"s_nop 0\n" \
"Wa_won%=:\n" \
"s_cmp_lg_u32 s71, 0\n" \
"s_cbranch_scc1 Wa_hot%=\n" \
"s_cmp_lt_u32 s66, 62\n" \
"s_cbranch_scc1 Wa_wo%=\n" \
"v_lshlrev_b32 v8, 2, v1\n" \
"v_and_b32 v8, 2047, v8\n" \
"v_add_u32 v8, 69206048, v8\n" \
"Wb_wo%=:\n" \
"s_add_u32 s68, s66, 1\n" \
"s_mov_b32 s77, 0\n" \
"s_cmp_lg_u32 s64, 0\n" \
"s_cbranch_scc1 Wb_p4%=\n" \
"Wb_p3%=:\n" \
"buffer_load_dword v60, v8, s[40:43], s74 offen sc1\n" \
"s_waitcnt vmcnt(0)\n" \
"v_cmp_eq_u32 vcc, 0, v60\n" \
"s_cbranch_vccz Wb_p3y%=\n" \
"s_sleep 1\n" \
"s_add_u32 s77, s77, 1\n" \
"s_cmp_gt_u32 s77, 65536\n" \
"s_cselect_b32 s76, 1, s76\n" \
"s_cmp_eq_u32 s76, 0\n" \
"s_cbranch_scc1 Wb_p3%=\n" \
"Wb_p3y%=:\n" \
"v_mov_b32 v60, s68\n" \
"ds_write_b32 v21, v60\n" \
"s_branch Wb_p3x%=\n" \
"Wb_p4%=:\n" \
"ds_read_b32 v60, v21\n" \
"s_waitcnt lgkmcnt(0)\n" \
"v_readfirstlane_b32 s67, v60\n" \
"s_nop 0\n" \
"s_cmp_ge_u32 s67, s68\n" \
"s_cbranch_scc1 Wb_p3x%=\n" \
"s_add_u32 s77, s77, 1\n" \
"s_cmp_lt_u32 s77, 2097152\n" \
"s_cbranch_scc1 Wb_p4%=\n" \
"Wb_p3x%=:\n" \
"buffer_load_dwordx2 v[40:41], v16, s[60:63], s75 offen\n" \
"s_add_u32 s68, s75, 67108864\n" \
"buffer_load_dwordx2 v[42:43], v16, s[40:43], s68 offen sc1\n" \
"s_add_u32 s68, s74, 2048\n" \
"buffer_load_dword v60, v8, s[40:43], s68 offen sc1\n" \
"s_add_u32 s67, s73, 0\n" \
"buffer_load_dwordx4 v[64:67], v15, s[40:43], s67 offen offset:0 sc1\n" \
"buffer_load_dwordx4 v[68:71], v15, s[40:43], s67 offen offset:2048 sc1\n" \
"s_add_u32 s67, s73, 4096\n" \
"buffer_load_dwordx4 v[72:75], v15, s[40:43], s67 offen offset:0 sc1\n" \
"buffer_load_dwordx4 v[76:79], v15, s[40:43], s67 offen offset:2048 sc1\n" \
"s_add_u32 s67, s73, 8192\n" \
"buffer_load_dwordx4 v[80:83], v15, s[40:43], s67 offen offset:0 sc1\n" \
"buffer_load_dwordx4 v[84:87], v15, s[40:43], s67 offen offset:2048 sc1\n" \
"s_add_u32 s67, s73, 12288\n" \
"buffer_load_dwordx4 v[88:91], v15, s[40:43], s67 offen offset:0 sc1\n" \
"buffer_load_dwordx4 v[92:95], v15, s[40:43], s67 offen offset:2048 sc1\n" \
"s_add_u32 s67, s73, 16384\n" \
"buffer_load_dwordx4 v[96:99], v15, s[40:43], s67 offen offset:0 sc1\n" \
"buffer_load_dwordx4 v[100:103], v15, s[40:43], s67 offen offset:2048 sc1\n" \
"s_add_u32 s67, s73, 20480\n" \
"buffer_load_dwordx4 v[104:107], v15, s[40:43], s67 offen offset:0 sc1\n" \
"buffer_load_dwordx4 v[108:111], v15, s[40:43], s67 offen offset:2048 sc1\n" \
"s_add_u32 s67, s73, 24576\n" \
"buffer_load_dwordx4 v[112:115], v15, s[40:43], s67 offen offset:0 sc1\n" \
"buffer_load_dwordx4 v[116:119], v15, s[40:43], s67 offen offset:2048 sc1\n" \
"s_add_u32 s67, s73, 28672\n" \
"buffer_load_dwordx4 v[120:123], v15, s[40:43], s67 offen offset:0 sc1\n" \
"buffer_load_dwordx4 v[124:127], v15, s[40:43], s67 offen offset:2048 sc1\n" \
"Wb_hot%=:\n" \
"s_add_u32 s73, s73, 262144\n" \
"s_add_u32 s74, s74, 2048\n" \
"s_add_u32 s66, s66, 1\n" \
"s_waitcnt vmcnt(16)\n" \
"v_cmp_eq_u32 vcc, 0, v60\n" \
"v_lshlrev_b32 v48, 16, v40\n" \
"v_and_b32 v49, 0xffff0000, v40\n" \
"v_lshlrev_b32 v50, 16, v41\n" \
"v_and_b32 v51, 0xffff0000, v41\n" \
"v_lshlrev_b32 v52, 16, v42\n" \
"v_and_b32 v53, 0xffff0000, v42\n" \
"v_lshlrev_b32 v54, 16, v43\n" \
"v_and_b32 v55, 0xffff0000, v43\n" \
"s_mov_b32 s72, s75\n" \
"s_add_u32 s75, s75, 32768\n" \
"s_cmp_eq_u64 vcc, 0\n" \
"s_cselect_b32 s71, 1, 0\n" \
"s_cmp_ge_u32 s66, 64\n" \
"s_cselect_b32 s71, 0, s71\n" \
"s_cmp_lg_u32 s71, 0\n" \
"s_cbranch_scc1 Wb_more%=\n" \
"s_waitcnt vmcnt(15)\n" \
"v_mfma_f32_16x16x32_bf16 v[208:211], a[0:3], v[64:67], 0\n" \
"v_mfma_f32_16x16x32_bf16 v[212:215], a[128:131], v[64:67], 0\n" \
"v_mfma_f32_16x16x32_bf16 v[216:219], a[4:7], v[64:67], 0\n" \
"v_mfma_f32_16x16x32_bf16 v[220:223], a[132:135], v[64:67], 0\n" \
"s_waitcnt vmcnt(14)\n" \
"v_mfma_f32_16x16x32_bf16 v[208:211], a[8:11], v[68:71], v[208:211]\n" \
"v_mfma_f32_16x16x32_bf16 v[212:215], a[136:139], v[68:71], v[212:215]\n" \
"v_mfma_f32_16x16x32_bf16 v[216:219], a[12:15], v[68:71], v[216:219]\n" \
"v_mfma_f32_16x16x32_bf16 v[220:223], a[140:143], v[68:71], v[220:223]\n" \
"s_waitcnt vmcnt(13)\n" \
"v_mfma_f32_16x16x32_bf16 v[208:211], a[16:19], v[72:75], v[208:211]\n" \
"v_mfma_f32_16x16x32_bf16 v[212:215], a[144:147], v[72:75], v[212:215]\n" \
"v_mfma_f32_16x16x32_bf16 v[216:219], a[20:23], v[72:75], v[216:219]\n" \
"v_mfma_f32_16x16x32_bf16 v[220:223], a[148:151], v[72:75], v[220:223]\n" \
"s_waitcnt vmcnt(12)\n" \
"v_mfma_f32_16x16x32_bf16 v[208:211], a[24:27], v[76:79], v[208:211]\n" \
"v_mfma_f32_16x16x32_bf16 v[212:215], a[152:155], v[76:79], v[212:215]\n" \
"v_mfma_f32_16x16x32_bf16 v[216:219], a[28:31], v[76:79], v[216:219]\n" \
"v_mfma_f32_16x16x32_bf16 v[220:223], a[156:159], v[76:79], v[220:223]\n" \
"s_waitcnt vmcnt(11)\n" \
"v_mfma_f32_16x16x32_bf16 v[208:211], a[32:35], v[80:83], v[208:211]\n" \
"v_mfma_f32_16x16x32_bf16 v[212:215], a[160:163], v[80:83], v[212:215]\n" \
"v_mfma_f32_16x16x32_bf16 v[216:219], a[36:39], v[80:83], v[216:219]\n" \
"v_mfma_f32_16x16x32_bf16 v[220:223], a[164:167], v[80:83], v[220:223]\n" \
"s_waitcnt vmcnt(10)\n" \
"v_mfma_f32_16x16x32_bf16 v[208:211], a[40:43], v[84:87], v[208:211]\n" \
"v_mfma_f32_16x16x32_bf16 v[212:215], a[168:171], v[84:87], v[212:215]\n" \
"v_mfma_f32_16x16x32_bf16 v[216:219], a[44:47], v[84:87], v[216:219]\n" \
"v_mfma_f32_16x16x32_bf16 v[220:223], a[172:175], v[84:87], v[220:223]\n" \
"s_waitcnt vmcnt(9)\n" \
"v_mfma_f32_16x16x32_bf16 v[208:211], a[48:51], v[88:91], v[208:211]\n" \
"v_mfma_f32_16x16x32_bf16 v[212:215], a[176:179], v[88:91], v[212:215]\n" \
"v_mfma_f32_16x16x32_bf16 v[216:219], a[52:55], v[88:91], v[216:219]\n" \
"v_mfma_f32_16x16x32_bf16 v[220:223], a[180:183], v[88:91], v[220:223]\n" \
"s_waitcnt vmcnt(8)\n" \
"v_mfma_f32_16x16x32_bf16 v[208:211], a[56:59], v[92:95], v[208:211]\n" \
"v_mfma_f32_16x16x32_bf16 v[212:215], a[184:187], v[92:95], v[212:215]\n" \
"v_mfma_f32_16x16x32_bf16 v[216:219], a[60:63], v[92:95], v[216:219]\n" \
"v_mfma_f32_16x16x32_bf16 v[220:223], a[188:191], v[92:95], v[220:223]\n" \
"s_waitcnt vmcnt(7)\n" \
"v_mfma_f32_16x16x32_bf16 v[208:211], a[64:67], v[96:99], v[208:211]\n" \
"v_mfma_f32_16x16x32_bf16 v[212:215], a[192:195], v[96:99], v[212:215]\n" \
"v_mfma_f32_16x16x32_bf16 v[216:219], a[68:71], v[96:99], v[216:219]\n" \
"v_mfma_f32_16x16x32_bf16 v[220:223], a[196:199], v[96:99], v[220:223]\n" \
"s_waitcnt vmcnt(6)\n" \
"v_mfma_f32_16x16x32_bf16 v[208:211], a[72:75], v[100:103], v[208:211]\n" \
"v_mfma_f32_16x16x32_bf16 v[212:215], a[200:203], v[100:103], v[212:215]\n" \
"v_mfma_f32_16x16x32_bf16 v[216:219], a[76:79], v[100:103], v[216:219]\n" \
"v_mfma_f32_16x16x32_bf16 v[220:223], a[204:207], v[100:103], v[220:223]\n" \
"s_waitcnt vmcnt(5)\n" \
"v_mfma_f32_16x16x32_bf16 v[208:211], a[80:83], v[104:107], v[208:211]\n" \
"v_mfma_f32_16x16x32_bf16 v[212:215], a[208:211], v[104:107], v[212:215]\n" \
"v_mfma_f32_16x16x32_bf16 v[216:219], a[84:87], v[104:107], v[216:219]\n" \
"v_mfma_f32_16x16x32_bf16 v[220:223], a[212:215], v[104:107], v[220:223]\n" \
"s_waitcnt vmcnt(4)\n" \
"v_mfma_f32_16x16x32_bf16 v[208:211], a[88:91], v[108:111], v[208:211]\n" \
"v_mfma_f32_16x16x32_bf16 v[212:215], a[216:219], v[108:111], v[212:215]\n" \
"v_mfma_f32_16x16x32_bf16 v[216:219], a[92:95], v[108:111], v[216:219]\n" \
"v_mfma_f32_16x16x32_bf16 v[220:223], a[220:223], v[108:111], v[220:223]\n" \
"s_waitcnt vmcnt(3)\n" \
"v_mfma_f32_16x16x32_bf16 v[208:211], a[96:99], v[112:115], v[208:211]\n" \
"v_mfma_f32_16x16x32_bf16 v[212:215], a[224:227], v[112:115], v[212:215]\n" \
"v_mfma_f32_16x16x32_bf16 v[216:219], a[100:103], v[112:115], v[216:219]\n" \
"v_mfma_f32_16x16x32_bf16 v[220:223], a[228:231], v[112:115], v[220:223]\n" \
"s_waitcnt vmcnt(2)\n" \
"v_mfma_f32_16x16x32_bf16 v[208:211], a[104:107], v[116:119], v[208:211]\n" \
"v_mfma_f32_16x16x32_bf16 v[212:215], a[232:235], v[116:119], v[212:215]\n" \
"v_mfma_f32_16x16x32_bf16 v[216:219], a[108:111], v[116:119], v[216:219]\n" \
"v_mfma_f32_16x16x32_bf16 v[220:223], a[236:239], v[116:119], v[220:223]\n" \
"s_waitcnt vmcnt(1)\n" \
"v_mfma_f32_16x16x32_bf16 v[208:211], a[112:115], v[120:123], v[208:211]\n" \
"v_mfma_f32_16x16x32_bf16 v[212:215], a[240:243], v[120:123], v[212:215]\n" \
"v_mfma_f32_16x16x32_bf16 v[216:219], a[116:119], v[120:123], v[216:219]\n" \
"v_mfma_f32_16x16x32_bf16 v[220:223], a[244:247], v[120:123], v[220:223]\n" \
"s_waitcnt vmcnt(0)\n" \
"v_mfma_f32_16x16x32_bf16 v[208:211], a[120:123], v[124:127], v[208:211]\n" \
"v_mfma_f32_16x16x32_bf16 v[212:215], a[248:251], v[124:127], v[212:215]\n" \
"v_mfma_f32_16x16x32_bf16 v[216:219], a[124:127], v[124:127], v[216:219]\n" \
"v_mfma_f32_16x16x32_bf16 v[220:223], a[252:255], v[124:127], v[220:223]\n" \
"s_nop 7\n" \
"s_branch Wb_red%=\n" \
"Wb_more%=:\n" \
"s_cmp_lg_u32 s64, 0\n" \
"s_cbranch_scc1 Wb_mor2%=\n" \
"s_add_u32 s68, s66, 1\n" \
"v_mov_b32 v61, s68\n" \
"ds_write_b32 v21, v61\n" \
"Wb_mor2%=:\n" \
"buffer_load_dwordx2 v[40:41], v16, s[60:63], s75 offen\n" \
"s_add_u32 s68, s75, 67108864\n" \
"buffer_load_dwordx2 v[42:43], v16, s[40:43], s68 offen sc1\n" \
"s_add_u32 s68, s74, 2048\n" \
"buffer_load_dword v60, v8, s[40:43], s68 offen sc1\n" \
"s_waitcnt vmcnt(18)\n" \
"v_mfma_f32_16x16x32_bf16 v[208:211], a[0:3], v[64:67], 0\n" \
"v_mfma_f32_16x16x32_bf16 v[212:215], a[128:131], v[64:67], 0\n" \
"v_mfma_f32_16x16x32_bf16 v[216:219], a[4:7], v[64:67], 0\n" \
"v_mfma_f32_16x16x32_bf16 v[220:223], a[132:135], v[64:67], 0\n" \
"s_add_u32 s67, s73, 0\n" \
"buffer_load_dwordx4 v[64:67], v15, s[40:43], s67 offen offset:0 sc1\n" \
"s_waitcnt vmcnt(18)\n" \
"v_mfma_f32_16x16x32_bf16 v[208:211], a[8:11], v[68:71], v[208:211]\n" \
"v_mfma_f32_16x16x32_bf16 v[212:215], a[136:139], v[68:71], v[212:215]\n" \
"v_mfma_f32_16x16x32_bf16 v[216:219], a[12:15], v[68:71], v[216:219]\n" \
"v_mfma_f32_16x16x32_bf16 v[220:223], a[140:143], v[68:71], v[220:223]\n" \
"buffer_load_dwordx4 v[68:71], v15, s[40:43], s67 offen offset:2048 sc1\n" \
"s_waitcnt vmcnt(18)\n" \
"v_mfma_f32_16x16x32_bf16 v[208:211], a[16:19], v[72:75], v[208:211]\n" \
"v_mfma_f32_16x16x32_bf16 v[212:215], a[144:147], v[72:75], v[212:215]\n" \
"v_mfma_f32_16x16x32_bf16 v[216:219], a[20:23], v[72:75], v[216:219]\n" \
"v_mfma_f32_16x16x32_bf16 v[220:223], a[148:151], v[72:75], v[220:223]\n" \
"s_add_u32 s67, s73, 4096\n" \
"buffer_load_dwordx4 v[72:75], v15, s[40:43], s67 offen offset:0 sc1\n" \
"s_waitcnt vmcnt(18)\n" \
"v_mfma_f32_16x16x32_bf16 v[208:211], a[24:27], v[76:79], v[208:211]\n" \
"v_mfma_f32_16x16x32_bf16 v[212:215], a[152:155], v[76:79], v[212:215]\n" \
"v_mfma_f32_16x16x32_bf16 v[216:219], a[28:31], v[76:79], v[216:219]\n" \
"v_mfma_f32_16x16x32_bf16 v[220:223], a[156:159], v[76:79], v[220:223]\n" \
"buffer_load_dwordx4 v[76:79], v15, s[40:43], s67 offen offset:2048 sc1\n" \
"s_waitcnt vmcnt(18)\n" \
"v_mfma_f32_16x16x32_bf16 v[208:211], a[32:35], v[80:83], v[208:211]\n" \
"v_mfma_f32_16x16x32_bf16 v[212:215], a[160:163], v[80:83], v[212:215]\n" \
"v_mfma_f32_16x16x32_bf16 v[216:219], a[36:39], v[80:83], v[216:219]\n" \
"v_mfma_f32_16x16x32_bf16 v[220:223], a[164:167], v[80:83], v[220:223]\n" \
"s_add_u32 s67, s73, 8192\n" \
"buffer_load_dwordx4 v[80:83], v15, s[40:43], s67 offen offset:0 sc1\n" \
"s_waitcnt vmcnt(18)\n" \
"v_mfma_f32_16x16x32_bf16 v[208:211], a[40:43], v[84:87], v[208:211]\n" \
"v_mfma_f32_16x16x32_bf16 v[212:215], a[168:171], v[84:87], v[212:215]\n" \
"v_mfma_f32_16x16x32_bf16 v[216:219], a[44:47], v[84:87], v[216:219]\n" \
"v_mfma_f32_16x16x32_bf16 v[220:223], a[172:175], v[84:87], v[220:223]\n" \
"buffer_load_dwordx4 v[84:87], v15, s[40:43], s67 offen offset:2048 sc1\n" \
"s_waitcnt vmcnt(18)\n" \
"v_mfma_f32_16x16x32_bf16 v[208:211], a[48:51], v[88:91], v[208:211]\n" \
"v_mfma_f32_16x16x32_bf16 v[212:215], a[176:179], v[88:91], v[212:215]\n" \
"v_mfma_f32_16x16x32_bf16 v[216:219], a[52:55], v[88:91], v[216:219]\n" \
"v_mfma_f32_16x16x32_bf16 v[220:223], a[180:183], v[88:91], v[220:223]\n" \
"s_add_u32 s67, s73, 12288\n" \
"buffer_load_dwordx4 v[88:91], v15, s[40:43], s67 offen offset:0 sc1\n" \
"s_waitcnt vmcnt(18)\n" \
"v_mfma_f32_16x16x32_bf16 v[208:211], a[56:59], v[92:95], v[208:211]\n" \
"v_mfma_f32_16x16x32_bf16 v[212:215], a[184:187], v[92:95], v[212:215]\n" \
"v_mfma_f32_16x16x32_bf16 v[216:219], a[60:63], v[92:95], v[216:219]\n" \
"v_mfma_f32_16x16x32_bf16 v[220:223], a[188:191], v[92:95], v[220:223]\n" \
"buffer_load_dwordx4 v[92:95], v15, s[40:43], s67 offen offset:2048 sc1\n" \
"s_waitcnt vmcnt(18)\n" \
"v_mfma_f32_16x16x32_bf16 v[208:211], a[64:67], v[96:99], v[208:211]\n" \
"v_mfma_f32_16x16x32_bf16 v[212:215], a[192:195], v[96:99], v[212:215]\n" \
"v_mfma_f32_16x16x32_bf16 v[216:219], a[68:71], v[96:99], v[216:219]\n" \
"v_mfma_f32_16x16x32_bf16 v[220:223], a[196:199], v[96:99], v[220:223]\n" \
"s_add_u32 s67, s73, 16384\n" \
"buffer_load_dwordx4 v[96:99], v15, s[40:43], s67 offen offset:0 sc1\n" \
"s_waitcnt vmcnt(18)\n" \
"v_mfma_f32_16x16x32_bf16 v[208:211], a[72:75], v[100:103], v[208:211]\n" \
"v_mfma_f32_16x16x32_bf16 v[212:215], a[200:203], v[100:103], v[212:215]\n" \
"v_mfma_f32_16x16x32_bf16 v[216:219], a[76:79], v[100:103], v[216:219]\n" \
"v_mfma_f32_16x16x32_bf16 v[220:223], a[204:207], v[100:103], v[220:223]\n" \
"buffer_load_dwordx4 v[100:103], v15, s[40:43], s67 offen offset:2048 sc1\n" \
"s_waitcnt vmcnt(18)\n" \
"v_mfma_f32_16x16x32_bf16 v[208:211], a[80:83], v[104:107], v[208:211]\n" \
"v_mfma_f32_16x16x32_bf16 v[212:215], a[208:211], v[104:107], v[212:215]\n" \
"v_mfma_f32_16x16x32_bf16 v[216:219], a[84:87], v[104:107], v[216:219]\n" \
"v_mfma_f32_16x16x32_bf16 v[220:223], a[212:215], v[104:107], v[220:223]\n" \
"s_add_u32 s67, s73, 20480\n" \
"buffer_load_dwordx4 v[104:107], v15, s[40:43], s67 offen offset:0 sc1\n" \
"s_waitcnt vmcnt(18)\n" \
"v_mfma_f32_16x16x32_bf16 v[208:211], a[88:91], v[108:111], v[208:211]\n" \
"v_mfma_f32_16x16x32_bf16 v[212:215], a[216:219], v[108:111], v[212:215]\n" \
"v_mfma_f32_16x16x32_bf16 v[216:219], a[92:95], v[108:111], v[216:219]\n" \
"v_mfma_f32_16x16x32_bf16 v[220:223], a[220:223], v[108:111], v[220:223]\n" \
"buffer_load_dwordx4 v[108:111], v15, s[40:43], s67 offen offset:2048 sc1\n" \
"s_waitcnt vmcnt(18)\n" \
"v_mfma_f32_16x16x32_bf16 v[208:211], a[96:99], v[112:115], v[208:211]\n" \
"v_mfma_f32_16x16x32_bf16 v[212:215], a[224:227], v[112:115], v[212:215]\n" \
"v_mfma_f32_16x16x32_bf16 v[216:219], a[100:103], v[112:115], v[216:219]\n" \
"v_mfma_f32_16x16x32_bf16 v[220:223], a[228:231], v[112:115], v[220:223]\n" \
"s_add_u32 s67, s73, 24576\n" \
"buffer_load_dwordx4 v[112:115], v15, s[40:43], s67 offen offset:0 sc1\n" \
"s_waitcnt vmcnt(18)\n" \
"v_mfma_f32_16x16x32_bf16 v[208:211], a[104:107], v[116:119], v[208:211]\n" \
"v_mfma_f32_16x16x32_bf16 v[212:215], a[232:235], v[116:119], v[212:215]\n" \
"v_mfma_f32_16x16x32_bf16 v[216:219], a[108:111], v[116:119], v[216:219]\n" \
"v_mfma_f32_16x16x32_bf16 v[220:223], a[236:239], v[116:119], v[220:223]\n" \
"buffer_load_dwordx4 v[116:119], v15, s[40:43], s67 offen offset:2048 sc1\n" \
"s_waitcnt vmcnt(18)\n" \
"v_mfma_f32_16x16x32_bf16 v[208:211], a[112:115], v[120:123], v[208:211]\n" \
"v_mfma_f32_16x16x32_bf16 v[212:215], a[240:243], v[120:123], v[212:215]\n" \
"v_mfma_f32_16x16x32_bf16 v[216:219], a[116:119], v[120:123], v[216:219]\n" \
"v_mfma_f32_16x16x32_bf16 v[220:223], a[244:247], v[120:123], v[220:223]\n" \
"s_add_u32 s67, s73, 28672\n" \
"buffer_load_dwordx4 v[120:123], v15, s[40:43], s67 offen offset:0 sc1\n" \
"s_waitcnt vmcnt(18)\n" \
"v_mfma_f32_16x16x32_bf16 v[208:211], a[120:123], v[124:127], v[208:211]\n" \
"v_mfma_f32_16x16x32_bf16 v[212:215], a[248:251], v[124:127], v[212:215]\n" \
"v_mfma_f32_16x16x32_bf16 v[216:219], a[124:127], v[124:127], v[216:219]\n" \
"v_mfma_f32_16x16x32_bf16 v[220:223], a[252:255], v[124:127], v[220:223]\n" \
"buffer_load_dwordx4 v[124:127], v15, s[40:43], s67 offen offset:2048 sc1\n" \
"s_nop 6\n" \
"Wb_red%=:\n" \
"v_add_f32_dpp v208, v216, v208 row_ror:8 row_mask:0xf bank_mask:0xf\n" \
"v_add_f32_dpp v209, v217, v209 row_ror:8 row_mask:0xf bank_mask:0xf\n" \
"v_add_f32_dpp v210, v218, v210 row_ror:8 row_mask:0xf bank_mask:0xf\n" \
"v_add_f32_dpp v211, v219, v211 row_ror:8 row_mask:0xf bank_mask:0xf\n" \
"v_add_f32_dpp v212, v220, v212 row_ror:8 row_mask:0xf bank_mask:0xf\n" \
"v_add_f32_dpp v213, v221, v213 row_ror:8 row_mask:0xf bank_mask:0xf\n" \
"v_add_f32_dpp v214, v222, v214 row_ror:8 row_mask:0xf bank_mask:0xf\n" \
"v_add_f32_dpp v215, v223, v215 row_ror:8 row_mask:0xf bank_mask:0xf\n" \
"ds_write_b128 v22, v[208:211] offset:0\n" \
"ds_write_b128 v22, v[212:215] offset:2048\n" \
"v_xor_b32 v22, 32768, v22\n" \
"s_waitcnt lgkmcnt(0)\n" \
"s_barrier \n" \
"s_cmp_gt_u32 s64, 1\n" \
"s_cbranch_scc1 Wb_won%=\n" \
"ds_read_b128 v[192:195], v23 offset:0\n" \
"ds_read_b128 v[196:199], v23 offset:8192\n" \
"ds_read_b128 v[200:203], v23 offset:16384\n" \
"ds_read_b128 v[204:207], v23 offset:24576\n" \
"v_xor_b32 v23, 32768, v23\n" \
"s_waitcnt lgkmcnt(2)\n" \
"v_add_f32 v192, v192, v196\n" \
"v_add_f32 v193, v193, v197\n" \
"v_add_f32 v194, v194, v198\n" \
"v_add_f32 v195, v195, v199\n" \
"s_waitcnt lgkmcnt(1)\n" \
"v_add_f32 v192, v192, v200\n" \
"v_add_f32 v193, v193, v201\n" \
"v_add_f32 v194, v194, v202\n" \
"v_add_f32 v195, v195, v203\n" \
"s_waitcnt lgkmcnt(0)\n" \
"v_add_f32 v192, v192, v204\n" \
"v_add_f32 v193, v193, v205\n" \
"v_add_f32 v194, v194, v206\n" \
"v_add_f32 v195, v195, v207\n" \
"v_fma_f32 v192, v192, v52, v48\n" \
"v_fma_f32 v193, v193, v53, v49\n" \
"v_fma_f32 v194, v194, v54, v50\n" \
"v_fma_f32 v195, v195, v55, v51\n" \
"v_cvt_pk_bf16_f32 v56, v192, v193\n" \
"v_cvt_pk_bf16_f32 v57, v194, v195\n" \
"buffer_store_dwordx2 v[56:57], v16, s[88:91], s72 offen\n" \
"s_nop 0\n" \
"Wb_won%=:\n" \
"s_cmp_lg_u32 s71, 0\n" \
"s_cbranch_scc1 Wb_hot%=\n" \
"s_cmp_lt_u32 s66, 64\n" \
"s_cbranch_scc1 Wb_wo%=\n" \
"s_waitcnt vmcnt(0)\n" \
"s_branch ROLE_END%=\n" \
"ROLE_A%=:\n" \
"s_mov_b64 s[86:87], s[68:69]\n" \
"ds_read_b32 v1, v0 offset:0\n" \
"ds_read_b32 v2, v0 offset:1024\n" \
"ds_read_b32 v3, v0 offset:2048\n" \
"ds_read_b32 v4, v0 offset:3072\n" \
"ds_read_b32 v5, v0 offset:4096\n" \
"ds_read_b32 v6, v0 offset:5120\n" \
"ds_read_b32 v7, v0 offset:6144\n" \
"ds_read_b32 v8, v0 offset:7168\n" \
"ds_read_b32 v9, v0 offset:8192\n" \
"ds_read_b32 v10, v0 offset:9216\n" \
"ds_read_b32 v11, v0 offset:10240\n" \
"ds_read_b32 v12, v0 offset:11264\n" \
"ds_read_b32 v13, v0 offset:12288\n" \
"ds_read_b32 v16, v0 offset:13312\n" \
"ds_read_b32 v17, v0 offset:14336\n" \
"ds_read_b32 v18, v0 offset:15360\n" \
"ds_read_b32 v19, v0 offset:16384\n" \
"s_waitcnt lgkmcnt(0)\n" \
"s_barrier \n" \
"v_mov_b32 v20, 1\n" \
"v_add_u32 v14, 16384, v1\n" \
"s_mul_i32 s68, s64, 28672\n" \
"s_add_u32 s68, s68, 49152\n" \
"s_lshl_b32 s69, s65, 15\n" \
"s_lshl_b32 s70, s64, 13\n" \
"s_add_u32 s70, s70, 16384\n" \
"s_mov_b32 s82, 32768\n" \
"s_lshl_b32 s71, s65, 19\n" \
"s_lshl_b32 s72, s65, 11\n" \
"s_lshl_b32 s75, s65, 16\n" \
"s_lshl_b32 s76, s65, 18\n" \
"s_lshl_b32 s77, s65, 11\n" \
"s_mov_b32 s73, 81920\n" \
"s_mov_b32 s74, 81920\n" \
"s_mov_b32 s66, 0\n" \
"s_mov_b32 s78, 0\n" \
"s_mov_b32 s80, 0x3e0293ee\n" \
"s_mov_b32 s81, 0xbe0293ee\n" \
"s_add_u32 m0, s70, 0\n" \
"s_nop 0\n" \
"buffer_load_dwordx4 v2, s[40:43], s69 offen offset:0 lds\n" \
"buffer_load_dwordx4 v2, s[40:43], s69 offen offset:1024 lds\n" \
"buffer_load_dwordx4 v2, s[40:43], s69 offen offset:2048 lds\n" \
"buffer_load_dwordx4 v2, s[40:43], s69 offen offset:3072 lds\n" \
"s_add_u32 m0, s70, 4096\n" \
"s_add_u32 s67, s69, 4096\n" \
"buffer_load_dwordx4 v2, s[40:43], s67 offen offset:0 lds\n" \
"buffer_load_dwordx4 v2, s[40:43], s67 offen offset:1024 lds\n" \
"buffer_load_dwordx4 v2, s[40:43], s67 offen offset:2048 lds\n" \
"buffer_load_dwordx4 v2, s[40:43], s67 offen offset:3072 lds\n" \
"s_add_u32 s69, s69, 65536\n" \
"s_add_u32 s70, s70, s82\n" \
"s_sub_u32 s82, 0, s82\n" \
"s_waitcnt vmcnt(34)\n" \
"ds_read_b64_tr_b16 a[128:129], v13 offset:0\n" \
"ds_read_b64_tr_b16 a[130:131], v13 offset:256\n" \
"ds_read_b64_tr_b16 a[192:193], v13 offset:8\n" \
"ds_read_b64_tr_b16 a[194:195], v13 offset:264\n" \
"s_waitcnt vmcnt(32)\n" \
"ds_read_b64_tr_b16 a[132:133], v13 offset:2048\n" \
"ds_read_b64_tr_b16 a[134:135], v13 offset:2304\n" \
"ds_read_b64_tr_b16 a[196:197], v13 offset:2056\n" \
"ds_read_b64_tr_b16 a[198:199], v13 offset:2312\n" \
"s_waitcnt lgkmcnt(4)\n" \
"s_add_u32 m0, s68, 0\n" \
"s_mov_b32 s67, 3670016\n" \
"buffer_load_dwordx4 v12, s[52:55], s67 offen lds\n" \
"s_add_u32 m0, s68, 1024\n" \
"s_mov_b32 s67, 3801088\n" \
"buffer_load_dwordx4 v12, s[52:55], s67 offen lds\n" \
"s_waitcnt vmcnt(32)\n" \
"ds_read_b64_tr_b16 a[136:137], v13 offset:4096\n" \
"ds_read_b64_tr_b16 a[138:139], v13 offset:4352\n" \
"ds_read_b64_tr_b16 a[200:201], v13 offset:4104\n" \
"ds_read_b64_tr_b16 a[202:203], v13 offset:4360\n" \
"s_waitcnt lgkmcnt(4)\n" \
"s_add_u32 m0, s68, 2048\n" \
"s_mov_b32 s67, 3932160\n" \
"buffer_load_dwordx4 v12, s[52:55], s67 offen lds\n" \
"s_add_u32 m0, s68, 3072\n" \
"s_mov_b32 s67, 4063232\n" \
"buffer_load_dwordx4 v12, s[52:55], s67 offen lds\n" \
"s_waitcnt vmcnt(32)\n" \
"ds_read_b64_tr_b16 a[140:141], v13 offset:6144\n" \
"ds_read_b64_tr_b16 a[142:143], v13 offset:6400\n" \
"ds_read_b64_tr_b16 a[204:205], v13 offset:6152\n" \
"ds_read_b64_tr_b16 a[206:207], v13 offset:6408\n" \
"s_waitcnt lgkmcnt(4)\n" \
"s_add_u32 m0, s68, 4096\n" \
"s_mov_b32 s67, 0\n" \
"buffer_load_dwordx4 v12, s[56:59], s67 offen lds\n" \
"s_add_u32 m0, s68, 5120\n" \
"s_mov_b32 s67, 131072\n" \
"buffer_load_dwordx4 v12, s[56:59], s67 offen lds\n" \
"s_waitcnt vmcnt(32)\n" \
"ds_read_b64_tr_b16 a[144:145], v13 offset:8192\n" \
"ds_read_b64_tr_b16 a[146:147], v13 offset:8448\n" \
"ds_read_b64_tr_b16 a[208:209], v13 offset:8200\n" \
"ds_read_b64_tr_b16 a[210:211], v13 offset:8456\n" \
"s_waitcnt lgkmcnt(4)\n" \
"s_add_u32 m0, s68, 6144\n" \
"s_mov_b32 s67, 262144\n" \
"buffer_load_dwordx4 v12, s[56:59], s67 offen lds\n" \
"s_add_u32 m0, s68, 7168\n" \
"s_mov_b32 s67, 393216\n" \
"buffer_load_dwordx4 v12, s[56:59], s67 offen lds\n" \
"s_waitcnt vmcnt(32)\n" \
"ds_read_b64_tr_b16 a[148:149], v13 offset:10240\n" \
"ds_read_b64_tr_b16 a[150:151], v13 offset:10496\n" \
"ds_read_b64_tr_b16 a[212:213], v13 offset:10248\n" \
"ds_read_b64_tr_b16 a[214:215], v13 offset:10504\n" \
"s_waitcnt lgkmcnt(4)\n" \
"s_add_u32 m0, s68, 8192\n" \
"s_mov_b32 s67, 524288\n" \
"buffer_load_dwordx4 v12, s[56:59], s67 offen lds\n" \
"s_add_u32 m0, s68, 9216\n" \
"s_mov_b32 s67, 655360\n" \
"buffer_load_dwordx4 v12, s[56:59], s67 offen lds\n" \
"s_waitcnt vmcnt(32)\n" \
"ds_read_b64_tr_b16 a[152:153], v13 offset:12288\n" \
"ds_read_b64_tr_b16 a[154:155], v13 offset:12544\n" \
"ds_read_b64_tr_b16 a[216:217], v13 offset:12296\n" \
"ds_read_b64_tr_b16 a[218:219], v13 offset:12552\n" \
"s_waitcnt lgkmcnt(4)\n" \
"s_add_u32 m0, s68, 10240\n" \
"s_mov_b32 s67, 786432\n" \
"buffer_load_dwordx4 v12, s[56:59], s67 offen lds\n" \
"s_add_u32 m0, s68, 11264\n" \
"s_mov_b32 s67, 917504\n" \
"buffer_load_dwordx4 v12, s[56:59], s67 offen lds\n" \
"s_waitcnt vmcnt(32)\n" \
"ds_read_b64_tr_b16 a[156:157], v13 offset:14336\n" \
"ds_read_b64_tr_b16 a[158:159], v13 offset:14592\n" \
"ds_read_b64_tr_b16 a[220:221], v13 offset:14344\n" \
"ds_read_b64_tr_b16 a[222:223], v13 offset:14600\n" \
"s_waitcnt lgkmcnt(4)\n" \
"s_add_u32 m0, s68, 12288\n" \
"s_mov_b32 s67, 1048576\n" \
"buffer_load_dwordx4 v12, s[56:59], s67 offen lds\n" \
"s_add_u32 m0, s68, 13312\n" \
"s_mov_b32 s67, 1179648\n" \
"buffer_load_dwordx4 v12, s[56:59], s67 offen lds\n" \
"s_waitcnt vmcnt(32)\n" \
"ds_read_b64_tr_b16 a[160:161], v13 offset:16384\n" \
"ds_read_b64_tr_b16 a[162:163], v13 offset:16640\n" \
"ds_read_b64_tr_b16 a[224:225], v13 offset:16392\n" \
"ds_read_b64_tr_b16 a[226:227], v13 offset:16648\n" \
"s_waitcnt lgkmcnt(4)\n" \
"s_add_u32 m0, s68, 14336\n" \
"s_mov_b32 s67, 1310720\n" \
"buffer_load_dwordx4 v12, s[56:59], s67 offen lds\n" \
"s_add_u32 m0, s68, 15360\n" \
"s_mov_b32 s67, 1441792\n" \
"buffer_load_dwordx4 v12, s[56:59], s67 offen lds\n" \
"s_waitcnt vmcnt(32)\n" \
"ds_read_b64_tr_b16 a[164:165], v13 offset:18432\n" \
"ds_read_b64_tr_b16 a[166:167], v13 offset:18688\n" \
"ds_read_b64_tr_b16 a[228:229], v13 offset:18440\n" \
"ds_read_b64_tr_b16 a[230:231], v13 offset:18696\n" \
"s_waitcnt lgkmcnt(4)\n" \
"s_add_u32 m0, s68, 16384\n" \
"s_mov_b32 s67, 1572864\n" \
"buffer_load_dwordx4 v12, s[56:59], s67 offen lds\n" \
"s_add_u32 m0, s68, 17408\n" \
"s_mov_b32 s67, 1703936\n" \
"buffer_load_dwordx4 v12, s[56:59], s67 offen lds\n" \
"s_waitcnt vmcnt(32)\n" \
"ds_read_b64_tr_b16 a[168:169], v13 offset:20480\n" \
"ds_read_b64_tr_b16 a[170:171], v13 offset:20736\n" \
"ds_read_b64_tr_b16 a[232:233], v13 offset:20488\n" \
"ds_read_b64_tr_b16 a[234:235], v13 offset:20744\n" \
"s_waitcnt lgkmcnt(4)\n" \
"s_add_u32 m0, s68, 18432\n" \
"s_mov_b32 s67, 1835008\n" \
"buffer_load_dwordx4 v12, s[56:59], s67 offen lds\n" \
"s_add_u32 m0, s68, 19456\n" \
"s_mov_b32 s67, 1966080\n" \
"buffer_load_dwordx4 v12, s[56:59], s67 offen lds\n" \
"s_waitcnt vmcnt(32)\n" \
"ds_read_b64_tr_b16 a[172:173], v13 offset:22528\n" \
"ds_read_b64_tr_b16 a[174:175], v13 offset:22784\n" \
"ds_read_b64_tr_b16 a[236:237], v13 offset:22536\n" \
"ds_read_b64_tr_b16 a[238:239], v13 offset:22792\n" \
"s_waitcnt lgkmcnt(4)\n" \
"s_add_u32 m0, s68, 20480\n" \
"s_mov_b32 s67, 2097152\n" \
"buffer_load_dwordx4 v12, s[56:59], s67 offen lds\n" \
"s_add_u32 m0, s68, 21504\n" \
"s_mov_b32 s67, 2228224\n" \
"buffer_load_dwordx4 v12, s[56:59], s67 offen lds\n" \
"s_waitcnt vmcnt(32)\n" \
"ds_read_b64_tr_b16 a[176:177], v13 offset:24576\n" \
"ds_read_b64_tr_b16 a[178:179], v13 offset:24832\n" \
"ds_read_b64_tr_b16 a[240:241], v13 offset:24584\n" \
"ds_read_b64_tr_b16 a[242:243], v13 offset:24840\n" \
"s_waitcnt lgkmcnt(4)\n" \
"s_add_u32 m0, s68, 22528\n" \
"s_mov_b32 s67, 2359296\n" \
"buffer_load_dwordx4 v12, s[56:59], s67 offen lds\n" \
"s_add_u32 m0, s68, 23552\n" \
"s_mov_b32 s67, 2490368\n" \
"buffer_load_dwordx4 v12, s[56:59], s67 offen lds\n" \
"s_waitcnt vmcnt(32)\n" \
"ds_read_b64_tr_b16 a[180:181], v13 offset:26624\n" \
"ds_read_b64_tr_b16 a[182:183], v13 offset:26880\n" \
"ds_read_b64_tr_b16 a[244:245], v13 offset:26632\n" \
"ds_read_b64_tr_b16 a[246:247], v13 offset:26888\n" \
"s_waitcnt lgkmcnt(4)\n" \
"s_add_u32 m0, s68, 24576\n" \
"s_mov_b32 s67, 2621440\n" \
"buffer_load_dwordx4 v12, s[56:59], s67 offen lds\n" \
"s_add_u32 m0, s68, 25600\n" \
"s_mov_b32 s67, 2752512\n" \
"buffer_load_dwordx4 v12, s[56:59], s67 offen lds\n" \
"s_waitcnt vmcnt(24)\n" \
"ds_read_b64_tr_b16 a[184:185], v13 offset:0\n" \
"ds_read_b64_tr_b16 a[186:187], v13 offset:256\n" \
"ds_read_b64_tr_b16 a[248:249], v13 offset:8\n" \
"ds_read_b64_tr_b16 a[250:251], v13 offset:264\n" \
"s_waitcnt lgkmcnt(4)\n" \
"s_add_u32 m0, s68, 26624\n" \
"s_mov_b32 s67, 2883584\n" \
"buffer_load_dwordx4 v12, s[56:59], s67 offen lds\n" \
"s_add_u32 m0, s68, 27648\n" \
"s_mov_b32 s67, 3014656\n" \
"buffer_load_dwordx4 v12, s[56:59], s67 offen lds\n" \
"s_waitcnt vmcnt(24)\n" \
"ds_read_b64_tr_b16 a[188:189], v13 offset:2048\n" \
"ds_read_b64_tr_b16 a[190:191], v13 offset:2304\n" \
"ds_read_b64_tr_b16 a[252:253], v13 offset:2056\n" \
"ds_read_b64_tr_b16 a[254:255], v13 offset:2312\n" \
"s_waitcnt lgkmcnt(4)\n" \
"s_add_u32 m0, s68, 0\n" \
"s_mov_b32 s67, 3145728\n" \
"buffer_load_dwordx4 v12, s[56:59], s67 offen lds\n" \
"s_add_u32 m0, s68, 1024\n" \
"s_mov_b32 s67, 3276800\n" \
"buffer_load_dwordx4 v12, s[56:59], s67 offen lds\n" \
"s_waitcnt vmcnt(24)\n" \
"ds_read_b64_tr_b16 v[128:129], v13 offset:4096\n" \
"ds_read_b64_tr_b16 v[130:131], v13 offset:4352\n" \
"ds_read_b64_tr_b16 v[192:193], v13 offset:4104\n" \
"ds_read_b64_tr_b16 v[194:195], v13 offset:4360\n" \
"s_waitcnt lgkmcnt(4)\n" \
"s_add_u32 m0, s68, 2048\n" \
"s_mov_b32 s67, 3407872\n" \
"buffer_load_dwordx4 v12, s[56:59], s67 offen lds\n" \
"s_add_u32 m0, s68, 3072\n" \
"s_mov_b32 s67, 3538944\n" \
"buffer_load_dwordx4 v12, s[56:59], s67 offen lds\n" \
"s_waitcnt vmcnt(24)\n" \
"ds_read_b64_tr_b16 v[132:133], v13 offset:6144\n" \
"ds_read_b64_tr_b16 v[134:135], v13 offset:6400\n" \
"ds_read_b64_tr_b16 v[196:197], v13 offset:6152\n" \
"ds_read_b64_tr_b16 v[198:199], v13 offset:6408\n" \
"s_waitcnt lgkmcnt(4)\n" \
"s_add_u32 m0, s68, 4096\n" \
"s_mov_b32 s67, 3670016\n" \
"buffer_load_dwordx4 v12, s[56:59], s67 offen lds\n" \
"s_add_u32 m0, s68, 5120\n" \
"s_mov_b32 s67, 3801088\n" \
"buffer_load_dwordx4 v12, s[56:59], s67 offen lds\n" \
"s_waitcnt vmcnt(24)\n" \
"ds_read_b64_tr_b16 v[136:137], v13 offset:8192\n" \
"ds_read_b64_tr_b16 v[138:139], v13 offset:8448\n" \
"ds_read_b64_tr_b16 v[200:201], v13 offset:8200\n" \
"ds_read_b64_tr_b16 v[202:203], v13 offset:8456\n" \
"s_waitcnt lgkmcnt(4)\n" \
"s_add_u32 m0, s68, 6144\n" \
"s_mov_b32 s67, 3932160\n" \
"buffer_load_dwordx4 v12, s[56:59], s67 offen lds\n" \
"s_add_u32 m0, s68, 7168\n" \
"s_mov_b32 s67, 4063232\n" \
"buffer_load_dwordx4 v12, s[56:59], s67 offen lds\n" \
"s_waitcnt vmcnt(24)\n" \
"ds_read_b64_tr_b16 v[140:141], v13 offset:10240\n" \
"ds_read_b64_tr_b16 v[142:143], v13 offset:10496\n" \
"ds_read_b64_tr_b16 v[204:205], v13 offset:10248\n" \
"ds_read_b64_tr_b16 v[206:207], v13 offset:10504\n" \
"s_waitcnt lgkmcnt(4)\n" \
"s_add_u32 m0, s68, 8192\n" \
"s_mov_b32 s67, 0\n" \
"buffer_load_dwordx4 v12, s[48:51], s67 offen lds\n" \
"s_add_u32 m0, s68, 9216\n" \
"s_mov_b32 s67, 131072\n" \
"buffer_load_dwordx4 v12, s[48:51], s67 offen lds\n" \
"s_waitcnt vmcnt(24)\n" \
"ds_read_b64_tr_b16 v[144:145], v13 offset:12288\n" \
"ds_read_b64_tr_b16 v[146:147], v13 offset:12544\n" \
"ds_read_b64_tr_b16 v[208:209], v13 offset:12296\n" \
"ds_read_b64_tr_b16 v[210:211], v13 offset:12552\n" \
"s_waitcnt lgkmcnt(4)\n" \
"s_add_u32 m0, s68, 10240\n" \
"s_mov_b32 s67, 262144\n" \
"buffer_load_dwordx4 v12, s[48:51], s67 offen lds\n" \
"s_add_u32 m0, s68, 11264\n" \
"s_mov_b32 s67, 393216\n" \
"buffer_load_dwordx4 v12, s[48:51], s67 offen lds\n" \
"s_waitcnt vmcnt(24)\n" \
"ds_read_b64_tr_b16 v[148:149], v13 offset:14336\n" \
"ds_read_b64_tr_b16 v[150:151], v13 offset:14592\n" \
"ds_read_b64_tr_b16 v[212:213], v13 offset:14344\n" \
"ds_read_b64_tr_b16 v[214:215], v13 offset:14600\n" \
"s_waitcnt lgkmcnt(4)\n" \
"s_add_u32 m0, s68, 12288\n" \
"s_mov_b32 s67, 524288\n" \
"buffer_load_dwordx4 v12, s[48:51], s67 offen lds\n" \
"s_add_u32 m0, s68, 13312\n" \
"s_mov_b32 s67, 655360\n" \
"buffer_load_dwordx4 v12, s[48:51], s67 offen lds\n" \
"s_waitcnt vmcnt(24)\n" \
"ds_read_b64_tr_b16 v[152:153], v13 offset:16384\n" \
"ds_read_b64_tr_b16 v[154:155], v13 offset:16640\n" \
"ds_read_b64_tr_b16 v[216:217], v13 offset:16392\n" \
"ds_read_b64_tr_b16 v[218:219], v13 offset:16648\n" \
"s_waitcnt lgkmcnt(4)\n" \
"s_add_u32 m0, s68, 14336\n" \
"s_mov_b32 s67, 786432\n" \
"buffer_load_dwordx4 v12, s[48:51], s67 offen lds\n" \
"s_add_u32 m0, s68, 15360\n" \
"s_mov_b32 s67, 917504\n" \
"buffer_load_dwordx4 v12, s[48:51], s67 offen lds\n" \
"s_waitcnt vmcnt(24)\n" \
"ds_read_b64_tr_b16 v[156:157], v13 offset:18432\n" \
"ds_read_b64_tr_b16 v[158:159], v13 offset:18688\n" \
"ds_read_b64_tr_b16 v[220:221], v13 offset:18440\n" \
"ds_read_b64_tr_b16 v[222:223], v13 offset:18696\n" \
"s_waitcnt lgkmcnt(4)\n" \
"s_add_u32 m0, s68, 16384\n" \
"s_mov_b32 s67, 1048576\n" \
"buffer_load_dwordx4 v12, s[48:51], s67 offen lds\n" \
"s_add_u32 m0, s68, 17408\n" \
"s_mov_b32 s67, 1179648\n" \
"buffer_load_dwordx4 v12, s[48:51], s67 offen lds\n" \
"s_waitcnt vmcnt(24)\n" \
"ds_read_b64_tr_b16 v[160:161], v13 offset:20480\n" \
"ds_read_b64_tr_b16 v[162:163], v13 offset:20736\n" \
"ds_read_b64_tr_b16 v[224:225], v13 offset:20488\n" \
"ds_read_b64_tr_b16 v[226:227], v13 offset:20744\n" \
"s_waitcnt lgkmcnt(4)\n" \
"s_add_u32 m0, s68, 18432\n" \
"s_mov_b32 s67, 1310720\n" \
"buffer_load_dwordx4 v12, s[48:51], s67 offen lds\n" \
"s_add_u32 m0, s68, 19456\n" \
"s_mov_b32 s67, 1441792\n" \
"buffer_load_dwordx4 v12, s[48:51], s67 offen lds\n" \
"s_waitcnt vmcnt(24)\n" \
"ds_read_b64_tr_b16 v[164:165], v13 offset:22528\n" \
"ds_read_b64_tr_b16 v[166:167], v13 offset:22784\n" \
"ds_read_b64_tr_b16 v[228:229], v13 offset:22536\n" \
"ds_read_b64_tr_b16 v[230:231], v13 offset:22792\n" \
"s_waitcnt lgkmcnt(4)\n" \
"s_add_u32 m0, s68, 20480\n" \
"s_mov_b32 s67, 1572864\n" \
"buffer_load_dwordx4 v12, s[48:51], s67 offen lds\n" \
"s_add_u32 m0, s68, 21504\n" \
"s_mov_b32 s67, 1703936\n" \
"buffer_load_dwordx4 v12, s[48:51], s67 offen lds\n" \
"s_waitcnt vmcnt(24)\n" \
"ds_read_b64_tr_b16 v[168:169], v13 offset:24576\n" \
"ds_read_b64_tr_b16 v[170:171], v13 offset:24832\n" \
"ds_read_b64_tr_b16 v[232:233], v13 offset:24584\n" \
"ds_read_b64_tr_b16 v[234:235], v13 offset:24840\n" \
"s_waitcnt lgkmcnt(4)\n" \
"s_add_u32 m0, s68, 22528\n" \
"s_mov_b32 s67, 1835008\n" \
"buffer_load_dwordx4 v12, s[48:51], s67 offen lds\n" \
"s_add_u32 m0, s68, 23552\n" \
"s_mov_b32 s67, 1966080\n" \
"buffer_load_dwordx4 v12, s[48:51], s67 offen lds\n" \
"s_waitcnt vmcnt(24)\n" \
"ds_read_b64_tr_b16 v[172:173], v13 offset:26624\n" \
"ds_read_b64_tr_b16 v[174:175], v13 offset:26880\n" \
"ds_read_b64_tr_b16 v[236:237], v13 offset:26632\n" \
"ds_read_b64_tr_b16 v[238:239], v13 offset:26888\n" \
"s_waitcnt lgkmcnt(4)\n" \
"s_add_u32 m0, s68, 24576\n" \
"s_mov_b32 s67, 2097152\n" \
"buffer_load_dwordx4 v12, s[48:51], s67 offen lds\n" \
"s_add_u32 m0, s68, 25600\n" \
"s_mov_b32 s67, 2228224\n" \
"buffer_load_dwordx4 v12, s[48:51], s67 offen lds\n" \
"s_waitcnt vmcnt(24)\n" \
"ds_read_b64_tr_b16 v[176:177], v13 offset:0\n" \
"ds_read_b64_tr_b16 v[178:179], v13 offset:256\n" \
"ds_read_b64_tr_b16 v[240:241], v13 offset:8\n" \
"ds_read_b64_tr_b16 v[242:243], v13 offset:264\n" \
"s_waitcnt lgkmcnt(4)\n" \
"s_add_u32 m0, s68, 26624\n" \
"s_mov_b32 s67, 2359296\n" \
"buffer_load_dwordx4 v12, s[48:51], s67 offen lds\n" \
"s_add_u32 m0, s68, 27648\n" \
"s_mov_b32 s67, 2490368\n" \
"buffer_load_dwordx4 v12, s[48:51], s67 offen lds\n" \
"s_waitcnt vmcnt(24)\n" \
"ds_read_b64_tr_b16 v[180:181], v13 offset:2048\n" \
"ds_read_b64_tr_b16 v[182:183], v13 offset:2304\n" \
"ds_read_b64_tr_b16 v[244:245], v13 offset:2056\n" \
"ds_read_b64_tr_b16 v[246:247], v13 offset:2312\n" \
"s_waitcnt lgkmcnt(4)\n" \
"s_add_u32 m0, s68, 0\n" \
"s_mov_b32 s67, 2621440\n" \
"buffer_load_dwordx4 v12, s[48:51], s67 offen lds\n" \
"s_add_u32 m0, s68, 1024\n" \
"s_mov_b32 s67, 2752512\n" \
"buffer_load_dwordx4 v12, s[48:51], s67 offen lds\n" \
"s_waitcnt vmcnt(24)\n" \
"ds_read_b64_tr_b16 v[184:185], v13 offset:4096\n" \
"ds_read_b64_tr_b16 v[186:187], v13 offset:4352\n" \
"ds_read_b64_tr_b16 v[248:249], v13 offset:4104\n" \
"ds_read_b64_tr_b16 v[250:251], v13 offset:4360\n" \
"s_waitcnt lgkmcnt(4)\n" \
"s_add_u32 m0, s68, 2048\n" \
"s_mov_b32 s67, 2883584\n" \
"buffer_load_dwordx4 v12, s[48:51], s67 offen lds\n" \
"s_add_u32 m0, s68, 3072\n" \
"s_mov_b32 s67, 3014656\n" \
"buffer_load_dwordx4 v12, s[48:51], s67 offen lds\n" \
"s_waitcnt vmcnt(24)\n" \
"ds_read_b64_tr_b16 v[188:189], v13 offset:6144\n" \
"ds_read_b64_tr_b16 v[190:191], v13 offset:6400\n" \
"ds_read_b64_tr_b16 v[252:253], v13 offset:6152\n" \
"ds_read_b64_tr_b16 v[254:255], v13 offset:6408\n" \
"s_waitcnt lgkmcnt(4)\n" \
"s_add_u32 m0, s68, 4096\n" \
"s_mov_b32 s67, 3145728\n" \
"buffer_load_dwordx4 v12, s[48:51], s67 offen lds\n" \
"s_add_u32 m0, s68, 5120\n" \
"s_mov_b32 s67, 3276800\n" \
"buffer_load_dwordx4 v12, s[48:51], s67 offen lds\n" \
"s_waitcnt lgkmcnt(0)\n" \
"s_add_u32 m0, s68, 6144\n" \
"s_mov_b32 s67, 3407872\n" \
"buffer_load_dwordx4 v12, s[48:51], s67 offen lds\n" \
"s_add_u32 m0, s68, 7168\n" \
"s_mov_b32 s67, 3538944\n" \
"buffer_load_dwordx4 v12, s[48:51], s67 offen lds\n" \
"s_barrier \n" \
"ds_read_b128 v[40:43], v14 offset:0\n" \
"ds_read_b128 v[44:47], v14 offset:16384\n" \
"ds_read_b128 v[48:51], v14 offset:1024\n" \
"ds_read_b128 v[52:55], v14 offset:17408\n" \
"s_waitcnt lgkmcnt(3)\n" \
"v_mfma_f32_16x16x32_bf16 v[64:67], a[128:131], v[40:43], 0\n" \
"ds_read_b128 v[56:59], v14 offset:2048\n" \
"ds_read_b128 v[60:63], v14 offset:18432\n" \
"v_mfma_f32_16x16x32_bf16 v[68:71], a[192:195], v[40:43], 0\n" \
"v_mfma_f32_16x16x32_bf16 v[80:83], v[128:131], v[40:43], 0\n" \
"v_mfma_f32_16x16x32_bf16 v[84:87], v[192:195], v[40:43], 0\n" \
"s_waitcnt lgkmcnt(4)\n" \
"v_mfma_f32_16x16x32_bf16 v[72:75], a[128:131], v[44:47], 0\n" \
"v_mfma_f32_16x16x32_bf16 v[76:79], a[192:195], v[44:47], 0\n" \
"v_mfma_f32_16x16x32_bf16 v[88:91], v[128:131], v[44:47], 0\n" \
"v_mfma_f32_16x16x32_bf16 v[92:95], v[192:195], v[44:47], 0\n" \
"s_waitcnt lgkmcnt(3)\n" \
"v_mfma_f32_16x16x32_bf16 v[64:67], a[132:135], v[48:51], v[64:67]\n" \
"ds_read_b128 v[40:43], v14 offset:3072\n" \
"ds_read_b128 v[44:47], v14 offset:19456\n" \
"v_mfma_f32_16x16x32_bf16 v[68:71], a[196:199], v[48:51], v[68:71]\n" \
"v_mfma_f32_16x16x32_bf16 v[80:83], v[132:135], v[48:51], v[80:83]\n" \
"v_mfma_f32_16x16x32_bf16 v[84:87], v[196:199], v[48:51], v[84:87]\n" \
"s_waitcnt lgkmcnt(4)\n" \
"v_mfma_f32_16x16x32_bf16 v[72:75], a[132:135], v[52:55], v[72:75]\n" \
"v_mfma_f32_16x16x32_bf16 v[76:79], a[196:199], v[52:55], v[76:79]\n" \
"v_mfma_f32_16x16x32_bf16 v[88:91], v[132:135], v[52:55], v[88:91]\n" \
"v_mfma_f32_16x16x32_bf16 v[92:95], v[196:199], v[52:55], v[92:95]\n" \
"s_waitcnt lgkmcnt(3)\n" \
"v_mfma_f32_16x16x32_bf16 v[64:67], a[136:139], v[56:59], v[64:67]\n" \
"ds_read_b128 v[48:51], v14 offset:4096\n" \
"ds_read_b128 v[52:55], v14 offset:20480\n" \
"v_mfma_f32_16x16x32_bf16 v[68:71], a[200:203], v[56:59], v[68:71]\n" \
"v_mfma_f32_16x16x32_bf16 v[80:83], v[136:139], v[56:59], v[80:83]\n" \
"v_mfma_f32_16x16x32_bf16 v[84:87], v[200:203], v[56:59], v[84:87]\n" \
"s_waitcnt lgkmcnt(4)\n" \
"v_mfma_f32_16x16x32_bf16 v[72:75], a[136:139], v[60:63], v[72:75]\n" \
"v_mfma_f32_16x16x32_bf16 v[76:79], a[200:203], v[60:63], v[76:79]\n" \
"v_mfma_f32_16x16x32_bf16 v[88:91], v[136:139], v[60:63], v[88:91]\n" \
"v_mfma_f32_16x16x32_bf16 v[92:95], v[200:203], v[60:63], v[92:95]\n" \
"s_waitcnt lgkmcnt(3)\n" \
"v_mfma_f32_16x16x32_bf16 v[64:67], a[140:143], v[40:43], v[64:67]\n" \
"ds_read_b128 v[56:59], v14 offset:5120\n" \
"ds_read_b128 v[60:63], v14 offset:21504\n" \
"v_mfma_f32_16x16x32_bf16 v[68:71], a[204:207], v[40:43], v[68:71]\n" \
"v_mfma_f32_16x16x32_bf16 v[80:83], v[140:143], v[40:43], v[80:83]\n" \
"v_mfma_f32_16x16x32_bf16 v[84:87], v[204:207], v[40:43], v[84:87]\n" \
"s_waitcnt lgkmcnt(4)\n" \
"v_mfma_f32_16x16x32_bf16 v[72:75], a[140:143], v[44:47], v[72:75]\n" \
"v_mfma_f32_16x16x32_bf16 v[76:79], a[204:207], v[44:47], v[76:79]\n" \
"v_mfma_f32_16x16x32_bf16 v[88:91], v[140:143], v[44:47], v[88:91]\n" \
"v_mfma_f32_16x16x32_bf16 v[92:95], v[204:207], v[44:47], v[92:95]\n" \
"s_waitcnt lgkmcnt(3)\n" \
"v_mfma_f32_16x16x32_bf16 v[64:67], a[144:147], v[48:51], v[64:67]\n" \
"ds_read_b128 v[40:43], v14 offset:6144\n" \
"ds_read_b128 v[44:47], v14 offset:22528\n" \
"v_mfma_f32_16x16x32_bf16 v[68:71], a[208:211], v[48:51], v[68:71]\n" \
"v_mfma_f32_16x16x32_bf16 v[80:83], v[144:147], v[48:51], v[80:83]\n" \
"v_mfma_f32_16x16x32_bf16 v[84:87], v[208:211], v[48:51], v[84:87]\n" \
"s_waitcnt lgkmcnt(4)\n" \
"v_mfma_f32_16x16x32_bf16 v[72:75], a[144:147], v[52:55], v[72:75]\n" \
"v_mfma_f32_16x16x32_bf16 v[76:79], a[208:211], v[52:55], v[76:79]\n" \
"v_mfma_f32_16x16x32_bf16 v[88:91], v[144:147], v[52:55], v[88:91]\n" \
"v_mfma_f32_16x16x32_bf16 v[92:95], v[208:211], v[52:55], v[92:95]\n" \
"s_waitcnt lgkmcnt(3)\n" \
"v_mfma_f32_16x16x32_bf16 v[64:67], a[148:151], v[56:59], v[64:67]\n" \
"ds_read_b128 v[48:51], v14 offset:7168\n" \
"ds_read_b128 v[52:55], v14 offset:23552\n" \
"v_mfma_f32_16x16x32_bf16 v[68:71], a[212:215], v[56:59], v[68:71]\n" \
"v_mfma_f32_16x16x32_bf16 v[80:83], v[148:151], v[56:59], v[80:83]\n" \
"v_mfma_f32_16x16x32_bf16 v[84:87], v[212:215], v[56:59], v[84:87]\n" \
"s_waitcnt lgkmcnt(4)\n" \
"v_mfma_f32_16x16x32_bf16 v[72:75], a[148:151], v[60:63], v[72:75]\n" \
"v_mfma_f32_16x16x32_bf16 v[76:79], a[212:215], v[60:63], v[76:79]\n" \
"v_mfma_f32_16x16x32_bf16 v[88:91], v[148:151], v[60:63], v[88:91]\n" \
"v_mfma_f32_16x16x32_bf16 v[92:95], v[212:215], v[60:63], v[92:95]\n" \
"s_waitcnt lgkmcnt(3)\n" \
"v_mfma_f32_16x16x32_bf16 v[64:67], a[152:155], v[40:43], v[64:67]\n" \
"ds_read_b128 v[56:59], v14 offset:8192\n" \
"ds_read_b128 v[60:63], v14 offset:24576\n" \
"v_mfma_f32_16x16x32_bf16 v[68:71], a[216:219], v[40:43], v[68:71]\n" \
"v_mfma_f32_16x16x32_bf16 v[80:83], v[152:155], v[40:43], v[80:83]\n" \
"v_mfma_f32_16x16x32_bf16 v[84:87], v[216:219], v[40:43], v[84:87]\n" \
"s_waitcnt lgkmcnt(4)\n" \
"v_mfma_f32_16x16x32_bf16 v[72:75], a[152:155], v[44:47], v[72:75]\n" \
"v_mfma_f32_16x16x32_bf16 v[76:79], a[216:219], v[44:47], v[76:79]\n" \
"v_mfma_f32_16x16x32_bf16 v[88:91], v[152:155], v[44:47], v[88:91]\n" \
"v_mfma_f32_16x16x32_bf16 v[92:95], v[216:219], v[44:47], v[92:95]\n" \
"s_waitcnt lgkmcnt(3)\n" \
"v_mfma_f32_16x16x32_bf16 v[64:67], a[156:159], v[48:51], v[64:67]\n" \
"ds_read_b128 v[40:43], v14 offset:9216\n" \
"ds_read_b128 v[44:47], v14 offset:25600\n" \
"v_mfma_f32_16x16x32_bf16 v[68:71], a[220:223], v[48:51], v[68:71]\n" \
"v_mfma_f32_16x16x32_bf16 v[80:83], v[156:159], v[48:51], v[80:83]\n" \
"v_mfma_f32_16x16x32_bf16 v[84:87], v[220:223], v[48:51], v[84:87]\n" \
"s_waitcnt lgkmcnt(4)\n" \
"v_mfma_f32_16x16x32_bf16 v[72:75], a[156:159], v[52:55], v[72:75]\n" \
"v_mfma_f32_16x16x32_bf16 v[76:79], a[220:223], v[52:55], v[76:79]\n" \
"v_mfma_f32_16x16x32_bf16 v[88:91], v[156:159], v[52:55], v[88:91]\n" \
"v_mfma_f32_16x16x32_bf16 v[92:95], v[220:223], v[52:55], v[92:95]\n" \
"s_waitcnt lgkmcnt(3)\n" \
"v_mfma_f32_16x16x32_bf16 v[64:67], a[160:163], v[56:59], v[64:67]\n" \
"ds_read_b128 v[48:51], v14 offset:10240\n" \
"ds_read_b128 v[52:55], v14 offset:26624\n" \
"v_mfma_f32_16x16x32_bf16 v[68:71], a[224:227], v[56:59], v[68:71]\n" \
"v_mfma_f32_16x16x32_bf16 v[80:83], v[160:163], v[56:59], v[80:83]\n" \
"v_mfma_f32_16x16x32_bf16 v[84:87], v[224:227], v[56:59], v[84:87]\n" \
"s_waitcnt lgkmcnt(4)\n" \
"v_mfma_f32_16x16x32_bf16 v[72:75], a[160:163], v[60:63], v[72:75]\n" \
"v_mfma_f32_16x16x32_bf16 v[76:79], a[224:227], v[60:63], v[76:79]\n" \
"v_mfma_f32_16x16x32_bf16 v[88:91], v[160:163], v[60:63], v[88:91]\n" \
"v_mfma_f32_16x16x32_bf16 v[92:95], v[224:227], v[60:63], v[92:95]\n" \
"s_waitcnt lgkmcnt(3)\n" \
"v_mfma_f32_16x16x32_bf16 v[64:67], a[164:167], v[40:43], v[64:67]\n" \
"ds_read_b128 v[56:59], v14 offset:11264\n" \
"ds_read_b128 v[60:63], v14 offset:27648\n" \
"v_mfma_f32_16x16x32_bf16 v[68:71], a[228:231], v[40:43], v[68:71]\n" \
"v_mfma_f32_16x16x32_bf16 v[80:83], v[164:167], v[40:43], v[80:83]\n" \
"v_mfma_f32_16x16x32_bf16 v[84:87], v[228:231], v[40:43], v[84:87]\n" \
"s_waitcnt lgkmcnt(4)\n" \
"v_mfma_f32_16x16x32_bf16 v[72:75], a[164:167], v[44:47], v[72:75]\n" \
"v_mfma_f32_16x16x32_bf16 v[76:79], a[228:231], v[44:47], v[76:79]\n" \
"v_mfma_f32_16x16x32_bf16 v[88:91], v[164:167], v[44:47], v[88:91]\n" \
"v_mfma_f32_16x16x32_bf16 v[92:95], v[228:231], v[44:47], v[92:95]\n" \
"s_waitcnt lgkmcnt(3)\n" \
"v_mfma_f32_16x16x32_bf16 v[64:67], a[168:171], v[48:51], v[64:67]\n" \
"ds_read_b128 v[40:43], v14 offset:12288\n" \
"ds_read_b128 v[44:47], v14 offset:28672\n" \
"v_mfma_f32_16x16x32_bf16 v[68:71], a[232:235], v[48:51], v[68:71]\n" \
"v_mfma_f32_16x16x32_bf16 v[80:83], v[168:171], v[48:51], v[80:83]\n" \
"v_mfma_f32_16x16x32_bf16 v[84:87], v[232:235], v[48:51], v[84:87]\n" \
"s_waitcnt lgkmcnt(4)\n" \
"v_mfma_f32_16x16x32_bf16 v[72:75], a[168:171], v[52:55], v[72:75]\n" \
"v_mfma_f32_16x16x32_bf16 v[76:79], a[232:235], v[52:55], v[76:79]\n" \
"v_mfma_f32_16x16x32_bf16 v[88:91], v[168:171], v[52:55], v[88:91]\n" \
"v_mfma_f32_16x16x32_bf16 v[92:95], v[232:235], v[52:55], v[92:95]\n" \
"s_waitcnt lgkmcnt(3)\n" \
"v_mfma_f32_16x16x32_bf16 v[64:67], a[172:175], v[56:59], v[64:67]\n" \
"ds_read_b128 v[48:51], v14 offset:13312\n" \
"ds_read_b128 v[52:55], v14 offset:29696\n" \
"v_mfma_f32_16x16x32_bf16 v[68:71], a[236:239], v[56:59], v[68:71]\n" \
"v_mfma_f32_16x16x32_bf16 v[80:83], v[172:175], v[56:59], v[80:83]\n" \
"v_mfma_f32_16x16x32_bf16 v[84:87], v[236:239], v[56:59], v[84:87]\n" \
"s_waitcnt lgkmcnt(4)\n" \
"v_mfma_f32_16x16x32_bf16 v[72:75], a[172:175], v[60:63], v[72:75]\n" \
"v_mfma_f32_16x16x32_bf16 v[76:79], a[236:239], v[60:63], v[76:79]\n" \
"v_mfma_f32_16x16x32_bf16 v[88:91], v[172:175], v[60:63], v[88:91]\n" \
"v_mfma_f32_16x16x32_bf16 v[92:95], v[236:239], v[60:63], v[92:95]\n" \
"s_waitcnt lgkmcnt(3)\n" \
"v_mfma_f32_16x16x32_bf16 v[64:67], a[176:179], v[40:43], v[64:67]\n" \
"ds_read_b128 v[56:59], v14 offset:14336\n" \
"ds_read_b128 v[60:63], v14 offset:30720\n" \
"v_mfma_f32_16x16x32_bf16 v[68:71], a[240:243], v[40:43], v[68:71]\n" \
"v_mfma_f32_16x16x32_bf16 v[80:83], v[176:179], v[40:43], v[80:83]\n" \
"v_mfma_f32_16x16x32_bf16 v[84:87], v[240:243], v[40:43], v[84:87]\n" \
"s_waitcnt lgkmcnt(4)\n" \
"v_mfma_f32_16x16x32_bf16 v[72:75], a[176:179], v[44:47], v[72:75]\n" \
"v_mfma_f32_16x16x32_bf16 v[76:79], a[240:243], v[44:47], v[76:79]\n" \
"v_mfma_f32_16x16x32_bf16 v[88:91], v[176:179], v[44:47], v[88:91]\n" \
"v_mfma_f32_16x16x32_bf16 v[92:95], v[240:243], v[44:47], v[92:95]\n" \
"s_waitcnt lgkmcnt(3)\n" \
"v_mfma_f32_16x16x32_bf16 v[64:67], a[180:183], v[48:51], v[64:67]\n" \
"ds_read_b128 v[40:43], v14 offset:15360\n" \
"ds_read_b128 v[44:47], v14 offset:31744\n" \
"v_mfma_f32_16x16x32_bf16 v[68:71], a[244:247], v[48:51], v[68:71]\n" \
"v_mfma_f32_16x16x32_bf16 v[80:83], v[180:183], v[48:51], v[80:83]\n" \
"v_mfma_f32_16x16x32_bf16 v[84:87], v[244:247], v[48:51], v[84:87]\n" \
"s_waitcnt lgkmcnt(4)\n" \
"v_mfma_f32_16x16x32_bf16 v[72:75], a[180:183], v[52:55], v[72:75]\n" \
"v_mfma_f32_16x16x32_bf16 v[76:79], a[244:247], v[52:55], v[76:79]\n" \
"v_mfma_f32_16x16x32_bf16 v[88:91], v[180:183], v[52:55], v[88:91]\n" \
"v_mfma_f32_16x16x32_bf16 v[92:95], v[244:247], v[52:55], v[92:95]\n" \
"s_waitcnt lgkmcnt(3)\n" \
"v_mfma_f32_16x16x32_bf16 v[64:67], a[184:187], v[56:59], v[64:67]\n" \
"v_mfma_f32_16x16x32_bf16 v[68:71], a[248:251], v[56:59], v[68:71]\n" \
"v_mfma_f32_16x16x32_bf16 v[80:83], v[184:187], v[56:59], v[80:83]\n" \
"v_mfma_f32_16x16x32_bf16 v[84:87], v[248:251], v[56:59], v[84:87]\n" \
"s_waitcnt lgkmcnt(2)\n" \
"v_mfma_f32_16x16x32_bf16 v[72:75], a[184:187], v[60:63], v[72:75]\n" \
"v_mfma_f32_16x16x32_bf16 v[76:79], a[248:251], v[60:63], v[76:79]\n" \
"v_mfma_f32_16x16x32_bf16 v[88:91], v[184:187], v[60:63], v[88:91]\n" \
"v_mfma_f32_16x16x32_bf16 v[92:95], v[248:251], v[60:63], v[92:95]\n" \
"s_waitcnt lgkmcnt(1)\n" \
"v_mfma_f32_16x16x32_bf16 v[64:67], a[188:191], v[40:43], v[64:67]\n" \
"v_mfma_f32_16x16x32_bf16 v[68:71], a[252:255], v[40:43], v[68:71]\n" \
"v_mfma_f32_16x16x32_bf16 v[80:83], v[188:191], v[40:43], v[80:83]\n" \
"v_mfma_f32_16x16x32_bf16 v[84:87], v[252:255], v[40:43], v[84:87]\n" \
"s_waitcnt lgkmcnt(0)\n" \
"v_mfma_f32_16x16x32_bf16 v[72:75], a[188:191], v[44:47], v[72:75]\n" \
"v_mfma_f32_16x16x32_bf16 v[76:79], a[252:255], v[44:47], v[76:79]\n" \
"v_mfma_f32_16x16x32_bf16 v[88:91], v[188:191], v[44:47], v[88:91]\n" \
"v_mfma_f32_16x16x32_bf16 v[92:95], v[252:255], v[44:47], v[92:95]\n" \
"v_cvt_pk_bf16_f32 v96, v64, v65\n" \
"v_cvt_pk_bf16_f32 v97, v66, v67\n" \
"v_cvt_pk_bf16_f32 v98, v68, v69\n" \
"v_cvt_pk_bf16_f32 v99, v70, v71\n" \
"buffer_store_dwordx4 v[96:99], v3, s[40:43], s71 offen sc1\n" \
"v_cvt_pk_bf16_f32 v100, v72, v73\n" \
"v_cvt_pk_bf16_f32 v101, v74, v75\n" \
"v_cvt_pk_bf16_f32 v102, v76, v77\n" \
"v_cvt_pk_bf16_f32 v103, v78, v79\n" \
"s_add_u32 s67, s71, 4096\n" \
"buffer_store_dwordx4 v[100:103], v3, s[40:43], s67 offen sc1\n" \
"v_cvt_pk_bf16_f32 v104, v80, v81\n" \
"v_cvt_pk_bf16_f32 v105, v82, v83\n" \
"v_cvt_pk_bf16_f32 v106, v84, v85\n" \
"v_cvt_pk_bf16_f32 v107, v86, v87\n" \
"s_add_u32 s67, s71, 8192\n" \
"buffer_store_dwordx4 v[104:107], v3, s[40:43], s67 offen sc1\n" \
"v_cvt_pk_bf16_f32 v108, v88, v89\n" \
"v_cvt_pk_bf16_f32 v109, v90, v91\n" \
"v_cvt_pk_bf16_f32 v110, v92, v93\n" \
"v_cvt_pk_bf16_f32 v111, v94, v95\n" \
"s_add_u32 s67, s71, 12288\n" \
"buffer_store_dwordx4 v[108:111], v3, s[40:43], s67 offen sc1\n" \
"s_waitcnt vmcnt(0)\n" \
"s_barrier \n" \
"s_cmp_lg_u32 s64, 0\n" \
"s_cbranch_scc1 A_s0%=\n" \
"s_mov_b64 exec, 1\n" \
"buffer_store_dword v20, v10, s[40:43], s72 offen sc1\n" \
"s_mov_b64 exec, -1\n" \
"A_s0%=:\n" \
"s_add_u32 s72, s72, 4096\n" \
"s_waitcnt vmcnt(30)\n" \
"ds_read_b64_tr_b16 a[0:1], v13 offset:8192\n" \
"ds_read_b64_tr_b16 a[2:3], v13 offset:8448\n" \
"ds_read_b64_tr_b16 a[64:65], v13 offset:8200\n" \
"ds_read_b64_tr_b16 a[66:67], v13 offset:8456\n" \
"s_waitcnt vmcnt(28)\n" \
"ds_read_b64_tr_b16 a[4:5], v13 offset:10240\n" \
"ds_read_b64_tr_b16 a[6:7], v13 offset:10496\n" \
"ds_read_b64_tr_b16 a[68:69], v13 offset:10248\n" \
"ds_read_b64_tr_b16 a[70:71], v13 offset:10504\n" \
"s_waitcnt lgkmcnt(4)\n" \
"s_add_u32 m0, s68, 8192\n" \
"s_mov_b32 s67, 3670016\n" \
"buffer_load_dwordx4 v12, s[48:51], s67 offen lds\n" \
"s_add_u32 m0, s68, 9216\n" \
"s_mov_b32 s67, 3801088\n" \
"buffer_load_dwordx4 v12, s[48:51], s67 offen lds\n" \
"s_waitcnt vmcnt(28)\n" \
"ds_read_b64_tr_b16 a[8:9], v13 offset:12288\n" \
"ds_read_b64_tr_b16 a[10:11], v13 offset:12544\n" \
"ds_read_b64_tr_b16 a[72:73], v13 offset:12296\n" \
"ds_read_b64_tr_b16 a[74:75], v13 offset:12552\n" \
"s_waitcnt lgkmcnt(4)\n" \
"s_add_u32 m0, s68, 10240\n" \
"s_mov_b32 s67, 3932160\n" \
"buffer_load_dwordx4 v12, s[48:51], s67 offen lds\n" \
"s_add_u32 m0, s68, 11264\n" \
"s_mov_b32 s67, 4063232\n" \
"buffer_load_dwordx4 v12, s[48:51], s67 offen lds\n" \
"s_waitcnt vmcnt(28)\n" \
"ds_read_b64_tr_b16 a[12:13], v13 offset:14336\n" \
"ds_read_b64_tr_b16 a[14:15], v13 offset:14592\n" \
"ds_read_b64_tr_b16 a[76:77], v13 offset:14344\n" \
"ds_read_b64_tr_b16 a[78:79], v13 offset:14600\n" \
"s_waitcnt vmcnt(26)\n" \
"ds_read_b64_tr_b16 a[16:17], v13 offset:16384\n" \
"ds_read_b64_tr_b16 a[18:19], v13 offset:16640\n" \
"ds_read_b64_tr_b16 a[80:81], v13 offset:16392\n" \
"ds_read_b64_tr_b16 a[82:83], v13 offset:16648\n" \
"s_waitcnt vmcnt(24)\n" \
"ds_read_b64_tr_b16 a[20:21], v13 offset:18432\n" \
"ds_read_b64_tr_b16 a[22:23], v13 offset:18688\n" \
"ds_read_b64_tr_b16 a[84:85], v13 offset:18440\n" \
"ds_read_b64_tr_b16 a[86:87], v13 offset:18696\n" \
"s_waitcnt vmcnt(22)\n" \
"ds_read_b64_tr_b16 a[24:25], v13 offset:20480\n" \
"ds_read_b64_tr_b16 a[26:27], v13 offset:20736\n" \
"ds_read_b64_tr_b16 a[88:89], v13 offset:20488\n" \
"ds_read_b64_tr_b16 a[90:91], v13 offset:20744\n" \
"s_waitcnt vmcnt(20)\n" \
"ds_read_b64_tr_b16 a[28:29], v13 offset:22528\n" \
"ds_read_b64_tr_b16 a[30:31], v13 offset:22784\n" \
"ds_read_b64_tr_b16 a[92:93], v13 offset:22536\n" \
"ds_read_b64_tr_b16 a[94:95], v13 offset:22792\n" \
"s_waitcnt vmcnt(18)\n" \
"ds_read_b64_tr_b16 a[32:33], v13 offset:24576\n" \
"ds_read_b64_tr_b16 a[34:35], v13 offset:24832\n" \
"ds_read_b64_tr_b16 a[96:97], v13 offset:24584\n" \
"ds_read_b64_tr_b16 a[98:99], v13 offset:24840\n" \
"s_waitcnt vmcnt(16)\n" \
"ds_read_b64_tr_b16 a[36:37], v13 offset:26624\n" \
"ds_read_b64_tr_b16 a[38:39], v13 offset:26880\n" \
"ds_read_b64_tr_b16 a[100:101], v13 offset:26632\n" \
"ds_read_b64_tr_b16 a[102:103], v13 offset:26888\n" \
"s_waitcnt vmcnt(14)\n" \
"ds_read_b64_tr_b16 a[40:41], v13 offset:0\n" \
"ds_read_b64_tr_b16 a[42:43], v13 offset:256\n" \
"ds_read_b64_tr_b16 a[104:105], v13 offset:8\n" \
"ds_read_b64_tr_b16 a[106:107], v13 offset:264\n" \
"s_waitcnt vmcnt(12)\n" \
"ds_read_b64_tr_b16 a[44:45], v13 offset:2048\n" \
"ds_read_b64_tr_b16 a[46:47], v13 offset:2304\n" \
"ds_read_b64_tr_b16 a[108:109], v13 offset:2056\n" \
"ds_read_b64_tr_b16 a[110:111], v13 offset:2312\n" \
"s_waitcnt vmcnt(10)\n" \
"ds_read_b64_tr_b16 a[48:49], v13 offset:4096\n" \
"ds_read_b64_tr_b16 a[50:51], v13 offset:4352\n" \
"ds_read_b64_tr_b16 a[112:113], v13 offset:4104\n" \
"ds_read_b64_tr_b16 a[114:115], v13 offset:4360\n" \
"s_waitcnt vmcnt(8)\n" \
"ds_read_b64_tr_b16 a[52:53], v13 offset:6144\n" \
"ds_read_b64_tr_b16 a[54:55], v13 offset:6400\n" \
"ds_read_b64_tr_b16 a[116:117], v13 offset:6152\n" \
"ds_read_b64_tr_b16 a[118:119], v13 offset:6408\n" \
"s_waitcnt vmcnt(2)\n" \
"ds_read_b64_tr_b16 a[56:57], v13 offset:8192\n" \
"ds_read_b64_tr_b16 a[58:59], v13 offset:8448\n" \
"ds_read_b64_tr_b16 a[120:121], v13 offset:8200\n" \
"ds_read_b64_tr_b16 a[122:123], v13 offset:8456\n" \
"s_waitcnt vmcnt(0)\n" \
"ds_read_b64_tr_b16 a[60:61], v13 offset:10240\n" \
"ds_read_b64_tr_b16 a[62:63], v13 offset:10496\n" \
"ds_read_b64_tr_b16 a[124:125], v13 offset:10248\n" \
"ds_read_b64_tr_b16 a[126:127], v13 offset:10504\n" \
"s_waitcnt lgkmcnt(0)\n" \
"s_lshl_b32 s67, s64, 11\n" \
"v_add_u32 v12, s67, v1\n" \
"s_waitcnt vmcnt(0)\n" \
"s_barrier \n" \
"s_add_u32 m0, s70, 0\n" \
"s_nop 0\n" \
"buffer_load_dwordx4 v2, s[40:43], s69 offen offset:0 lds\n" \
"buffer_load_dwordx4 v2, s[40:43], s69 offen offset:1024 lds\n" \
"buffer_load_dwordx4 v2, s[40:43], s69 offen offset:2048 lds\n" \
"buffer_load_dwordx4 v2, s[40:43], s69 offen offset:3072 lds\n" \
"s_add_u32 m0, s70, 4096\n" \
"s_add_u32 s67, s69, 4096\n" \
"buffer_load_dwordx4 v2, s[40:43], s67 offen offset:0 lds\n" \
"buffer_load_dwordx4 v2, s[40:43], s67 offen offset:1024 lds\n" \
"buffer_load_dwordx4 v2, s[40:43], s67 offen offset:2048 lds\n" \
"buffer_load_dwordx4 v2, s[40:43], s67 offen offset:3072 lds\n" \
"s_add_u32 s69, s69, 65536\n" \
"s_add_u32 s70, s70, s82\n" \
"s_sub_u32 s82, 0, s82\n" \
"s_lshl_b32 s67, s64, 10\n" \
"s_add_u32 s67, s67, s73\n" \
"v_add_u32 v15, s67, v1\n" \
"ds_write_b128 v15, v[96:99] offset:8192\n" \
"ds_write_b128 v15, v[100:103] offset:12288\n" \
"ds_write_b128 v15, v[104:107] offset:16384\n" \
"ds_write_b128 v15, v[108:111] offset:20480\n" \
"ds_read_b128 v[40:43], v14 offset:0\n" \
"ds_read_b128 v[44:47], v14 offset:16384\n" \
"ds_read_b128 v[48:51], v14 offset:1024\n" \
"ds_read_b128 v[52:55], v14 offset:17408\n" \
"s_waitcnt lgkmcnt(3)\n" \
"v_mfma_f32_16x16x32_bf16 v[24:27], a[0:3], v[40:43], 0\n" \
"ds_read_b128 v[56:59], v14 offset:2048\n" \
"ds_read_b128 v[60:63], v14 offset:18432\n" \
"v_mfma_f32_16x16x32_bf16 v[28:31], a[64:67], v[40:43], 0\n" \
"s_waitcnt lgkmcnt(4)\n" \
"v_mfma_f32_16x16x32_bf16 v[32:35], a[0:3], v[44:47], 0\n" \
"v_mfma_f32_16x16x32_bf16 v[36:39], a[64:67], v[44:47], 0\n" \
"s_waitcnt lgkmcnt(3)\n" \
"v_mfma_f32_16x16x32_bf16 v[24:27], a[4:7], v[48:51], v[24:27]\n" \
"ds_read_b128 v[40:43], v14 offset:3072\n" \
"ds_read_b128 v[44:47], v14 offset:19456\n" \
"v_mfma_f32_16x16x32_bf16 v[28:31], a[68:71], v[48:51], v[28:31]\n" \
"s_waitcnt lgkmcnt(4)\n" \
"v_mfma_f32_16x16x32_bf16 v[32:35], a[4:7], v[52:55], v[32:35]\n" \
"v_mfma_f32_16x16x32_bf16 v[36:39], a[68:71], v[52:55], v[36:39]\n" \
"s_waitcnt lgkmcnt(3)\n" \
"v_mfma_f32_16x16x32_bf16 v[24:27], a[8:11], v[56:59], v[24:27]\n" \
"ds_read_b128 v[48:51], v14 offset:4096\n" \
"ds_read_b128 v[52:55], v14 offset:20480\n" \
"v_mfma_f32_16x16x32_bf16 v[28:31], a[72:75], v[56:59], v[28:31]\n" \
"s_waitcnt lgkmcnt(4)\n" \
"v_mfma_f32_16x16x32_bf16 v[32:35], a[8:11], v[60:63], v[32:35]\n" \
"v_mfma_f32_16x16x32_bf16 v[36:39], a[72:75], v[60:63], v[36:39]\n" \
"s_waitcnt lgkmcnt(3)\n" \
"v_mfma_f32_16x16x32_bf16 v[24:27], a[12:15], v[40:43], v[24:27]\n" \
"ds_read_b128 v[56:59], v14 offset:5120\n" \
"ds_read_b128 v[60:63], v14 offset:21504\n" \
"v_mfma_f32_16x16x32_bf16 v[28:31], a[76:79], v[40:43], v[28:31]\n" \
"s_waitcnt lgkmcnt(4)\n" \
"v_mfma_f32_16x16x32_bf16 v[32:35], a[12:15], v[44:47], v[32:35]\n" \
"v_mfma_f32_16x16x32_bf16 v[36:39], a[76:79], v[44:47], v[36:39]\n" \
"s_waitcnt lgkmcnt(3)\n" \
"v_mfma_f32_16x16x32_bf16 v[24:27], a[16:19], v[48:51], v[24:27]\n" \
"ds_read_b128 v[40:43], v14 offset:6144\n" \
"ds_read_b128 v[44:47], v14 offset:22528\n" \
"v_mfma_f32_16x16x32_bf16 v[28:31], a[80:83], v[48:51], v[28:31]\n" \
"s_waitcnt lgkmcnt(4)\n" \
"v_mfma_f32_16x16x32_bf16 v[32:35], a[16:19], v[52:55], v[32:35]\n" \
"v_mfma_f32_16x16x32_bf16 v[36:39], a[80:83], v[52:55], v[36:39]\n" \
"s_waitcnt lgkmcnt(3)\n" \
"v_mfma_f32_16x16x32_bf16 v[24:27], a[20:23], v[56:59], v[24:27]\n" \
"ds_read_b128 v[48:51], v14 offset:7168\n" \
"ds_read_b128 v[52:55], v14 offset:23552\n" \
"v_mfma_f32_16x16x32_bf16 v[28:31], a[84:87], v[56:59], v[28:31]\n" \
"s_waitcnt lgkmcnt(4)\n" \
"v_mfma_f32_16x16x32_bf16 v[32:35], a[20:23], v[60:63], v[32:35]\n" \
"v_mfma_f32_16x16x32_bf16 v[36:39], a[84:87], v[60:63], v[36:39]\n" \
"s_waitcnt lgkmcnt(3)\n" \
"v_mfma_f32_16x16x32_bf16 v[24:27], a[24:27], v[40:43], v[24:27]\n" \
"ds_read_b128 v[56:59], v14 offset:8192\n" \
"ds_read_b128 v[60:63], v14 offset:24576\n" \
"v_mfma_f32_16x16x32_bf16 v[28:31], a[88:91], v[40:43], v[28:31]\n" \
"s_waitcnt lgkmcnt(4)\n" \
"v_mfma_f32_16x16x32_bf16 v[32:35], a[24:27], v[44:47], v[32:35]\n" \
"v_mfma_f32_16x16x32_bf16 v[36:39], a[88:91], v[44:47], v[36:39]\n" \
"s_waitcnt lgkmcnt(3)\n" \
"v_mfma_f32_16x16x32_bf16 v[24:27], a[28:31], v[48:51], v[24:27]\n" \
"ds_read_b128 v[40:43], v14 offset:9216\n" \
"ds_read_b128 v[44:47], v14 offset:25600\n" \
"v_mfma_f32_16x16x32_bf16 v[28:31], a[92:95], v[48:51], v[28:31]\n" \
"s_waitcnt lgkmcnt(4)\n" \
"v_mfma_f32_16x16x32_bf16 v[32:35], a[28:31], v[52:55], v[32:35]\n" \
"v_mfma_f32_16x16x32_bf16 v[36:39], a[92:95], v[52:55], v[36:39]\n" \
"s_waitcnt lgkmcnt(3)\n" \
"v_mfma_f32_16x16x32_bf16 v[24:27], a[32:35], v[56:59], v[24:27]\n" \
"ds_read_b128 v[48:51], v14 offset:10240\n" \
"ds_read_b128 v[52:55], v14 offset:26624\n" \
"v_mfma_f32_16x16x32_bf16 v[28:31], a[96:99], v[56:59], v[28:31]\n" \
"s_waitcnt lgkmcnt(4)\n" \
"v_mfma_f32_16x16x32_bf16 v[32:35], a[32:35], v[60:63], v[32:35]\n" \
"v_mfma_f32_16x16x32_bf16 v[36:39], a[96:99], v[60:63], v[36:39]\n" \
"s_waitcnt lgkmcnt(3)\n" \
"v_mfma_f32_16x16x32_bf16 v[24:27], a[36:39], v[40:43], v[24:27]\n" \
"ds_read_b128 v[56:59], v14 offset:11264\n" \
"ds_read_b128 v[60:63], v14 offset:27648\n" \
"v_mfma_f32_16x16x32_bf16 v[28:31], a[100:103], v[40:43], v[28:31]\n" \
"s_waitcnt lgkmcnt(4)\n" \
"v_mfma_f32_16x16x32_bf16 v[32:35], a[36:39], v[44:47], v[32:35]\n" \
"v_mfma_f32_16x16x32_bf16 v[36:39], a[100:103], v[44:47], v[36:39]\n" \
"s_waitcnt lgkmcnt(3)\n" \
"v_mfma_f32_16x16x32_bf16 v[24:27], a[40:43], v[48:51], v[24:27]\n" \
"ds_read_b128 v[40:43], v14 offset:12288\n" \
"ds_read_b128 v[44:47], v14 offset:28672\n" \
"v_mfma_f32_16x16x32_bf16 v[28:31], a[104:107], v[48:51], v[28:31]\n" \
"s_waitcnt lgkmcnt(4)\n" \
"v_mfma_f32_16x16x32_bf16 v[32:35], a[40:43], v[52:55], v[32:35]\n" \
"v_mfma_f32_16x16x32_bf16 v[36:39], a[104:107], v[52:55], v[36:39]\n" \
"s_waitcnt lgkmcnt(3)\n" \
"v_mfma_f32_16x16x32_bf16 v[24:27], a[44:47], v[56:59], v[24:27]\n" \
"ds_read_b128 v[48:51], v14 offset:13312\n" \
"ds_read_b128 v[52:55], v14 offset:29696\n" \
"v_mfma_f32_16x16x32_bf16 v[28:31], a[108:111], v[56:59], v[28:31]\n" \
"s_waitcnt lgkmcnt(4)\n" \
"v_mfma_f32_16x16x32_bf16 v[32:35], a[44:47], v[60:63], v[32:35]\n" \
"v_mfma_f32_16x16x32_bf16 v[36:39], a[108:111], v[60:63], v[36:39]\n" \
"s_waitcnt lgkmcnt(3)\n" \
"v_mfma_f32_16x16x32_bf16 v[24:27], a[48:51], v[40:43], v[24:27]\n" \
"ds_read_b128 v[56:59], v14 offset:14336\n" \
"ds_read_b128 v[60:63], v14 offset:30720\n" \
"v_mfma_f32_16x16x32_bf16 v[28:31], a[112:115], v[40:43], v[28:31]\n" \
"s_waitcnt lgkmcnt(4)\n" \
"v_mfma_f32_16x16x32_bf16 v[32:35], a[48:51], v[44:47], v[32:35]\n" \
"v_mfma_f32_16x16x32_bf16 v[36:39], a[112:115], v[44:47], v[36:39]\n" \
"s_waitcnt lgkmcnt(3)\n" \
"v_mfma_f32_16x16x32_bf16 v[24:27], a[52:55], v[48:51], v[24:27]\n" \
"ds_read_b128 v[40:43], v14 offset:15360\n" \
"ds_read_b128 v[44:47], v14 offset:31744\n" \
"v_mfma_f32_16x16x32_bf16 v[28:31], a[116:119], v[48:51], v[28:31]\n" \
"s_waitcnt lgkmcnt(4)\n" \
"v_mfma_f32_16x16x32_bf16 v[32:35], a[52:55], v[52:55], v[32:35]\n" \
"v_mfma_f32_16x16x32_bf16 v[36:39], a[116:119], v[52:55], v[36:39]\n" \
"s_waitcnt lgkmcnt(3)\n" \
"v_mfma_f32_16x16x32_bf16 v[24:27], a[56:59], v[56:59], v[24:27]\n" \
"v_mfma_f32_16x16x32_bf16 v[28:31], a[120:123], v[56:59], v[28:31]\n" \
"s_waitcnt lgkmcnt(2)\n" \
"v_mfma_f32_16x16x32_bf16 v[32:35], a[56:59], v[60:63], v[32:35]\n" \
"v_mfma_f32_16x16x32_bf16 v[36:39], a[120:123], v[60:63], v[36:39]\n" \
"s_waitcnt lgkmcnt(1)\n" \
"v_mfma_f32_16x16x32_bf16 v[24:27], a[60:63], v[40:43], v[24:27]\n" \
"v_mfma_f32_16x16x32_bf16 v[28:31], a[124:127], v[40:43], v[28:31]\n" \
"s_waitcnt lgkmcnt(0)\n" \
"v_mfma_f32_16x16x32_bf16 v[32:35], a[60:63], v[44:47], v[32:35]\n" \
"v_mfma_f32_16x16x32_bf16 v[36:39], a[124:127], v[44:47], v[36:39]\n" \
"v_xor_b32 v14, 32768, v14\n" \
"s_add_u32 s71, s71, 1048576\n" \
"s_add_u32 s73, s73, 24576\n" \
"v_cvt_pk_bf16_f32 v64, v24, v25\n" \
"v_cvt_pk_bf16_f32 v65, v26, v27\n" \
"v_cvt_pk_bf16_f32 v66, v28, v29\n" \
"v_cvt_pk_bf16_f32 v67, v30, v31\n" \
"ds_write_b128 v15, v[64:67] offset:0\n" \
"v_cvt_pk_bf16_f32 v68, v32, v33\n" \
"v_cvt_pk_bf16_f32 v69, v34, v35\n" \
"v_cvt_pk_bf16_f32 v70, v36, v37\n" \
"v_cvt_pk_bf16_f32 v71, v38, v39\n" \
"ds_write_b128 v15, v[68:71] offset:4096\n" \
"s_waitcnt vmcnt(0)\n" \
"s_waitcnt lgkmcnt(0)\n" \
"s_barrier \n" \
"A_pre%=:\n" \
"ds_read_b128 v[40:43], v14 offset:0\n" \
"ds_read_b128 v[44:47], v14 offset:16384\n" \
"ds_read_b128 v[48:51], v14 offset:1024\n" \
"ds_read_b128 v[52:55], v14 offset:17408\n" \
"s_waitcnt lgkmcnt(3)\n" \
"v_mfma_f32_16x16x32_bf16 v[64:67], a[128:131], v[40:43], 0\n" \
"ds_read_b128 v[56:59], v14 offset:2048\n" \
"ds_read_b128 v[60:63], v14 offset:18432\n" \
"v_mfma_f32_16x16x32_bf16 v[68:71], a[192:195], v[40:43], 0\n" \
"v_mfma_f32_16x16x32_bf16 v[80:83], v[128:131], v[40:43], 0\n" \
"v_mfma_f32_16x16x32_bf16 v[84:87], v[192:195], v[40:43], 0\n" \
"s_waitcnt lgkmcnt(4)\n" \
"v_mfma_f32_16x16x32_bf16 v[72:75], a[128:131], v[44:47], 0\n" \
"v_mfma_f32_16x16x32_bf16 v[76:79], a[192:195], v[44:47], 0\n" \
"s_add_u32 m0, s70, 0\n" \
"v_mfma_f32_16x16x32_bf16 v[88:91], v[128:131], v[44:47], 0\n" \
"buffer_load_dwordx4 v2, s[40:43], s69 offen offset:0 lds\n" \
"v_mfma_f32_16x16x32_bf16 v[92:95], v[192:195], v[44:47], 0\n" \
"buffer_load_dwordx4 v2, s[40:43], s69 offen offset:1024 lds\n" \
"s_waitcnt lgkmcnt(3)\n" \
"v_mfma_f32_16x16x32_bf16 v[64:67], a[132:135], v[48:51], v[64:67]\n" \
"ds_read_b128 v[40:43], v14 offset:3072\n" \
"ds_read_b128 v[44:47], v14 offset:19456\n" \
"v_mfma_f32_16x16x32_bf16 v[68:71], a[196:199], v[48:51], v[68:71]\n" \
"buffer_load_dwordx4 v2, s[40:43], s69 offen offset:2048 lds\n" \
"v_mfma_f32_16x16x32_bf16 v[80:83], v[132:135], v[48:51], v[80:83]\n" \
"buffer_load_dwordx4 v2, s[40:43], s69 offen offset:3072 lds\n" \
"v_mfma_f32_16x16x32_bf16 v[84:87], v[196:199], v[48:51], v[84:87]\n" \
"s_add_u32 m0, s70, 4096\n" \
"s_waitcnt lgkmcnt(4)\n" \
"v_mfma_f32_16x16x32_bf16 v[72:75], a[132:135], v[52:55], v[72:75]\n" \
"s_add_u32 s67, s69, 4096\n" \
"v_mfma_f32_16x16x32_bf16 v[76:79], a[196:199], v[52:55], v[76:79]\n" \
"buffer_load_dwordx4 v2, s[40:43], s67 offen offset:0 lds\n" \
"v_mfma_f32_16x16x32_bf16 v[88:91], v[132:135], v[52:55], v[88:91]\n" \
"buffer_load_dwordx4 v2, s[40:43], s67 offen offset:1024 lds\n" \
"v_mfma_f32_16x16x32_bf16 v[92:95], v[196:199], v[52:55], v[92:95]\n" \
"buffer_load_dwordx4 v2, s[40:43], s67 offen offset:2048 lds\n" \
"s_waitcnt lgkmcnt(3)\n" \
"v_mfma_f32_16x16x32_bf16 v[64:67], a[136:139], v[56:59], v[64:67]\n" \
"ds_read_b128 v[48:51], v14 offset:4096\n" \
"ds_read_b128 v[52:55], v14 offset:20480\n" \
"v_mfma_f32_16x16x32_bf16 v[68:71], a[200:203], v[56:59], v[68:71]\n" \
"buffer_load_dwordx4 v2, s[40:43], s67 offen offset:3072 lds\n" \
"v_mfma_f32_16x16x32_bf16 v[80:83], v[136:139], v[56:59], v[80:83]\n" \
"s_add_u32 s69, s69, 65536\n" \
"v_mfma_f32_16x16x32_bf16 v[84:87], v[200:203], v[56:59], v[84:87]\n" \
"s_add_u32 s70, s70, s82\n" \
"s_waitcnt lgkmcnt(4)\n" \
"v_mfma_f32_16x16x32_bf16 v[72:75], a[136:139], v[60:63], v[72:75]\n" \
"s_sub_u32 s82, 0, s82\n" \
"v_mfma_f32_16x16x32_bf16 v[76:79], a[200:203], v[60:63], v[76:79]\n" \
"s_lshl_b32 s67, s64, 10\n" \
"v_mfma_f32_16x16x32_bf16 v[88:91], v[136:139], v[60:63], v[88:91]\n" \
"s_add_u32 s67, s67, s73\n" \
"v_mfma_f32_16x16x32_bf16 v[92:95], v[200:203], v[60:63], v[92:95]\n" \
"v_add_u32 v15, s67, v1\n" \
"s_waitcnt lgkmcnt(3)\n" \
"v_mfma_f32_16x16x32_bf16 v[64:67], a[140:143], v[40:43], v[64:67]\n" \
"ds_read_b128 v[56:59], v14 offset:5120\n" \
"ds_read_b128 v[60:63], v14 offset:21504\n" \
"v_mfma_f32_16x16x32_bf16 v[68:71], a[204:207], v[40:43], v[68:71]\n" \
"v_mfma_f32_16x16x32_bf16 v[80:83], v[140:143], v[40:43], v[80:83]\n" \
"v_mfma_f32_16x16x32_bf16 v[84:87], v[204:207], v[40:43], v[84:87]\n" \
"s_waitcnt lgkmcnt(4)\n" \
"v_mfma_f32_16x16x32_bf16 v[72:75], a[140:143], v[44:47], v[72:75]\n" \
"v_mfma_f32_16x16x32_bf16 v[76:79], a[204:207], v[44:47], v[76:79]\n" \
"v_mfma_f32_16x16x32_bf16 v[88:91], v[140:143], v[44:47], v[88:91]\n" \
"v_mfma_f32_16x16x32_bf16 v[92:95], v[204:207], v[44:47], v[92:95]\n" \
"s_waitcnt lgkmcnt(3)\n" \
"v_mfma_f32_16x16x32_bf16 v[64:67], a[144:147], v[48:51], v[64:67]\n" \
"ds_read_b128 v[40:43], v14 offset:6144\n" \
"ds_read_b128 v[44:47], v14 offset:22528\n" \
"v_mfma_f32_16x16x32_bf16 v[68:71], a[208:211], v[48:51], v[68:71]\n" \
"v_mfma_f32_16x16x32_bf16 v[80:83], v[144:147], v[48:51], v[80:83]\n" \
"v_mfma_f32_16x16x32_bf16 v[84:87], v[208:211], v[48:51], v[84:87]\n" \
"s_waitcnt lgkmcnt(4)\n" \
"v_mfma_f32_16x16x32_bf16 v[72:75], a[144:147], v[52:55], v[72:75]\n" \
"v_mfma_f32_16x16x32_bf16 v[76:79], a[208:211], v[52:55], v[76:79]\n" \
"v_mfma_f32_16x16x32_bf16 v[88:91], v[144:147], v[52:55], v[88:91]\n" \
"v_mfma_f32_16x16x32_bf16 v[92:95], v[208:211], v[52:55], v[92:95]\n" \
"s_waitcnt lgkmcnt(3)\n" \
"v_mfma_f32_16x16x32_bf16 v[64:67], a[148:151], v[56:59], v[64:67]\n" \
"ds_read_b128 v[48:51], v14 offset:7168\n" \
"ds_read_b128 v[52:55], v14 offset:23552\n" \
"v_mfma_f32_16x16x32_bf16 v[68:71], a[212:215], v[56:59], v[68:71]\n" \
"v_mfma_f32_16x16x32_bf16 v[80:83], v[148:151], v[56:59], v[80:83]\n" \
"v_mfma_f32_16x16x32_bf16 v[84:87], v[212:215], v[56:59], v[84:87]\n" \
"s_waitcnt lgkmcnt(4)\n" \
"v_mfma_f32_16x16x32_bf16 v[72:75], a[148:151], v[60:63], v[72:75]\n" \
"v_mfma_f32_16x16x32_bf16 v[76:79], a[212:215], v[60:63], v[76:79]\n" \
"v_mfma_f32_16x16x32_bf16 v[88:91], v[148:151], v[60:63], v[88:91]\n" \
"v_mfma_f32_16x16x32_bf16 v[92:95], v[212:215], v[60:63], v[92:95]\n" \
"s_waitcnt lgkmcnt(3)\n" \
"v_mfma_f32_16x16x32_bf16 v[64:67], a[152:155], v[40:43], v[64:67]\n" \
"ds_read_b128 v[56:59], v14 offset:8192\n" \
"ds_read_b128 v[60:63], v14 offset:24576\n" \
"v_mfma_f32_16x16x32_bf16 v[68:71], a[216:219], v[40:43], v[68:71]\n" \
"v_mfma_f32_16x16x32_bf16 v[80:83], v[152:155], v[40:43], v[80:83]\n" \
"v_mfma_f32_16x16x32_bf16 v[84:87], v[216:219], v[40:43], v[84:87]\n" \
"s_waitcnt lgkmcnt(4)\n" \
"v_mfma_f32_16x16x32_bf16 v[72:75], a[152:155], v[44:47], v[72:75]\n" \
"v_mfma_f32_16x16x32_bf16 v[76:79], a[216:219], v[44:47], v[76:79]\n" \
"v_mfma_f32_16x16x32_bf16 v[88:91], v[152:155], v[44:47], v[88:91]\n" \
"v_mfma_f32_16x16x32_bf16 v[92:95], v[216:219], v[44:47], v[92:95]\n" \
"s_waitcnt lgkmcnt(3)\n" \
"v_mfma_f32_16x16x32_bf16 v[64:67], a[156:159], v[48:51], v[64:67]\n" \
"ds_read_b128 v[40:43], v14 offset:9216\n" \
"ds_read_b128 v[44:47], v14 offset:25600\n" \
"v_mfma_f32_16x16x32_bf16 v[68:71], a[220:223], v[48:51], v[68:71]\n" \
"v_mfma_f32_16x16x32_bf16 v[80:83], v[156:159], v[48:51], v[80:83]\n" \
"v_mfma_f32_16x16x32_bf16 v[84:87], v[220:223], v[48:51], v[84:87]\n" \
"s_waitcnt lgkmcnt(4)\n" \
"v_mfma_f32_16x16x32_bf16 v[72:75], a[156:159], v[52:55], v[72:75]\n" \
"v_mfma_f32_16x16x32_bf16 v[76:79], a[220:223], v[52:55], v[76:79]\n" \
"v_mfma_f32_16x16x32_bf16 v[88:91], v[156:159], v[52:55], v[88:91]\n" \
"v_mfma_f32_16x16x32_bf16 v[92:95], v[220:223], v[52:55], v[92:95]\n" \
"s_waitcnt lgkmcnt(3)\n" \
"v_mfma_f32_16x16x32_bf16 v[64:67], a[160:163], v[56:59], v[64:67]\n" \
"ds_read_b128 v[48:51], v14 offset:10240\n" \
"ds_read_b128 v[52:55], v14 offset:26624\n" \
"v_mfma_f32_16x16x32_bf16 v[68:71], a[224:227], v[56:59], v[68:71]\n" \
"v_mfma_f32_16x16x32_bf16 v[80:83], v[160:163], v[56:59], v[80:83]\n" \
"v_mfma_f32_16x16x32_bf16 v[84:87], v[224:227], v[56:59], v[84:87]\n" \
"s_waitcnt lgkmcnt(4)\n" \
"v_mfma_f32_16x16x32_bf16 v[72:75], a[160:163], v[60:63], v[72:75]\n" \
"v_mfma_f32_16x16x32_bf16 v[76:79], a[224:227], v[60:63], v[76:79]\n" \
"v_mfma_f32_16x16x32_bf16 v[88:91], v[160:163], v[60:63], v[88:91]\n" \
"v_mfma_f32_16x16x32_bf16 v[92:95], v[224:227], v[60:63], v[92:95]\n" \
"s_waitcnt lgkmcnt(3)\n" \
"v_mfma_f32_16x16x32_bf16 v[64:67], a[164:167], v[40:43], v[64:67]\n" \
"ds_read_b128 v[56:59], v14 offset:11264\n" \
"ds_read_b128 v[60:63], v14 offset:27648\n" \
"v_mfma_f32_16x16x32_bf16 v[68:71], a[228:231], v[40:43], v[68:71]\n" \
"v_mfma_f32_16x16x32_bf16 v[80:83], v[164:167], v[40:43], v[80:83]\n" \
"v_mfma_f32_16x16x32_bf16 v[84:87], v[228:231], v[40:43], v[84:87]\n" \
"s_waitcnt lgkmcnt(4)\n" \
"v_mfma_f32_16x16x32_bf16 v[72:75], a[164:167], v[44:47], v[72:75]\n" \
"v_mfma_f32_16x16x32_bf16 v[76:79], a[228:231], v[44:47], v[76:79]\n" \
"v_mfma_f32_16x16x32_bf16 v[88:91], v[164:167], v[44:47], v[88:91]\n" \
"v_mfma_f32_16x16x32_bf16 v[92:95], v[228:231], v[44:47], v[92:95]\n" \
"s_waitcnt lgkmcnt(3)\n" \
"v_mfma_f32_16x16x32_bf16 v[64:67], a[168:171], v[48:51], v[64:67]\n" \
"ds_read_b128 v[40:43], v14 offset:12288\n" \
"ds_read_b128 v[44:47], v14 offset:28672\n" \
"v_mfma_f32_16x16x32_bf16 v[68:71], a[232:235], v[48:51], v[68:71]\n" \
"v_mfma_f32_16x16x32_bf16 v[80:83], v[168:171], v[48:51], v[80:83]\n" \
"v_mfma_f32_16x16x32_bf16 v[84:87], v[232:235], v[48:51], v[84:87]\n" \
"s_waitcnt lgkmcnt(4)\n" \
"v_mfma_f32_16x16x32_bf16 v[72:75], a[168:171], v[52:55], v[72:75]\n" \
"v_mfma_f32_16x16x32_bf16 v[76:79], a[232:235], v[52:55], v[76:79]\n" \
"v_mfma_f32_16x16x32_bf16 v[88:91], v[168:171], v[52:55], v[88:91]\n" \
"v_mfma_f32_16x16x32_bf16 v[92:95], v[232:235], v[52:55], v[92:95]\n" \
"s_waitcnt lgkmcnt(3)\n" \
"v_mfma_f32_16x16x32_bf16 v[64:67], a[172:175], v[56:59], v[64:67]\n" \
"ds_read_b128 v[48:51], v14 offset:13312\n" \
"ds_read_b128 v[52:55], v14 offset:29696\n" \
"v_mfma_f32_16x16x32_bf16 v[68:71], a[236:239], v[56:59], v[68:71]\n" \
"v_mfma_f32_16x16x32_bf16 v[80:83], v[172:175], v[56:59], v[80:83]\n" \
"v_mfma_f32_16x16x32_bf16 v[84:87], v[236:239], v[56:59], v[84:87]\n" \
"s_waitcnt lgkmcnt(4)\n" \
"v_mfma_f32_16x16x32_bf16 v[72:75], a[172:175], v[60:63], v[72:75]\n" \
"v_mfma_f32_16x16x32_bf16 v[76:79], a[236:239], v[60:63], v[76:79]\n" \
"v_mfma_f32_16x16x32_bf16 v[88:91], v[172:175], v[60:63], v[88:91]\n" \
"v_mfma_f32_16x16x32_bf16 v[92:95], v[236:239], v[60:63], v[92:95]\n" \
"s_waitcnt lgkmcnt(3)\n" \
"v_mfma_f32_16x16x32_bf16 v[64:67], a[176:179], v[40:43], v[64:67]\n" \
"ds_read_b128 v[56:59], v14 offset:14336\n" \
"ds_read_b128 v[60:63], v14 offset:30720\n" \
"v_mfma_f32_16x16x32_bf16 v[68:71], a[240:243], v[40:43], v[68:71]\n" \
"v_mfma_f32_16x16x32_bf16 v[80:83], v[176:179], v[40:43], v[80:83]\n" \
"v_mfma_f32_16x16x32_bf16 v[84:87], v[240:243], v[40:43], v[84:87]\n" \
"s_waitcnt lgkmcnt(4)\n" \
"v_mfma_f32_16x16x32_bf16 v[72:75], a[176:179], v[44:47], v[72:75]\n" \
"v_mfma_f32_16x16x32_bf16 v[76:79], a[240:243], v[44:47], v[76:79]\n" \
"v_mfma_f32_16x16x32_bf16 v[88:91], v[176:179], v[44:47], v[88:91]\n" \
"v_mfma_f32_16x16x32_bf16 v[92:95], v[240:243], v[44:47], v[92:95]\n" \
"s_waitcnt lgkmcnt(3)\n" \
"v_mfma_f32_16x16x32_bf16 v[64:67], a[180:183], v[48:51], v[64:67]\n" \
"ds_read_b128 v[40:43], v14 offset:15360\n" \
"ds_read_b128 v[44:47], v14 offset:31744\n" \
"v_mfma_f32_16x16x32_bf16 v[68:71], a[244:247], v[48:51], v[68:71]\n" \
"v_mfma_f32_16x16x32_bf16 v[80:83], v[180:183], v[48:51], v[80:83]\n" \
"v_mfma_f32_16x16x32_bf16 v[84:87], v[244:247], v[48:51], v[84:87]\n" \
"s_waitcnt lgkmcnt(4)\n" \
"v_mfma_f32_16x16x32_bf16 v[72:75], a[180:183], v[52:55], v[72:75]\n" \
"v_mfma_f32_16x16x32_bf16 v[76:79], a[244:247], v[52:55], v[76:79]\n" \
"v_mfma_f32_16x16x32_bf16 v[88:91], v[180:183], v[52:55], v[88:91]\n" \
"v_mfma_f32_16x16x32_bf16 v[92:95], v[244:247], v[52:55], v[92:95]\n" \
"s_waitcnt lgkmcnt(3)\n" \
"v_mfma_f32_16x16x32_bf16 v[64:67], a[184:187], v[56:59], v[64:67]\n" \
"ds_read_b128 v[48:51], v14 offset:0\n" \
"ds_read_b128 v[52:55], v14 offset:16384\n" \
"v_mfma_f32_16x16x32_bf16 v[68:71], a[248:251], v[56:59], v[68:71]\n" \
"v_mfma_f32_16x16x32_bf16 v[80:83], v[184:187], v[56:59], v[80:83]\n" \
"v_mfma_f32_16x16x32_bf16 v[84:87], v[248:251], v[56:59], v[84:87]\n" \
"s_waitcnt lgkmcnt(4)\n" \
"v_mfma_f32_16x16x32_bf16 v[72:75], a[184:187], v[60:63], v[72:75]\n" \
"v_mfma_f32_16x16x32_bf16 v[76:79], a[248:251], v[60:63], v[76:79]\n" \
"v_mfma_f32_16x16x32_bf16 v[88:91], v[184:187], v[60:63], v[88:91]\n" \
"v_mfma_f32_16x16x32_bf16 v[92:95], v[248:251], v[60:63], v[92:95]\n" \
"s_waitcnt lgkmcnt(3)\n" \
"v_mfma_f32_16x16x32_bf16 v[64:67], a[188:191], v[40:43], v[64:67]\n" \
"ds_read_b128 v[56:59], v14 offset:1024\n" \
"ds_read_b128 v[60:63], v14 offset:17408\n" \
"v_mfma_f32_16x16x32_bf16 v[68:71], a[252:255], v[40:43], v[68:71]\n" \
"v_mfma_f32_16x16x32_bf16 v[80:83], v[188:191], v[40:43], v[80:83]\n" \
"v_mfma_f32_16x16x32_bf16 v[84:87], v[252:255], v[40:43], v[84:87]\n" \
"s_waitcnt lgkmcnt(4)\n" \
"v_mfma_f32_16x16x32_bf16 v[72:75], a[188:191], v[44:47], v[72:75]\n" \
"v_mfma_f32_16x16x32_bf16 v[76:79], a[252:255], v[44:47], v[76:79]\n" \
"v_mfma_f32_16x16x32_bf16 v[88:91], v[188:191], v[44:47], v[88:91]\n" \
"v_mfma_f32_16x16x32_bf16 v[92:95], v[252:255], v[44:47], v[92:95]\n" \
"s_waitcnt lgkmcnt(3)\n" \
"v_mfma_f32_16x16x32_bf16 v[24:27], a[0:3], v[48:51], 0\n" \
"ds_read_b128 v[40:43], v14 offset:2048\n" \
"ds_read_b128 v[44:47], v14 offset:18432\n" \
"v_mfma_f32_16x16x32_bf16 v[28:31], a[64:67], v[48:51], 0\n" \
"s_waitcnt lgkmcnt(4)\n" \
"v_mfma_f32_16x16x32_bf16 v[32:35], a[0:3], v[52:55], 0\n" \
"v_cvt_pk_bf16_f32 v96, v64, v65\n" \
"v_cvt_pk_bf16_f32 v97, v66, v67\n" \
"v_mfma_f32_16x16x32_bf16 v[36:39], a[64:67], v[52:55], 0\n" \
"v_cvt_pk_bf16_f32 v98, v68, v69\n" \
"v_cvt_pk_bf16_f32 v99, v70, v71\n" \
"s_waitcnt lgkmcnt(3)\n" \
"v_mfma_f32_16x16x32_bf16 v[24:27], a[4:7], v[56:59], v[24:27]\n" \
"ds_read_b128 v[48:51], v14 offset:3072\n" \
"ds_read_b128 v[52:55], v14 offset:19456\n" \
"v_mfma_f32_16x16x32_bf16 v[28:31], a[68:71], v[56:59], v[28:31]\n" \
"buffer_store_dwordx4 v[96:99], v3, s[40:43], s71 offen sc1\n" \
"v_cvt_pk_bf16_f32 v100, v72, v73\n" \
"s_waitcnt lgkmcnt(4)\n" \
"v_mfma_f32_16x16x32_bf16 v[32:35], a[4:7], v[60:63], v[32:35]\n" \
"v_cvt_pk_bf16_f32 v101, v74, v75\n" \
"v_cvt_pk_bf16_f32 v102, v76, v77\n" \
"v_mfma_f32_16x16x32_bf16 v[36:39], a[68:71], v[60:63], v[36:39]\n" \
"v_cvt_pk_bf16_f32 v103, v78, v79\n" \
"s_add_u32 s67, s71, 4096\n" \
"s_waitcnt lgkmcnt(3)\n" \
"v_mfma_f32_16x16x32_bf16 v[24:27], a[8:11], v[40:43], v[24:27]\n" \
"ds_read_b128 v[56:59], v14 offset:4096\n" \
"ds_read_b128 v[60:63], v14 offset:20480\n" \
"v_mfma_f32_16x16x32_bf16 v[28:31], a[72:75], v[40:43], v[28:31]\n" \
"buffer_store_dwordx4 v[100:103], v3, s[40:43], s67 offen sc1\n" \
"v_cvt_pk_bf16_f32 v104, v80, v81\n" \
"s_waitcnt lgkmcnt(4)\n" \
"v_mfma_f32_16x16x32_bf16 v[32:35], a[8:11], v[44:47], v[32:35]\n" \
"v_cvt_pk_bf16_f32 v105, v82, v83\n" \
"v_cvt_pk_bf16_f32 v106, v84, v85\n" \
"v_mfma_f32_16x16x32_bf16 v[36:39], a[72:75], v[44:47], v[36:39]\n" \
"v_cvt_pk_bf16_f32 v107, v86, v87\n" \
"s_add_u32 s67, s71, 8192\n" \
"s_waitcnt lgkmcnt(3)\n" \
"v_mfma_f32_16x16x32_bf16 v[24:27], a[12:15], v[48:51], v[24:27]\n" \
"ds_read_b128 v[40:43], v14 offset:5120\n" \
"ds_read_b128 v[44:47], v14 offset:21504\n" \
"v_mfma_f32_16x16x32_bf16 v[28:31], a[76:79], v[48:51], v[28:31]\n" \
"buffer_store_dwordx4 v[104:107], v3, s[40:43], s67 offen sc1\n" \
"v_cvt_pk_bf16_f32 v108, v88, v89\n" \
"s_waitcnt lgkmcnt(4)\n" \
"v_mfma_f32_16x16x32_bf16 v[32:35], a[12:15], v[52:55], v[32:35]\n" \
"v_cvt_pk_bf16_f32 v109, v90, v91\n" \
"v_cvt_pk_bf16_f32 v110, v92, v93\n" \
"v_mfma_f32_16x16x32_bf16 v[36:39], a[76:79], v[52:55], v[36:39]\n" \
"v_cvt_pk_bf16_f32 v111, v94, v95\n" \
"s_add_u32 s67, s71, 12288\n" \
"s_waitcnt lgkmcnt(3)\n" \
"v_mfma_f32_16x16x32_bf16 v[24:27], a[16:19], v[56:59], v[24:27]\n" \
"ds_read_b128 v[48:51], v14 offset:6144\n" \
"ds_read_b128 v[52:55], v14 offset:22528\n" \
"v_mfma_f32_16x16x32_bf16 v[28:31], a[80:83], v[56:59], v[28:31]\n" \
"buffer_store_dwordx4 v[108:111], v3, s[40:43], s67 offen sc1\n" \
"ds_write_b128 v15, v[96:99] offset:8192\n" \
"s_waitcnt lgkmcnt(5)\n" \
"v_mfma_f32_16x16x32_bf16 v[32:35], a[16:19], v[60:63], v[32:35]\n" \
"ds_write_b128 v15, v[100:103] offset:12288\n" \
"ds_write_b128 v15, v[104:107] offset:16384\n" \
"v_mfma_f32_16x16x32_bf16 v[36:39], a[80:83], v[60:63], v[36:39]\n" \
"ds_write_b128 v15, v[108:111] offset:20480\n" \
"s_waitcnt lgkmcnt(7)\n" \
"v_mfma_f32_16x16x32_bf16 v[24:27], a[20:23], v[40:43], v[24:27]\n" \
"ds_read_b128 v[56:59], v14 offset:7168\n" \
"ds_read_b128 v[60:63], v14 offset:23552\n" \
"v_mfma_f32_16x16x32_bf16 v[28:31], a[84:87], v[40:43], v[28:31]\n" \
"s_waitcnt lgkmcnt(8)\n" \
"v_mfma_f32_16x16x32_bf16 v[32:35], a[20:23], v[44:47], v[32:35]\n" \
"v_mfma_f32_16x16x32_bf16 v[36:39], a[84:87], v[44:47], v[36:39]\n" \
"s_waitcnt lgkmcnt(7)\n" \
"v_mfma_f32_16x16x32_bf16 v[24:27], a[24:27], v[48:51], v[24:27]\n" \
"ds_read_b128 v[40:43], v14 offset:8192\n" \
"ds_read_b128 v[44:47], v14 offset:24576\n" \
"v_mfma_f32_16x16x32_bf16 v[28:31], a[88:91], v[48:51], v[28:31]\n" \
"s_waitcnt lgkmcnt(8)\n" \
"v_mfma_f32_16x16x32_bf16 v[32:35], a[24:27], v[52:55], v[32:35]\n" \
"v_mfma_f32_16x16x32_bf16 v[36:39], a[88:91], v[52:55], v[36:39]\n" \
"s_waitcnt lgkmcnt(3)\n" \
"v_mfma_f32_16x16x32_bf16 v[24:27], a[28:31], v[56:59], v[24:27]\n" \
"ds_read_b128 v[48:51], v14 offset:9216\n" \
"ds_read_b128 v[52:55], v14 offset:25600\n" \
"v_mfma_f32_16x16x32_bf16 v[28:31], a[92:95], v[56:59], v[28:31]\n" \
"s_waitcnt lgkmcnt(4)\n" \
"v_mfma_f32_16x16x32_bf16 v[32:35], a[28:31], v[60:63], v[32:35]\n" \
"v_mfma_f32_16x16x32_bf16 v[36:39], a[92:95], v[60:63], v[36:39]\n" \
"s_waitcnt lgkmcnt(3)\n" \
"v_mfma_f32_16x16x32_bf16 v[24:27], a[32:35], v[40:43], v[24:27]\n" \
"ds_read_b128 v[56:59], v14 offset:10240\n" \
"ds_read_b128 v[60:63], v14 offset:26624\n" \
"v_mfma_f32_16x16x32_bf16 v[28:31], a[96:99], v[40:43], v[28:31]\n" \
"s_waitcnt lgkmcnt(4)\n" \
"v_mfma_f32_16x16x32_bf16 v[32:35], a[32:35], v[44:47], v[32:35]\n" \
"v_mfma_f32_16x16x32_bf16 v[36:39], a[96:99], v[44:47], v[36:39]\n" \
"s_waitcnt lgkmcnt(3)\n" \
"v_mfma_f32_16x16x32_bf16 v[24:27], a[36:39], v[48:51], v[24:27]\n" \
"ds_read_b128 v[40:43], v14 offset:11264\n" \
"ds_read_b128 v[44:47], v14 offset:27648\n" \
"v_mfma_f32_16x16x32_bf16 v[28:31], a[100:103], v[48:51], v[28:31]\n" \
"s_waitcnt lgkmcnt(4)\n" \
"v_mfma_f32_16x16x32_bf16 v[32:35], a[36:39], v[52:55], v[32:35]\n" \
"v_mfma_f32_16x16x32_bf16 v[36:39], a[100:103], v[52:55], v[36:39]\n" \
"s_waitcnt lgkmcnt(3)\n" \
"v_mfma_f32_16x16x32_bf16 v[24:27], a[40:43], v[56:59], v[24:27]\n" \
"ds_read_b128 v[48:51], v14 offset:12288\n" \
"ds_read_b128 v[52:55], v14 offset:28672\n" \
"v_mfma_f32_16x16x32_bf16 v[28:31], a[104:107], v[56:59], v[28:31]\n" \
"s_waitcnt lgkmcnt(4)\n" \
"v_mfma_f32_16x16x32_bf16 v[32:35], a[40:43], v[60:63], v[32:35]\n" \
"v_mfma_f32_16x16x32_bf16 v[36:39], a[104:107], v[60:63], v[36:39]\n" \
"s_waitcnt lgkmcnt(3)\n" \
"v_mfma_f32_16x16x32_bf16 v[24:27], a[44:47], v[40:43], v[24:27]\n" \
"ds_read_b128 v[56:59], v14 offset:13312\n" \
"ds_read_b128 v[60:63], v14 offset:29696\n" \
"v_mfma_f32_16x16x32_bf16 v[28:31], a[108:111], v[40:43], v[28:31]\n" \
"s_waitcnt lgkmcnt(4)\n" \
"v_mfma_f32_16x16x32_bf16 v[32:35], a[44:47], v[44:47], v[32:35]\n" \
"v_mfma_f32_16x16x32_bf16 v[36:39], a[108:111], v[44:47], v[36:39]\n" \
"s_waitcnt lgkmcnt(3)\n" \
"v_mfma_f32_16x16x32_bf16 v[24:27], a[48:51], v[48:51], v[24:27]\n" \
"ds_read_b128 v[40:43], v14 offset:14336\n" \
"ds_read_b128 v[44:47], v14 offset:30720\n" \
"v_mfma_f32_16x16x32_bf16 v[28:31], a[112:115], v[48:51], v[28:31]\n" \
"s_waitcnt lgkmcnt(4)\n" \
"v_mfma_f32_16x16x32_bf16 v[32:35], a[48:51], v[52:55], v[32:35]\n" \
"v_mfma_f32_16x16x32_bf16 v[36:39], a[112:115], v[52:55], v[36:39]\n" \
"s_waitcnt lgkmcnt(3)\n" \
"v_mfma_f32_16x16x32_bf16 v[24:27], a[52:55], v[56:59], v[24:27]\n" \
"ds_read_b128 v[48:51], v14 offset:15360\n" \
"ds_read_b128 v[52:55], v14 offset:31744\n" \
"v_mfma_f32_16x16x32_bf16 v[28:31], a[116:119], v[56:59], v[28:31]\n" \
"s_waitcnt lgkmcnt(4)\n" \
"v_mfma_f32_16x16x32_bf16 v[32:35], a[52:55], v[60:63], v[32:35]\n" \
"v_mfma_f32_16x16x32_bf16 v[36:39], a[116:119], v[60:63], v[36:39]\n" \
"s_waitcnt lgkmcnt(3)\n" \
"v_mfma_f32_16x16x32_bf16 v[24:27], a[56:59], v[40:43], v[24:27]\n" \
"v_mfma_f32_16x16x32_bf16 v[28:31], a[120:123], v[40:43], v[28:31]\n" \
"s_waitcnt lgkmcnt(2)\n" \
"v_mfma_f32_16x16x32_bf16 v[32:35], a[56:59], v[44:47], v[32:35]\n" \
"v_mfma_f32_16x16x32_bf16 v[36:39], a[120:123], v[44:47], v[36:39]\n" \
"s_waitcnt lgkmcnt(1)\n" \
"v_mfma_f32_16x16x32_bf16 v[24:27], a[60:63], v[48:51], v[24:27]\n" \
"v_mfma_f32_16x16x32_bf16 v[28:31], a[124:127], v[48:51], v[28:31]\n" \
"s_waitcnt lgkmcnt(0)\n" \
"v_mfma_f32_16x16x32_bf16 v[32:35], a[60:63], v[52:55], v[32:35]\n" \
"v_mfma_f32_16x16x32_bf16 v[36:39], a[124:127], v[52:55], v[36:39]\n" \
"s_waitcnt vmcnt(0)\n" \
"s_barrier \n" \
"s_cmp_lg_u32 s64, 0\n" \
"s_cbranch_scc1 A_s1%=\n" \
"s_mov_b64 exec, 1\n" \
"buffer_store_dword v20, v10, s[40:43], s72 offen sc1\n" \
"s_mov_b64 exec, -1\n" \
"A_s1%=:\n" \
"s_add_u32 s72, s72, 4096\n" \
"buffer_load_dwordx4 v[112:115], v5, s[40:43], s75 offen offset:0 sc1\n" \
"buffer_load_dwordx4 v[116:119], v5, s[40:43], s75 offen offset:1024 sc1\n" \
"v_xor_b32 v14, 32768, v14\n" \
"s_add_u32 s71, s71, 1048576\n" \
"s_add_u32 s73, s73, 24576\n" \
"s_cmp_eq_u32 s73, 155648\n" \
"s_cselect_b32 s73, 81920, s73\n" \
"v_cvt_pk_bf16_f32 v96, v24, v25\n" \
"v_cvt_pk_bf16_f32 v97, v26, v27\n" \
"v_cvt_pk_bf16_f32 v98, v28, v29\n" \
"v_cvt_pk_bf16_f32 v99, v30, v31\n" \
"ds_write_b128 v15, v[96:99] offset:0\n" \
"v_cvt_pk_bf16_f32 v100, v32, v33\n" \
"v_cvt_pk_bf16_f32 v101, v34, v35\n" \
"v_cvt_pk_bf16_f32 v102, v36, v37\n" \
"v_cvt_pk_bf16_f32 v103, v38, v39\n" \
"ds_write_b128 v15, v[100:103] offset:4096\n" \
"s_nop 0\n" \
"s_add_u32 s66, s66, 1\n" \
"s_cmp_lt_u32 s66, 2\n" \
"s_cbranch_scc1 A_pre%=\n" \
"s_waitcnt vmcnt(0)\n" \
"s_mov_b32 s66, 0\n" \
"A_loop%=:\n" \
"s_mov_b32 s79, 0\n" \
"s_waitcnt vmcnt(2)\n" \
"A_poll%=:\n" \
"v_max3_u32 v120, v112, v113, v114\n" \
"v_max3_u32 v120, v120, v115, v116\n" \
"v_max3_u32 v120, v120, v117, v118\n" \
"v_max_u32 v120, v120, v119\n" \
"v_cmp_eq_u32 vcc, -1, v120\n" \
"s_cbranch_vccz A_got%=\n" \
"s_sleep 2\n" \
"buffer_load_dwordx4 v[112:115], v5, s[40:43], s75 offen offset:0 sc1\n" \
"buffer_load_dwordx4 v[116:119], v5, s[40:43], s75 offen offset:1024 sc1\n" \
"s_add_u32 s79, s79, 1\n" \
"s_cmp_gt_u32 s79, 65536\n" \
"s_cselect_b32 s78, 1, s78\n" \
"s_waitcnt vmcnt(0)\n" \
"s_cmp_eq_u32 s78, 0\n" \
"s_cbranch_scc1 A_poll%=\n" \
"A_got%=:\n" \
"ds_write_b128 v12, v[112:115] offset:0\n" \
"ds_write_b128 v12, v[116:119] offset:1024\n" \
"s_waitcnt lgkmcnt(0)\n" \
"s_barrier \n" \
"ds_read_b128 v[24:27], v1 offset:0\n" \
"ds_read_b128 v[28:31], v1 offset:1024\n" \
"ds_read_b128 v[32:35], v1 offset:2048\n" \
"ds_read_b128 v[36:39], v1 offset:3072\n" \
"ds_read_b128 v[40:43], v1 offset:4096\n" \
"ds_read_b128 v[44:47], v1 offset:5120\n" \
"ds_read_b128 v[48:51], v1 offset:6144\n" \
"ds_read_b128 v[52:55], v1 offset:7168\n" \
"v_add_u32 v15, s74, v1\n" \
"v_mov_b64 v[104:105], 0\n" \
"v_mov_b64 v[106:107], 0\n" \
"v_mov_b64 v[108:109], 0\n" \
"v_mov_b64 v[110:111], 0\n" \
"v_mov_b64 v[112:113], 0\n" \
"v_mov_b64 v[114:115], 0\n" \
"v_mov_b64 v[116:117], 0\n" \
"v_mov_b64 v[118:119], 0\n" \
"s_waitcnt lgkmcnt(7)\n" \
"v_smfmac_f32_16x16x64_bf16 v[104:107], v[24:27], a[128:135], v6\n" \
"ds_read_b128 v[72:75], v15 offset:8192\n" \
"v_smfmac_f32_16x16x64_bf16 v[108:111], v[24:27], a[192:199], v6\n" \
"v_smfmac_f32_16x16x64_bf16 v[112:115], v[24:27], v[128:135], v6\n" \
"v_smfmac_f32_16x16x64_bf16 v[116:119], v[24:27], v[192:199], v6\n" \
"s_waitcnt lgkmcnt(7)\n" \
"v_smfmac_f32_16x16x64_bf16 v[104:107], v[28:31], a[136:143], v6\n" \
"ds_read_b128 v[76:79], v15 offset:9216\n" \
"v_smfmac_f32_16x16x64_bf16 v[108:111], v[28:31], a[200:207], v6\n" \
"ds_read_b128 v[24:27], v15 offset:0\n" \
"v_smfmac_f32_16x16x64_bf16 v[112:115], v[28:31], v[136:143], v6\n" \
"v_smfmac_f32_16x16x64_bf16 v[116:119], v[28:31], v[200:207], v6\n" \
"s_waitcnt lgkmcnt(8)\n" \
"v_smfmac_f32_16x16x64_bf16 v[104:107], v[32:35], a[144:151], v6\n" \
"ds_read_b128 v[80:83], v15 offset:10240\n" \
"v_smfmac_f32_16x16x64_bf16 v[108:111], v[32:35], a[208:215], v6\n" \
"ds_read_b128 v[28:31], v15 offset:1024\n" \
"v_smfmac_f32_16x16x64_bf16 v[112:115], v[32:35], v[144:151], v6\n" \
"v_mov_b32 v121, 0\n" \
"v_smfmac_f32_16x16x64_bf16 v[116:119], v[32:35], v[208:215], v6\n" \
"v_mov_b64 v[122:123], 0\n" \
"s_waitcnt lgkmcnt(9)\n" \
"v_smfmac_f32_16x16x64_bf16 v[104:107], v[36:39], a[152:159], v6\n" \
"ds_read_b128 v[84:87], v15 offset:11264\n" \
"v_smfmac_f32_16x16x64_bf16 v[108:111], v[36:39], a[216:223], v6\n" \
"ds_read_b128 v[32:35], v15 offset:2048\n" \
"v_smfmac_f32_16x16x64_bf16 v[112:115], v[36:39], v[152:159], v6\n" \
"v_mov_b32 v125, 0\n" \
"v_smfmac_f32_16x16x64_bf16 v[116:119], v[36:39], v[216:223], v6\n" \
"v_mov_b64 v[126:127], 0\n" \
"s_waitcnt lgkmcnt(10)\n" \
"v_smfmac_f32_16x16x64_bf16 v[104:107], v[40:43], a[160:167], v6\n" \
"ds_read_b128 v[88:91], v15 offset:12288\n" \
"v_smfmac_f32_16x16x64_bf16 v[108:111], v[40:43], a[224:231], v6\n" \
"ds_read_b128 v[36:39], v15 offset:3072\n" \
"v_smfmac_f32_16x16x64_bf16 v[112:115], v[40:43], v[160:167], v6\n" \
"v_smfmac_f32_16x16x64_bf16 v[116:119], v[40:43], v[224:231], v6\n" \
"s_waitcnt lgkmcnt(11)\n" \
"v_smfmac_f32_16x16x64_bf16 v[104:107], v[44:47], a[168:175], v6\n" \
"ds_read_b128 v[92:95], v15 offset:13312\n" \
"v_smfmac_f32_16x16x64_bf16 v[108:111], v[44:47], a[232:239], v6\n" \
"ds_read_b128 v[40:43], v15 offset:4096\n" \
"v_smfmac_f32_16x16x64_bf16 v[112:115], v[44:47], v[168:175], v6\n" \
"v_smfmac_f32_16x16x64_bf16 v[116:119], v[44:47], v[232:239], v6\n" \
"s_waitcnt lgkmcnt(12)\n" \
"v_smfmac_f32_16x16x64_bf16 v[104:107], v[48:51], a[176:183], v6\n" \
"ds_read_b128 v[96:99], v15 offset:14336\n" \
"v_smfmac_f32_16x16x64_bf16 v[108:111], v[48:51], a[240:247], v6\n" \
"ds_read_b128 v[44:47], v15 offset:5120\n" \
"v_smfmac_f32_16x16x64_bf16 v[112:115], v[48:51], v[176:183], v6\n" \
"v_smfmac_f32_16x16x64_bf16 v[116:119], v[48:51], v[240:247], v6\n" \
"s_waitcnt lgkmcnt(13)\n" \
"v_smfmac_f32_16x16x64_bf16 v[104:107], v[52:55], a[184:191], v6\n" \
"ds_read_b128 v[100:103], v15 offset:15360\n" \
"v_smfmac_f32_16x16x64_bf16 v[108:111], v[52:55], a[248:255], v6\n" \
"ds_read_b128 v[48:51], v15 offset:6144\n" \
"v_smfmac_f32_16x16x64_bf16 v[112:115], v[52:55], v[184:191], v6\n" \
"v_smfmac_f32_16x16x64_bf16 v[116:119], v[52:55], v[248:255], v6\n" \
"ds_read_b128 v[52:55], v15 offset:7168\n" \
"v_add_f32 v104, v104, v106\n" \
"v_add_f32 v105, v105, v107\n" \
"v_cvt_pk_bf16_f32 v56, v104, v105\n" \
"v_add_f32 v108, v108, v110\n" \
"v_add_f32 v109, v109, v111\n" \
"v_cvt_pk_bf16_f32 v57, v108, v109\n" \
"v_add_f32 v112, v112, v114\n" \
"v_add_f32 v113, v113, v115\n" \
"v_cvt_pk_bf16_f32 v120, v112, v113\n" \
"v_add_f32 v116, v116, v118\n" \
"v_add_f32 v117, v117, v119\n" \
"v_cvt_pk_bf16_f32 v124, v116, v117\n" \
"ds_write2_b32 v7, v56, v57 offset1:32\n" \
"s_waitcnt lgkmcnt(14)\n" \
"v_mfma_f32_16x16x32_bf16 v[104:107], v[72:75], v[24:27], v[16:19]\n" \
"s_waitcnt lgkmcnt(6)\n" \
"v_mfma_f32_16x16x32_bf16 v[108:111], v[72:75], v[40:43], 0\n" \
"v_mfma_f32_16x16x32_bf16 v[112:115], v[88:91], v[40:43], v[16:19]\n" \
"v_mfma_f32_16x16x32_bf16 v[104:107], v[76:79], v[28:31], v[104:107]\n" \
"s_waitcnt lgkmcnt(4)\n" \
"v_mfma_f32_16x16x32_bf16 v[108:111], v[76:79], v[44:47], v[108:111]\n" \
"v_mfma_f32_16x16x32_bf16 v[112:115], v[92:95], v[44:47], v[112:115]\n" \
"s_waitcnt vmcnt(0)\n" \
"s_waitcnt lgkmcnt(0)\n" \
"s_barrier \n" \
"ds_read_b64_tr_b16 v[56:57], v8 offset:0\n" \
"ds_read_b64_tr_b16 v[58:59], v8 offset:128\n" \
"ds_read_b64_tr_b16 v[60:61], v8 offset:1024\n" \
"ds_read_b64_tr_b16 v[62:63], v8 offset:1152\n" \
"ds_read_b64_tr_b16 v[64:65], v8 offset:2048\n" \
"ds_read_b64_tr_b16 v[66:67], v8 offset:2176\n" \
"ds_read_b64_tr_b16 v[68:69], v8 offset:3072\n" \
"ds_read_b64_tr_b16 v[70:71], v8 offset:3200\n" \
"v_mfma_f32_16x16x32_bf16 v[104:107], v[80:83], v[32:35], v[104:107]\n" \
"v_mfma_f32_16x16x32_bf16 v[108:111], v[80:83], v[48:51], v[108:111]\n" \
"v_mfma_f32_16x16x32_bf16 v[112:115], v[96:99], v[48:51], v[112:115]\n" \
"v_mfma_f32_16x16x32_bf16 v[104:107], v[84:87], v[36:39], v[104:107]\n" \
"v_mfma_f32_16x16x32_bf16 v[108:111], v[84:87], v[52:55], v[108:111]\n" \
"v_mfma_f32_16x16x32_bf16 v[112:115], v[100:103], v[52:55], v[112:115]\n" \
"s_waitcnt lgkmcnt(6)\n" \
"v_mfma_f32_16x16x32_bf16 v[116:119], v[56:59], v[24:27], 0\n" \
"s_lshl_b32 s67, s64, 10\n" \
"s_add_u32 s67, s67, s74\n" \
"v_mfma_f32_16x16x32_bf16 v[72:75], v[56:59], v[40:43], 0\n" \
"v_add_u32 v23, s67, v4\n" \
"v_mov_b32 v84, 0x3f803f80\n" \
"s_waitcnt lgkmcnt(4)\n" \
"v_mfma_f32_16x16x32_bf16 v[116:119], v[60:63], v[28:31], v[116:119]\n" \
"v_mov_b32 v85, 0x3f803f80\n" \
"v_mov_b32 v86, 0x3f803f80\n" \
"v_mfma_f32_16x16x32_bf16 v[72:75], v[60:63], v[44:47], v[72:75]\n" \
"v_mov_b32 v87, 0x3f803f80\n" \
"v_mov_b32 v88, 0x3f803f80\n" \
"s_waitcnt lgkmcnt(2)\n" \
"v_mfma_f32_16x16x32_bf16 v[116:119], v[64:67], v[32:35], v[116:119]\n" \
"ds_read_b64_tr_b16 v[76:77], v23 offset:16384\n" \
"ds_read_b64_tr_b16 v[78:79], v23 offset:20480\n" \
"v_mfma_f32_16x16x32_bf16 v[72:75], v[64:67], v[48:51], v[72:75]\n" \
"ds_read_b64_tr_b16 v[80:81], v23 offset:16392\n" \
"ds_read_b64_tr_b16 v[82:83], v23 offset:20488\n" \
"s_waitcnt lgkmcnt(4)\n" \
"v_mfma_f32_16x16x32_bf16 v[116:119], v[68:71], v[36:39], v[116:119]\n" \
"v_mov_b32 v89, 0\n" \
"v_mov_b64 v[90:91], 0\n" \
"v_mfma_f32_16x16x32_bf16 v[72:75], v[68:71], v[52:55], v[72:75]\n" \
"v_max3_f32 v21, v104, v105, v106\n" \
"v_max3_f32 v22, v108, v109, v110\n" \
"v_max3_f32 v22, v22, v111, v112\n" \
"v_max3_f32 v22, v22, v113, v114\n" \
"v_max_f32 v21, v21, v107\n" \
"v_max_f32 v22, v22, v115\n" \
"v_mov_b32 v93, 0\n" \
"v_mov_b32 v99, 0\n" \
"v_mov_b64 v[96:97], 0\n" \
"s_add_u32 s67, s75, 131072\n" \
"s_xor_b32 s68, s67, s75\n" \
"s_mov_b32 s75, s67\n" \
"s_bitcmp1_b32 s68, 21\n" \
"s_cselect_b32 s67, s98, 0\n" \
"s_lshr_b32 s79, s68, 5\n" \
"s_and_b32 s79, s79, 0x4000\n" \
"s_xor_b32 s67, s67, s79\n" \
"s_lshr_b32 s79, s68, 8\n" \
"s_and_b32 s79, s79, 0x1000\n" \
"s_xor_b32 s67, s67, s79\n" \
"v_xor_b32 v5, s67, v5\n" \
"s_add_u32 s74, s74, 24576\n" \
"s_cmp_eq_u32 s74, 155648\n" \
"s_cselect_b32 s74, 81920, s74\n" \
"s_sub_u32 s67, s66, 1\n" \
"s_cmp_ge_u32 s67, 29\n" \
"s_cbranch_scc1 A_nok%=\n" \
"s_cmp_lg_u32 s64, 0\n" \
"s_cbranch_scc1 A_s5%=\n" \
"s_mov_b64 exec, 1\n" \
"buffer_store_dword v20, v10, s[40:43], s72 offen sc1\n" \
"s_mov_b64 exec, -1\n" \
"A_s5%=:\n" \
"s_add_u32 s72, s72, 4096\n" \
"A_nok%=:\n" \
"s_add_u32 s66, s66, 1\n" \
"s_cmp_ge_u32 s66, 30\n" \
"s_cbranch_scc1 A_end%=\n" \
"ds_read_b128 v[40:43], v14 offset:0\n" \
"ds_read_b128 v[44:47], v14 offset:16384\n" \
"ds_read_b128 v[48:51], v14 offset:1024\n" \
"ds_read_b128 v[52:55], v14 offset:17408\n" \
"s_waitcnt lgkmcnt(3)\n" \
"v_mfma_f32_16x16x32_bf16 v[24:27], a[0:3], v[40:43], 0\n" \
"ds_read_b128 v[56:59], v14 offset:2048\n" \
"ds_read_b128 v[60:63], v14 offset:18432\n" \
"v_mfma_f32_16x16x32_bf16 v[28:31], a[64:67], v[40:43], 0\n" \
"v_max3_f32 v21, v21, v116, v117\n" \
"v_max3_f32 v22, v22, v72, v73\n" \
"s_waitcnt lgkmcnt(4)\n" \
"v_mfma_f32_16x16x32_bf16 v[32:35], a[0:3], v[44:47], 0\n" \
"v_permlane16_swap_b32 v21, v22\n" \
"v_mfma_f32_16x16x32_bf16 v[36:39], a[64:67], v[44:47], 0\n" \
"v_max_f32 v21, v21, v22\n" \
"v_mov_b32 v22, v21\n" \
"s_waitcnt lgkmcnt(3)\n" \
"v_mfma_f32_16x16x32_bf16 v[24:27], a[4:7], v[48:51], v[24:27]\n" \
"ds_read_b128 v[40:43], v14 offset:3072\n" \
"ds_read_b128 v[44:47], v14 offset:19456\n" \
"v_mfma_f32_16x16x32_bf16 v[28:31], a[68:71], v[48:51], v[28:31]\n" \
"v_permlane32_swap_b32 v21, v22\n" \
"s_waitcnt lgkmcnt(4)\n" \
"v_mfma_f32_16x16x32_bf16 v[32:35], a[4:7], v[52:55], v[32:35]\n" \
"v_max_f32 v21, v21, v22\n" \
"v_mul_f32 v22, s81, v21\n" \
"v_mfma_f32_16x16x32_bf16 v[36:39], a[68:71], v[52:55], v[36:39]\n" \
"v_mul_f32 v21, s81, v21\n" \
"s_waitcnt lgkmcnt(3)\n" \
"v_mfma_f32_16x16x32_bf16 v[24:27], a[8:11], v[56:59], v[24:27]\n" \
"ds_read_b128 v[48:51], v14 offset:4096\n" \
"ds_read_b128 v[52:55], v14 offset:20480\n" \
"v_mfma_f32_16x16x32_bf16 v[28:31], a[72:75], v[56:59], v[28:31]\n" \
"v_permlane16_swap_b32 v21, v22\n" \
"v_fma_f32 v116, v116, s80, v21\n" \
"s_waitcnt lgkmcnt(4)\n" \
"v_mfma_f32_16x16x32_bf16 v[32:35], a[8:11], v[60:63], v[32:35]\n" \
"v_fma_f32 v117, v117, s80, v21\n" \
"v_fma_f32 v104, v104, s80, v21\n" \
"v_mfma_f32_16x16x32_bf16 v[36:39], a[72:75], v[60:63], v[36:39]\n" \
"v_fma_f32 v105, v105, s80, v21\n" \
"v_fma_f32 v106, v106, s80, v21\n" \
"s_waitcnt lgkmcnt(3)\n" \
"v_mfma_f32_16x16x32_bf16 v[24:27], a[12:15], v[40:43], v[24:27]\n" \
"ds_read_b128 v[56:59], v14 offset:5120\n" \
"ds_read_b128 v[60:63], v14 offset:21504\n" \
"v_mfma_f32_16x16x32_bf16 v[28:31], a[76:79], v[40:43], v[28:31]\n" \
"v_fma_f32 v107, v107, s80, v21\n" \
"v_exp_f32 v116, v116\n" \
"s_waitcnt lgkmcnt(4)\n" \
"v_mfma_f32_16x16x32_bf16 v[32:35], a[12:15], v[44:47], v[32:35]\n" \
"v_exp_f32 v117, v117\n" \
"v_exp_f32 v104, v104\n" \
"v_mfma_f32_16x16x32_bf16 v[36:39], a[76:79], v[44:47], v[36:39]\n" \
"v_exp_f32 v105, v105\n" \
"v_exp_f32 v106, v106\n" \
"s_waitcnt lgkmcnt(3)\n" \
"v_mfma_f32_16x16x32_bf16 v[24:27], a[16:19], v[48:51], v[24:27]\n" \
"ds_read_b128 v[40:43], v14 offset:6144\n" \
"ds_read_b128 v[44:47], v14 offset:22528\n" \
"v_mfma_f32_16x16x32_bf16 v[28:31], a[80:83], v[48:51], v[28:31]\n" \
"v_exp_f32 v107, v107\n" \
"v_cvt_pk_bf16_f32 v94, v104, v105\n" \
"s_waitcnt lgkmcnt(4)\n" \
"v_mfma_f32_16x16x32_bf16 v[32:35], a[16:19], v[52:55], v[32:35]\n" \
"v_cvt_pk_bf16_f32 v95, v106, v107\n" \
"v_cvt_pk_bf16_f32 v92, v116, v117\n" \
"v_mfma_f32_16x16x32_bf16 v[36:39], a[80:83], v[52:55], v[36:39]\n" \
"v_fma_f32 v72, v72, s80, v22\n" \
"v_fma_f32 v73, v73, s80, v22\n" \
"s_waitcnt lgkmcnt(3)\n" \
"v_mfma_f32_16x16x32_bf16 v[24:27], a[20:23], v[56:59], v[24:27]\n" \
"ds_read_b128 v[48:51], v14 offset:7168\n" \
"ds_read_b128 v[52:55], v14 offset:23552\n" \
"v_mfma_f32_16x16x32_bf16 v[28:31], a[84:87], v[56:59], v[28:31]\n" \
"v_fma_f32 v108, v108, s80, v22\n" \
"v_fma_f32 v109, v109, s80, v22\n" \
"s_waitcnt lgkmcnt(4)\n" \
"v_mfma_f32_16x16x32_bf16 v[32:35], a[20:23], v[60:63], v[32:35]\n" \
"v_fma_f32 v110, v110, s80, v22\n" \
"v_fma_f32 v111, v111, s80, v22\n" \
"v_mfma_f32_16x16x32_bf16 v[36:39], a[84:87], v[60:63], v[36:39]\n" \
"v_fma_f32 v112, v112, s80, v22\n" \
"v_fma_f32 v113, v113, s80, v22\n" \
"s_waitcnt lgkmcnt(3)\n" \
"v_mfma_f32_16x16x32_bf16 v[24:27], a[24:27], v[40:43], v[24:27]\n" \
"ds_read_b128 v[56:59], v14 offset:8192\n" \
"ds_read_b128 v[60:63], v14 offset:24576\n" \
"v_mfma_f32_16x16x32_bf16 v[28:31], a[88:91], v[40:43], v[28:31]\n" \
"v_fma_f32 v114, v114, s80, v22\n" \
"v_fma_f32 v115, v115, s80, v22\n" \
"s_waitcnt lgkmcnt(4)\n" \
"v_mfma_f32_16x16x32_bf16 v[32:35], a[24:27], v[44:47], v[32:35]\n" \
"v_mfma_f32_16x16x32_bf16 v[104:107], v[84:87], v[94:97], 0\n" \
"v_mfma_f32_16x16x32_bf16 v[64:67], v[76:79], v[94:97], 0\n" \
"v_mfma_f32_16x16x32_bf16 v[36:39], a[88:91], v[44:47], v[36:39]\n" \
"v_mfma_f32_16x16x32_bf16 v[68:71], v[80:83], v[94:97], 0\n" \
"v_mfma_f32_16x16x32_bf16 v[104:107], v[88:91], v[92:95], v[104:107]\n" \
"s_waitcnt lgkmcnt(3)\n" \
"v_mfma_f32_16x16x32_bf16 v[24:27], a[28:31], v[48:51], v[24:27]\n" \
"ds_read_b128 v[40:43], v14 offset:9216\n" \
"ds_read_b128 v[44:47], v14 offset:25600\n" \
"v_mfma_f32_16x16x32_bf16 v[28:31], a[92:95], v[48:51], v[28:31]\n" \
"v_mfma_f32_16x16x32_bf16 v[64:67], v[120:123], v[92:95], v[64:67]\n" \
"v_mfma_f32_16x16x32_bf16 v[68:71], v[124:127], v[92:95], v[68:71]\n" \
"s_waitcnt lgkmcnt(4)\n" \
"v_mfma_f32_16x16x32_bf16 v[32:35], a[28:31], v[52:55], v[32:35]\n" \
"v_exp_f32 v72, v72\n" \
"v_exp_f32 v73, v73\n" \
"v_mfma_f32_16x16x32_bf16 v[36:39], a[92:95], v[52:55], v[36:39]\n" \
"v_exp_f32 v108, v108\n" \
"v_exp_f32 v109, v109\n" \
"s_waitcnt lgkmcnt(3)\n" \
"v_mfma_f32_16x16x32_bf16 v[24:27], a[32:35], v[56:59], v[24:27]\n" \
"ds_read_b128 v[48:51], v14 offset:10240\n" \
"ds_read_b128 v[52:55], v14 offset:26624\n" \
"v_mfma_f32_16x16x32_bf16 v[28:31], a[96:99], v[56:59], v[28:31]\n" \
"v_exp_f32 v110, v110\n" \
"v_exp_f32 v111, v111\n" \
"s_waitcnt lgkmcnt(4)\n" \
"v_mfma_f32_16x16x32_bf16 v[32:35], a[32:35], v[60:63], v[32:35]\n" \
"v_exp_f32 v112, v112\n" \
"v_exp_f32 v113, v113\n" \
"v_mfma_f32_16x16x32_bf16 v[36:39], a[96:99], v[60:63], v[36:39]\n" \
"v_exp_f32 v114, v114\n" \
"v_exp_f32 v115, v115\n" \
"s_waitcnt lgkmcnt(3)\n" \
"v_mfma_f32_16x16x32_bf16 v[24:27], a[36:39], v[40:43], v[24:27]\n" \
"ds_read_b128 v[56:59], v14 offset:11264\n" \
"ds_read_b128 v[60:63], v14 offset:27648\n" \
"v_mfma_f32_16x16x32_bf16 v[28:31], a[100:103], v[40:43], v[28:31]\n" \
"v_cvt_pk_bf16_f32 v100, v108, v109\n" \
"v_cvt_pk_bf16_f32 v101, v110, v111\n" \
"s_waitcnt lgkmcnt(4)\n" \
"v_mfma_f32_16x16x32_bf16 v[32:35], a[36:39], v[44:47], v[32:35]\n" \
"v_cvt_pk_bf16_f32 v102, v112, v113\n" \
"v_cvt_pk_bf16_f32 v103, v114, v115\n" \
"v_mfma_f32_16x16x32_bf16 v[36:39], a[100:103], v[44:47], v[36:39]\n" \
"v_cvt_pk_bf16_f32 v98, v72, v73\n" \
"s_waitcnt lgkmcnt(3)\n" \
"v_mfma_f32_16x16x32_bf16 v[24:27], a[40:43], v[48:51], v[24:27]\n" \
"ds_read_b128 v[40:43], v14 offset:12288\n" \
"ds_read_b128 v[44:47], v14 offset:28672\n" \
"v_mfma_f32_16x16x32_bf16 v[28:31], a[104:107], v[48:51], v[28:31]\n" \
"v_mfma_f32_16x16x32_bf16 v[72:75], v[84:87], v[100:103], 0\n" \
"v_mfma_f32_16x16x32_bf16 v[108:111], v[76:79], v[100:103], 0\n" \
"s_waitcnt lgkmcnt(4)\n" \
"v_mfma_f32_16x16x32_bf16 v[32:35], a[40:43], v[52:55], v[32:35]\n" \
"v_mfma_f32_16x16x32_bf16 v[112:115], v[80:83], v[100:103], 0\n" \
"v_mfma_f32_16x16x32_bf16 v[72:75], v[88:91], v[98:101], v[72:75]\n" \
"v_mfma_f32_16x16x32_bf16 v[36:39], a[104:107], v[52:55], v[36:39]\n" \
"v_mfma_f32_16x16x32_bf16 v[108:111], v[120:123], v[98:101], v[108:111]\n" \
"v_mfma_f32_16x16x32_bf16 v[112:115], v[124:127], v[98:101], v[112:115]\n" \
"s_waitcnt lgkmcnt(3)\n" \
"v_mfma_f32_16x16x32_bf16 v[24:27], a[44:47], v[56:59], v[24:27]\n" \
"ds_read_b128 v[48:51], v14 offset:13312\n" \
"ds_read_b128 v[52:55], v14 offset:29696\n" \
"v_mfma_f32_16x16x32_bf16 v[28:31], a[108:111], v[56:59], v[28:31]\n" \
"v_rcp_f32 v104, v104\n" \
"s_waitcnt lgkmcnt(4)\n" \
"v_mfma_f32_16x16x32_bf16 v[32:35], a[44:47], v[60:63], v[32:35]\n" \
"v_mul_f32 v64, v64, v104\n" \
"v_mul_f32 v65, v65, v104\n" \
"v_mfma_f32_16x16x32_bf16 v[36:39], a[108:111], v[60:63], v[36:39]\n" \
"v_mul_f32 v66, v66, v104\n" \
"v_mul_f32 v67, v67, v104\n" \
"s_waitcnt lgkmcnt(3)\n" \
"v_mfma_f32_16x16x32_bf16 v[24:27], a[48:51], v[40:43], v[24:27]\n" \
"ds_read_b128 v[56:59], v14 offset:14336\n" \
"ds_read_b128 v[60:63], v14 offset:30720\n" \
"v_mfma_f32_16x16x32_bf16 v[28:31], a[112:115], v[40:43], v[28:31]\n" \
"v_mul_f32 v68, v68, v104\n" \
"v_mul_f32 v69, v69, v104\n" \
"s_waitcnt lgkmcnt(4)\n" \
"v_mfma_f32_16x16x32_bf16 v[32:35], a[48:51], v[44:47], v[32:35]\n" \
"v_mul_f32 v70, v70, v104\n" \
"v_mul_f32 v71, v71, v104\n" \
"v_mfma_f32_16x16x32_bf16 v[36:39], a[112:115], v[44:47], v[36:39]\n" \
"v_cvt_pk_bf16_f32 v116, v64, v65\n" \
"v_cvt_pk_bf16_f32 v117, v66, v67\n" \
"s_waitcnt lgkmcnt(3)\n" \
"v_mfma_f32_16x16x32_bf16 v[24:27], a[52:55], v[48:51], v[24:27]\n" \
"ds_read_b128 v[40:43], v14 offset:15360\n" \
"ds_read_b128 v[44:47], v14 offset:31744\n" \
"v_mfma_f32_16x16x32_bf16 v[28:31], a[116:119], v[48:51], v[28:31]\n" \
"v_cvt_pk_bf16_f32 v118, v68, v69\n" \
"v_cvt_pk_bf16_f32 v119, v70, v71\n" \
"s_waitcnt lgkmcnt(4)\n" \
"v_mfma_f32_16x16x32_bf16 v[32:35], a[52:55], v[52:55], v[32:35]\n" \
"buffer_store_dwordx4 v[116:119], v9, s[40:43], s76 offen sc1\n" \
"v_mfma_f32_16x16x32_bf16 v[36:39], a[116:119], v[52:55], v[36:39]\n" \
"v_rcp_f32 v72, v72\n" \
"s_add_u32 s67, s76, 131072\n" \
"s_waitcnt lgkmcnt(3)\n" \
"v_mfma_f32_16x16x32_bf16 v[24:27], a[56:59], v[56:59], v[24:27]\n" \
"ds_read_b128 v[48:51], v14 offset:0\n" \
"ds_read_b128 v[52:55], v14 offset:16384\n" \
"v_mfma_f32_16x16x32_bf16 v[28:31], a[120:123], v[56:59], v[28:31]\n" \
"v_mul_f32 v108, v108, v72\n" \
"v_mul_f32 v109, v109, v72\n" \
"s_waitcnt lgkmcnt(4)\n" \
"v_mfma_f32_16x16x32_bf16 v[32:35], a[56:59], v[60:63], v[32:35]\n" \
"v_mul_f32 v110, v110, v72\n" \
"v_mul_f32 v111, v111, v72\n" \
"v_mfma_f32_16x16x32_bf16 v[36:39], a[120:123], v[60:63], v[36:39]\n" \
"v_mul_f32 v112, v112, v72\n" \
"v_mul_f32 v113, v113, v72\n" \
"s_waitcnt lgkmcnt(3)\n" \
"v_mfma_f32_16x16x32_bf16 v[24:27], a[60:63], v[40:43], v[24:27]\n" \
"ds_read_b128 v[56:59], v14 offset:1024\n" \
"ds_read_b128 v[60:63], v14 offset:17408\n" \
"v_mfma_f32_16x16x32_bf16 v[28:31], a[124:127], v[40:43], v[28:31]\n" \
"v_mul_f32 v114, v114, v72\n" \
"v_mul_f32 v115, v115, v72\n" \
"s_waitcnt lgkmcnt(4)\n" \
"v_mfma_f32_16x16x32_bf16 v[32:35], a[60:63], v[44:47], v[32:35]\n" \
"v_cvt_pk_bf16_f32 v92, v108, v109\n" \
"v_cvt_pk_bf16_f32 v93, v110, v111\n" \
"v_mfma_f32_16x16x32_bf16 v[36:39], a[124:127], v[44:47], v[36:39]\n" \
"v_cvt_pk_bf16_f32 v94, v112, v113\n" \
"v_cvt_pk_bf16_f32 v95, v114, v115\n" \
"buffer_store_dwordx4 v[92:95], v9, s[40:43], s67 offen sc1\n" \
"s_add_u32 s76, s76, 524288\n" \
"s_waitcnt lgkmcnt(3)\n" \
"v_mfma_f32_16x16x32_bf16 v[64:67], a[128:131], v[48:51], 0\n" \
"ds_read_b128 v[40:43], v14 offset:2048\n" \
"ds_read_b128 v[44:47], v14 offset:18432\n" \
"v_mfma_f32_16x16x32_bf16 v[68:71], a[192:195], v[48:51], 0\n" \
"s_waitcnt lgkmcnt(4)\n" \
"v_mfma_f32_16x16x32_bf16 v[72:75], a[128:131], v[52:55], 0\n" \
"v_mfma_f32_16x16x32_bf16 v[76:79], a[192:195], v[52:55], 0\n" \
"s_lshl_b32 s67, s64, 10\n" \
"s_waitcnt lgkmcnt(3)\n" \
"v_mfma_f32_16x16x32_bf16 v[64:67], a[132:135], v[56:59], v[64:67]\n" \
"ds_read_b128 v[48:51], v14 offset:3072\n" \
"ds_read_b128 v[52:55], v14 offset:19456\n" \
"v_mfma_f32_16x16x32_bf16 v[68:71], a[196:199], v[56:59], v[68:71]\n" \
"s_add_u32 s67, s67, s73\n" \
"s_waitcnt lgkmcnt(4)\n" \
"v_mfma_f32_16x16x32_bf16 v[72:75], a[132:135], v[60:63], v[72:75]\n" \
"v_add_u32 v15, s67, v1\n" \
"v_mfma_f32_16x16x32_bf16 v[76:79], a[196:199], v[60:63], v[76:79]\n" \
"v_cvt_pk_bf16_f32 v96, v24, v25\n" \
"s_waitcnt lgkmcnt(3)\n" \
"v_mfma_f32_16x16x32_bf16 v[64:67], a[136:139], v[40:43], v[64:67]\n" \
"ds_read_b128 v[56:59], v14 offset:4096\n" \
"ds_read_b128 v[60:63], v14 offset:20480\n" \
"v_mfma_f32_16x16x32_bf16 v[68:71], a[200:203], v[40:43], v[68:71]\n" \
"v_cvt_pk_bf16_f32 v97, v26, v27\n" \
"s_waitcnt lgkmcnt(4)\n" \
"v_mfma_f32_16x16x32_bf16 v[72:75], a[136:139], v[44:47], v[72:75]\n" \
"v_cvt_pk_bf16_f32 v98, v28, v29\n" \
"v_mfma_f32_16x16x32_bf16 v[76:79], a[200:203], v[44:47], v[76:79]\n" \
"v_cvt_pk_bf16_f32 v99, v30, v31\n" \
"s_waitcnt lgkmcnt(3)\n" \
"v_mfma_f32_16x16x32_bf16 v[64:67], a[140:143], v[48:51], v[64:67]\n" \
"ds_read_b128 v[40:43], v14 offset:5120\n" \
"ds_read_b128 v[44:47], v14 offset:21504\n" \
"v_mfma_f32_16x16x32_bf16 v[68:71], a[204:207], v[48:51], v[68:71]\n" \
"s_waitcnt lgkmcnt(4)\n" \
"v_mfma_f32_16x16x32_bf16 v[72:75], a[140:143], v[52:55], v[72:75]\n" \
"ds_write_b128 v15, v[96:99] offset:0\n" \
"v_mfma_f32_16x16x32_bf16 v[76:79], a[204:207], v[52:55], v[76:79]\n" \
"v_cvt_pk_bf16_f32 v100, v32, v33\n" \
"s_waitcnt lgkmcnt(4)\n" \
"v_mfma_f32_16x16x32_bf16 v[64:67], a[144:147], v[56:59], v[64:67]\n" \
"ds_read_b128 v[48:51], v14 offset:6144\n" \
"ds_read_b128 v[52:55], v14 offset:22528\n" \
"v_mfma_f32_16x16x32_bf16 v[68:71], a[208:211], v[56:59], v[68:71]\n" \
"v_cvt_pk_bf16_f32 v101, v34, v35\n" \
"s_waitcnt lgkmcnt(5)\n" \
"v_mfma_f32_16x16x32_bf16 v[72:75], a[144:147], v[60:63], v[72:75]\n" \
"v_cvt_pk_bf16_f32 v102, v36, v37\n" \
"v_mfma_f32_16x16x32_bf16 v[76:79], a[208:211], v[60:63], v[76:79]\n" \
"v_cvt_pk_bf16_f32 v103, v38, v39\n" \
"s_waitcnt lgkmcnt(4)\n" \
"v_mfma_f32_16x16x32_bf16 v[64:67], a[148:151], v[40:43], v[64:67]\n" \
"ds_read_b128 v[56:59], v14 offset:7168\n" \
"ds_read_b128 v[60:63], v14 offset:23552\n" \
"v_mfma_f32_16x16x32_bf16 v[68:71], a[212:215], v[40:43], v[68:71]\n" \
"s_waitcnt lgkmcnt(5)\n" \
"v_mfma_f32_16x16x32_bf16 v[72:75], a[148:151], v[44:47], v[72:75]\n" \
"ds_write_b128 v15, v[100:103] offset:4096\n" \
"v_mfma_f32_16x16x32_bf16 v[76:79], a[212:215], v[44:47], v[76:79]\n" \
"s_add_u32 m0, s70, 0\n" \
"s_waitcnt lgkmcnt(4)\n" \
"v_mfma_f32_16x16x32_bf16 v[64:67], a[152:155], v[48:51], v[64:67]\n" \
"ds_read_b128 v[40:43], v14 offset:8192\n" \
"ds_read_b128 v[44:47], v14 offset:24576\n" \
"v_mfma_f32_16x16x32_bf16 v[68:71], a[216:219], v[48:51], v[68:71]\n" \
"buffer_load_dwordx4 v2, s[40:43], s69 offen offset:0 lds\n" \
"s_waitcnt lgkmcnt(5)\n" \
"v_mfma_f32_16x16x32_bf16 v[72:75], a[152:155], v[52:55], v[72:75]\n" \
"buffer_load_dwordx4 v2, s[40:43], s69 offen offset:1024 lds\n" \
"v_mfma_f32_16x16x32_bf16 v[76:79], a[216:219], v[52:55], v[76:79]\n" \
"buffer_load_dwordx4 v2, s[40:43], s69 offen offset:2048 lds\n" \
"s_waitcnt lgkmcnt(4)\n" \
"v_mfma_f32_16x16x32_bf16 v[64:67], a[156:159], v[56:59], v[64:67]\n" \
"ds_read_b128 v[48:51], v14 offset:9216\n" \
"ds_read_b128 v[52:55], v14 offset:25600\n" \
"v_mfma_f32_16x16x32_bf16 v[68:71], a[220:223], v[56:59], v[68:71]\n" \
"buffer_load_dwordx4 v2, s[40:43], s69 offen offset:3072 lds\n" \
"s_waitcnt lgkmcnt(5)\n" \
"v_mfma_f32_16x16x32_bf16 v[72:75], a[156:159], v[60:63], v[72:75]\n" \
"s_add_u32 m0, s70, 4096\n" \
"v_mfma_f32_16x16x32_bf16 v[76:79], a[220:223], v[60:63], v[76:79]\n" \
"s_add_u32 s67, s69, 4096\n" \
"s_waitcnt lgkmcnt(3)\n" \
"v_mfma_f32_16x16x32_bf16 v[64:67], a[160:163], v[40:43], v[64:67]\n" \
"ds_read_b128 v[56:59], v14 offset:10240\n" \
"ds_read_b128 v[60:63], v14 offset:26624\n" \
"v_mfma_f32_16x16x32_bf16 v[68:71], a[224:227], v[40:43], v[68:71]\n" \
"buffer_load_dwordx4 v2, s[40:43], s67 offen offset:0 lds\n" \
"s_waitcnt lgkmcnt(4)\n" \
"v_mfma_f32_16x16x32_bf16 v[72:75], a[160:163], v[44:47], v[72:75]\n" \
"buffer_load_dwordx4 v2, s[40:43], s67 offen offset:1024 lds\n" \
"v_mfma_f32_16x16x32_bf16 v[76:79], a[224:227], v[44:47], v[76:79]\n" \
"buffer_load_dwordx4 v2, s[40:43], s67 offen offset:2048 lds\n" \
"s_waitcnt lgkmcnt(3)\n" \
"v_mfma_f32_16x16x32_bf16 v[64:67], a[164:167], v[48:51], v[64:67]\n" \
"ds_read_b128 v[40:43], v14 offset:11264\n" \
"ds_read_b128 v[44:47], v14 offset:27648\n" \
"v_mfma_f32_16x16x32_bf16 v[68:71], a[228:231], v[48:51], v[68:71]\n" \
"buffer_load_dwordx4 v2, s[40:43], s67 offen offset:3072 lds\n" \
"s_waitcnt lgkmcnt(4)\n" \
"v_mfma_f32_16x16x32_bf16 v[72:75], a[164:167], v[52:55], v[72:75]\n" \
"s_add_u32 s69, s69, 65536\n" \
"v_mfma_f32_16x16x32_bf16 v[76:79], a[228:231], v[52:55], v[76:79]\n" \
"s_add_u32 s70, s70, s82\n" \
"s_waitcnt lgkmcnt(3)\n" \
"v_mfma_f32_16x16x32_bf16 v[64:67], a[168:171], v[56:59], v[64:67]\n" \
"ds_read_b128 v[48:51], v14 offset:12288\n" \
"ds_read_b128 v[52:55], v14 offset:28672\n" \
"v_mfma_f32_16x16x32_bf16 v[68:71], a[232:235], v[56:59], v[68:71]\n" \
"s_sub_u32 s82, 0, s82\n" \
"s_waitcnt lgkmcnt(4)\n" \
"v_mfma_f32_16x16x32_bf16 v[72:75], a[168:171], v[60:63], v[72:75]\n" \
"v_mfma_f32_16x16x32_bf16 v[76:79], a[232:235], v[60:63], v[76:79]\n" \
"s_waitcnt lgkmcnt(3)\n" \
"v_mfma_f32_16x16x32_bf16 v[64:67], a[172:175], v[40:43], v[64:67]\n" \
"ds_read_b128 v[56:59], v14 offset:13312\n" \
"ds_read_b128 v[60:63], v14 offset:29696\n" \
"v_mfma_f32_16x16x32_bf16 v[68:71], a[236:239], v[40:43], v[68:71]\n" \
"s_waitcnt lgkmcnt(4)\n" \
"v_mfma_f32_16x16x32_bf16 v[72:75], a[172:175], v[44:47], v[72:75]\n" \
"v_mfma_f32_16x16x32_bf16 v[76:79], a[236:239], v[44:47], v[76:79]\n" \
"s_waitcnt lgkmcnt(3)\n" \
"v_mfma_f32_16x16x32_bf16 v[64:67], a[176:179], v[48:51], v[64:67]\n" \
"ds_read_b128 v[40:43], v14 offset:14336\n" \
"ds_read_b128 v[44:47], v14 offset:30720\n" \
"v_mfma_f32_16x16x32_bf16 v[68:71], a[240:243], v[48:51], v[68:71]\n" \
"s_waitcnt lgkmcnt(4)\n" \
"v_mfma_f32_16x16x32_bf16 v[72:75], a[176:179], v[52:55], v[72:75]\n" \
"v_mfma_f32_16x16x32_bf16 v[76:79], a[240:243], v[52:55], v[76:79]\n" \
"s_waitcnt lgkmcnt(3)\n" \
"v_mfma_f32_16x16x32_bf16 v[64:67], a[180:183], v[56:59], v[64:67]\n" \
"ds_read_b128 v[48:51], v14 offset:15360\n" \
"ds_read_b128 v[52:55], v14 offset:31744\n" \
"v_mfma_f32_16x16x32_bf16 v[68:71], a[244:247], v[56:59], v[68:71]\n" \
"s_waitcnt lgkmcnt(4)\n" \
"v_mfma_f32_16x16x32_bf16 v[72:75], a[180:183], v[60:63], v[72:75]\n" \
"v_mfma_f32_16x16x32_bf16 v[76:79], a[244:247], v[60:63], v[76:79]\n" \
"s_waitcnt lgkmcnt(3)\n" \
"v_mfma_f32_16x16x32_bf16 v[64:67], a[184:187], v[40:43], v[64:67]\n" \
"ds_read_b128 v[56:59], v14 offset:0\n" \
"ds_read_b128 v[60:63], v14 offset:16384\n" \
"v_mfma_f32_16x16x32_bf16 v[68:71], a[248:251], v[40:43], v[68:71]\n" \
"s_waitcnt lgkmcnt(4)\n" \
"v_mfma_f32_16x16x32_bf16 v[72:75], a[184:187], v[44:47], v[72:75]\n" \
"v_mfma_f32_16x16x32_bf16 v[76:79], a[248:251], v[44:47], v[76:79]\n" \
"s_waitcnt lgkmcnt(3)\n" \
"v_mfma_f32_16x16x32_bf16 v[64:67], a[188:191], v[48:51], v[64:67]\n" \
"ds_read_b128 v[40:43], v14 offset:1024\n" \
"ds_read_b128 v[44:47], v14 offset:17408\n" \
"v_mfma_f32_16x16x32_bf16 v[68:71], a[252:255], v[48:51], v[68:71]\n" \
"s_waitcnt lgkmcnt(4)\n" \
"v_mfma_f32_16x16x32_bf16 v[72:75], a[188:191], v[52:55], v[72:75]\n" \
"v_mfma_f32_16x16x32_bf16 v[76:79], a[252:255], v[52:55], v[76:79]\n" \
"s_waitcnt vmcnt(8)\n" \
"s_barrier \n" \
"s_cmp_lg_u32 s64, 0\n" \
"s_cbranch_scc1 A_s2%=\n" \
"s_mov_b64 exec, 1\n" \
"buffer_store_dword v20, v10, s[40:43], s77 offen offset:32 sc1\n" \
"s_mov_b64 exec, -1\n" \
"A_s2%=:\n" \
"s_add_u32 s77, s77, 4096\n" \
"s_waitcnt lgkmcnt(3)\n" \
"v_mfma_f32_16x16x32_bf16 v[80:83], v[128:131], v[56:59], 0\n" \
"ds_read_b128 v[48:51], v14 offset:2048\n" \
"ds_read_b128 v[52:55], v14 offset:18432\n" \
"v_mfma_f32_16x16x32_bf16 v[84:87], v[192:195], v[56:59], 0\n" \
"s_waitcnt lgkmcnt(4)\n" \
"v_mfma_f32_16x16x32_bf16 v[88:91], v[128:131], v[60:63], 0\n" \
"v_mfma_f32_16x16x32_bf16 v[92:95], v[192:195], v[60:63], 0\n" \
"v_cvt_pk_bf16_f32 v104, v64, v65\n" \
"s_waitcnt lgkmcnt(3)\n" \
"v_mfma_f32_16x16x32_bf16 v[80:83], v[132:135], v[40:43], v[80:83]\n" \
"ds_read_b128 v[56:59], v14 offset:3072\n" \
"ds_read_b128 v[60:63], v14 offset:19456\n" \
"v_mfma_f32_16x16x32_bf16 v[84:87], v[196:199], v[40:43], v[84:87]\n" \
"v_cvt_pk_bf16_f32 v105, v66, v67\n" \
"s_waitcnt lgkmcnt(4)\n" \
"v_mfma_f32_16x16x32_bf16 v[88:91], v[132:135], v[44:47], v[88:91]\n" \
"v_cvt_pk_bf16_f32 v106, v68, v69\n" \
"v_mfma_f32_16x16x32_bf16 v[92:95], v[196:199], v[44:47], v[92:95]\n" \
"v_cvt_pk_bf16_f32 v107, v70, v71\n" \
"s_waitcnt lgkmcnt(3)\n" \
"v_mfma_f32_16x16x32_bf16 v[80:83], v[136:139], v[48:51], v[80:83]\n" \
"ds_read_b128 v[40:43], v14 offset:4096\n" \
"ds_read_b128 v[44:47], v14 offset:20480\n" \
"v_mfma_f32_16x16x32_bf16 v[84:87], v[200:203], v[48:51], v[84:87]\n" \
"buffer_store_dwordx4 v[104:107], v3, s[40:43], s71 offen sc1\n" \
"s_waitcnt lgkmcnt(4)\n" \
"v_mfma_f32_16x16x32_bf16 v[88:91], v[136:139], v[52:55], v[88:91]\n" \
"ds_write_b128 v15, v[104:107] offset:8192\n" \
"v_mfma_f32_16x16x32_bf16 v[92:95], v[200:203], v[52:55], v[92:95]\n" \
"v_cvt_pk_bf16_f32 v108, v72, v73\n" \
"s_waitcnt lgkmcnt(4)\n" \
"v_mfma_f32_16x16x32_bf16 v[80:83], v[140:143], v[56:59], v[80:83]\n" \
"ds_read_b128 v[48:51], v14 offset:5120\n" \
"ds_read_b128 v[52:55], v14 offset:21504\n" \
"v_mfma_f32_16x16x32_bf16 v[84:87], v[204:207], v[56:59], v[84:87]\n" \
"v_cvt_pk_bf16_f32 v109, v74, v75\n" \
"s_waitcnt lgkmcnt(5)\n" \
"v_mfma_f32_16x16x32_bf16 v[88:91], v[140:143], v[60:63], v[88:91]\n" \
"v_cvt_pk_bf16_f32 v110, v76, v77\n" \
"v_mfma_f32_16x16x32_bf16 v[92:95], v[204:207], v[60:63], v[92:95]\n" \
"v_cvt_pk_bf16_f32 v111, v78, v79\n" \
"s_waitcnt lgkmcnt(4)\n" \
"v_mfma_f32_16x16x32_bf16 v[80:83], v[144:147], v[40:43], v[80:83]\n" \
"ds_read_b128 v[56:59], v14 offset:6144\n" \
"ds_read_b128 v[60:63], v14 offset:22528\n" \
"v_mfma_f32_16x16x32_bf16 v[84:87], v[208:211], v[40:43], v[84:87]\n" \
"s_add_u32 s67, s71, 4096\n" \
"s_waitcnt lgkmcnt(5)\n" \
"v_mfma_f32_16x16x32_bf16 v[88:91], v[144:147], v[44:47], v[88:91]\n" \
"buffer_store_dwordx4 v[108:111], v3, s[40:43], s67 offen sc1\n" \
"v_mfma_f32_16x16x32_bf16 v[92:95], v[208:211], v[44:47], v[92:95]\n" \
"ds_write_b128 v15, v[108:111] offset:12288\n" \
"s_waitcnt lgkmcnt(4)\n" \
"v_mfma_f32_16x16x32_bf16 v[80:83], v[148:151], v[48:51], v[80:83]\n" \
"ds_read_b128 v[40:43], v14 offset:7168\n" \
"ds_read_b128 v[44:47], v14 offset:23552\n" \
"v_mfma_f32_16x16x32_bf16 v[84:87], v[212:215], v[48:51], v[84:87]\n" \
"s_waitcnt lgkmcnt(5)\n" \
"v_mfma_f32_16x16x32_bf16 v[88:91], v[148:151], v[52:55], v[88:91]\n" \
"v_mfma_f32_16x16x32_bf16 v[92:95], v[212:215], v[52:55], v[92:95]\n" \
"s_waitcnt lgkmcnt(4)\n" \
"v_mfma_f32_16x16x32_bf16 v[80:83], v[152:155], v[56:59], v[80:83]\n" \
"ds_read_b128 v[48:51], v14 offset:8192\n" \
"ds_read_b128 v[52:55], v14 offset:24576\n" \
"v_mfma_f32_16x16x32_bf16 v[84:87], v[216:219], v[56:59], v[84:87]\n" \
"s_waitcnt lgkmcnt(5)\n" \
"v_mfma_f32_16x16x32_bf16 v[88:91], v[152:155], v[60:63], v[88:91]\n" \
"v_mfma_f32_16x16x32_bf16 v[92:95], v[216:219], v[60:63], v[92:95]\n" \
"s_waitcnt lgkmcnt(3)\n" \
"v_mfma_f32_16x16x32_bf16 v[80:83], v[156:159], v[40:43], v[80:83]\n" \
"ds_read_b128 v[56:59], v14 offset:9216\n" \
"ds_read_b128 v[60:63], v14 offset:25600\n" \
"v_mfma_f32_16x16x32_bf16 v[84:87], v[220:223], v[40:43], v[84:87]\n" \
"s_waitcnt lgkmcnt(4)\n" \
"v_mfma_f32_16x16x32_bf16 v[88:91], v[156:159], v[44:47], v[88:91]\n" \
"v_mfma_f32_16x16x32_bf16 v[92:95], v[220:223], v[44:47], v[92:95]\n" \
"buffer_load_dwordx4 v[112:115], v5, s[40:43], s75 offen offset:0 sc1\n" \
"s_waitcnt lgkmcnt(3)\n" \
"v_mfma_f32_16x16x32_bf16 v[80:83], v[160:163], v[48:51], v[80:83]\n" \
"ds_read_b128 v[40:43], v14 offset:10240\n" \
"ds_read_b128 v[44:47], v14 offset:26624\n" \
"v_mfma_f32_16x16x32_bf16 v[84:87], v[224:227], v[48:51], v[84:87]\n" \
"buffer_load_dwordx4 v[116:119], v5, s[40:43], s75 offen offset:1024 sc1\n" \
"s_waitcnt lgkmcnt(4)\n" \
"v_mfma_f32_16x16x32_bf16 v[88:91], v[160:163], v[52:55], v[88:91]\n" \
"v_mfma_f32_16x16x32_bf16 v[92:95], v[224:227], v[52:55], v[92:95]\n" \
"s_waitcnt lgkmcnt(3)\n" \
"v_mfma_f32_16x16x32_bf16 v[80:83], v[164:167], v[56:59], v[80:83]\n" \
"ds_read_b128 v[48:51], v14 offset:11264\n" \
"ds_read_b128 v[52:55], v14 offset:27648\n" \
"v_mfma_f32_16x16x32_bf16 v[84:87], v[228:231], v[56:59], v[84:87]\n" \
"s_waitcnt lgkmcnt(4)\n" \
"v_mfma_f32_16x16x32_bf16 v[88:91], v[164:167], v[60:63], v[88:91]\n" \
"v_mfma_f32_16x16x32_bf16 v[92:95], v[228:231], v[60:63], v[92:95]\n" \
"s_waitcnt lgkmcnt(3)\n" \
"v_mfma_f32_16x16x32_bf16 v[80:83], v[168:171], v[40:43], v[80:83]\n" \
"ds_read_b128 v[56:59], v14 offset:12288\n" \
"ds_read_b128 v[60:63], v14 offset:28672\n" \
"v_mfma_f32_16x16x32_bf16 v[84:87], v[232:235], v[40:43], v[84:87]\n" \
"s_waitcnt lgkmcnt(4)\n" \
"v_mfma_f32_16x16x32_bf16 v[88:91], v[168:171], v[44:47], v[88:91]\n" \
"v_mfma_f32_16x16x32_bf16 v[92:95], v[232:235], v[44:47], v[92:95]\n" \
"s_waitcnt lgkmcnt(3)\n" \
"v_mfma_f32_16x16x32_bf16 v[80:83], v[172:175], v[48:51], v[80:83]\n" \
"ds_read_b128 v[40:43], v14 offset:13312\n" \
"ds_read_b128 v[44:47], v14 offset:29696\n" \
"v_mfma_f32_16x16x32_bf16 v[84:87], v[236:239], v[48:51], v[84:87]\n" \
"s_waitcnt lgkmcnt(4)\n" \
"v_mfma_f32_16x16x32_bf16 v[88:91], v[172:175], v[52:55], v[88:91]\n" \
"v_mfma_f32_16x16x32_bf16 v[92:95], v[236:239], v[52:55], v[92:95]\n" \
"s_waitcnt lgkmcnt(3)\n" \
"v_mfma_f32_16x16x32_bf16 v[80:83], v[176:179], v[56:59], v[80:83]\n" \
"ds_read_b128 v[48:51], v14 offset:14336\n" \
"ds_read_b128 v[52:55], v14 offset:30720\n" \
"v_mfma_f32_16x16x32_bf16 v[84:87], v[240:243], v[56:59], v[84:87]\n" \
"s_waitcnt lgkmcnt(4)\n" \
"v_mfma_f32_16x16x32_bf16 v[88:91], v[176:179], v[60:63], v[88:91]\n" \
"v_mfma_f32_16x16x32_bf16 v[92:95], v[240:243], v[60:63], v[92:95]\n" \
"s_waitcnt lgkmcnt(3)\n" \
"v_mfma_f32_16x16x32_bf16 v[80:83], v[180:183], v[40:43], v[80:83]\n" \
"ds_read_b128 v[56:59], v14 offset:15360\n" \
"ds_read_b128 v[60:63], v14 offset:31744\n" \
"v_mfma_f32_16x16x32_bf16 v[84:87], v[244:247], v[40:43], v[84:87]\n" \
"s_waitcnt lgkmcnt(4)\n" \
"v_mfma_f32_16x16x32_bf16 v[88:91], v[180:183], v[44:47], v[88:91]\n" \
"v_mfma_f32_16x16x32_bf16 v[92:95], v[244:247], v[44:47], v[92:95]\n" \
"s_waitcnt lgkmcnt(3)\n" \
"v_mfma_f32_16x16x32_bf16 v[80:83], v[184:187], v[48:51], v[80:83]\n" \
"v_mfma_f32_16x16x32_bf16 v[84:87], v[248:251], v[48:51], v[84:87]\n" \
"s_waitcnt lgkmcnt(2)\n" \
"v_mfma_f32_16x16x32_bf16 v[88:91], v[184:187], v[52:55], v[88:91]\n" \
"v_mfma_f32_16x16x32_bf16 v[92:95], v[248:251], v[52:55], v[92:95]\n" \
"s_waitcnt lgkmcnt(1)\n" \
"v_mfma_f32_16x16x32_bf16 v[80:83], v[188:191], v[56:59], v[80:83]\n" \
"v_mfma_f32_16x16x32_bf16 v[84:87], v[252:255], v[56:59], v[84:87]\n" \
"s_waitcnt lgkmcnt(0)\n" \
"v_mfma_f32_16x16x32_bf16 v[88:91], v[188:191], v[60:63], v[88:91]\n" \
"v_mfma_f32_16x16x32_bf16 v[92:95], v[252:255], v[60:63], v[92:95]\n" \
"v_xor_b32 v14, 32768, v14\n" \
"s_add_u32 s73, s73, 24576\n" \
"s_cmp_eq_u32 s73, 155648\n" \
"s_cselect_b32 s73, 81920, s73\n" \
"v_cvt_pk_bf16_f32 v96, v80, v81\n" \
"v_cvt_pk_bf16_f32 v97, v82, v83\n" \
"v_cvt_pk_bf16_f32 v98, v84, v85\n" \
"v_cvt_pk_bf16_f32 v99, v86, v87\n" \
"s_add_u32 s67, s71, 8192\n" \
"buffer_store_dwordx4 v[96:99], v3, s[40:43], s67 offen sc1\n" \
"ds_write_b128 v15, v[96:99] offset:16384\n" \
"v_cvt_pk_bf16_f32 v100, v88, v89\n" \
"v_cvt_pk_bf16_f32 v101, v90, v91\n" \
"v_cvt_pk_bf16_f32 v102, v92, v93\n" \
"v_cvt_pk_bf16_f32 v103, v94, v95\n" \
"s_add_u32 s67, s71, 12288\n" \
"buffer_store_dwordx4 v[100:103], v3, s[40:43], s67 offen sc1\n" \
"ds_write_b128 v15, v[100:103] offset:20480\n" \
"s_add_u32 s71, s71, 1048576\n" \
"s_branch A_loop%=\n" \
"A_end%=:\n" \
"v_max3_f32 v21, v21, v116, v117\n" \
"v_max3_f32 v22, v22, v72, v73\n" \
"s_nop 1\n" \
"v_permlane16_swap_b32 v21, v22\n" \
"v_max_f32 v21, v21, v22\n" \
"v_mov_b32 v22, v21\n" \
"s_nop 1\n" \
"v_permlane32_swap_b32 v21, v22\n" \
"v_max_f32 v21, v21, v22\n" \
"v_mul_f32 v22, s81, v21\n" \
"v_mul_f32 v21, s81, v21\n" \
"s_nop 1\n" \
"v_permlane16_swap_b32 v21, v22\n" \
"v_fma_f32 v116, v116, s80, v21\n" \
"v_fma_f32 v117, v117, s80, v21\n" \
"v_fma_f32 v104, v104, s80, v21\n" \
"v_fma_f32 v105, v105, s80, v21\n" \
"v_fma_f32 v106, v106, s80, v21\n" \
"v_fma_f32 v107, v107, s80, v21\n" \
"v_exp_f32 v116, v116\n" \
"v_exp_f32 v117, v117\n" \
"v_exp_f32 v104, v104\n" \
"v_exp_f32 v105, v105\n" \
"v_exp_f32 v106, v106\n" \
"v_exp_f32 v107, v107\n" \
"v_cvt_pk_bf16_f32 v94, v104, v105\n" \
"v_cvt_pk_bf16_f32 v95, v106, v107\n" \
"v_cvt_pk_bf16_f32 v92, v116, v117\n" \
"v_fma_f32 v72, v72, s80, v22\n" \
"v_fma_f32 v73, v73, s80, v22\n" \
"v_fma_f32 v108, v108, s80, v22\n" \
"v_fma_f32 v109, v109, s80, v22\n" \
"v_fma_f32 v110, v110, s80, v22\n" \
"v_fma_f32 v111, v111, s80, v22\n" \
"v_fma_f32 v112, v112, s80, v22\n" \
"v_fma_f32 v113, v113, s80, v22\n" \
"v_fma_f32 v114, v114, s80, v22\n" \
"v_fma_f32 v115, v115, s80, v22\n" \
"v_mfma_f32_16x16x32_bf16 v[104:107], v[84:87], v[94:97], 0\n" \
"s_waitcnt lgkmcnt(2)\n" \
"v_mfma_f32_16x16x32_bf16 v[64:67], v[76:79], v[94:97], 0\n" \
"s_waitcnt lgkmcnt(0)\n" \
"v_mfma_f32_16x16x32_bf16 v[68:71], v[80:83], v[94:97], 0\n" \
"v_mfma_f32_16x16x32_bf16 v[104:107], v[88:91], v[92:95], v[104:107]\n" \
"v_mfma_f32_16x16x32_bf16 v[64:67], v[120:123], v[92:95], v[64:67]\n" \
"v_mfma_f32_16x16x32_bf16 v[68:71], v[124:127], v[92:95], v[68:71]\n" \
"v_exp_f32 v72, v72\n" \
"v_exp_f32 v73, v73\n" \
"v_exp_f32 v108, v108\n" \
"v_exp_f32 v109, v109\n" \
"v_exp_f32 v110, v110\n" \
"v_exp_f32 v111, v111\n" \
"v_exp_f32 v112, v112\n" \
"v_exp_f32 v113, v113\n" \
"v_exp_f32 v114, v114\n" \
"v_exp_f32 v115, v115\n" \
"v_cvt_pk_bf16_f32 v100, v108, v109\n" \
"v_cvt_pk_bf16_f32 v101, v110, v111\n" \
"v_cvt_pk_bf16_f32 v102, v112, v113\n" \
"v_cvt_pk_bf16_f32 v103, v114, v115\n" \
"v_cvt_pk_bf16_f32 v98, v72, v73\n" \
"s_nop 0\n" \
"v_mfma_f32_16x16x32_bf16 v[72:75], v[84:87], v[100:103], 0\n" \
"v_mfma_f32_16x16x32_bf16 v[108:111], v[76:79], v[100:103], 0\n" \
"v_mfma_f32_16x16x32_bf16 v[112:115], v[80:83], v[100:103], 0\n" \
"v_mfma_f32_16x16x32_bf16 v[72:75], v[88:91], v[98:101], v[72:75]\n" \
"v_mfma_f32_16x16x32_bf16 v[108:111], v[120:123], v[98:101], v[108:111]\n" \
"v_mfma_f32_16x16x32_bf16 v[112:115], v[124:127], v[98:101], v[112:115]\n" \
"v_rcp_f32 v104, v104\n" \
"s_nop 0\n" \
"v_mul_f32 v64, v64, v104\n" \
"v_mul_f32 v65, v65, v104\n" \
"v_mul_f32 v66, v66, v104\n" \
"v_mul_f32 v67, v67, v104\n" \
"v_mul_f32 v68, v68, v104\n" \
"v_mul_f32 v69, v69, v104\n" \
"v_mul_f32 v70, v70, v104\n" \
"v_mul_f32 v71, v71, v104\n" \
"v_cvt_pk_bf16_f32 v116, v64, v65\n" \
"v_cvt_pk_bf16_f32 v117, v66, v67\n" \
"v_cvt_pk_bf16_f32 v118, v68, v69\n" \
"v_cvt_pk_bf16_f32 v119, v70, v71\n" \
"buffer_store_dwordx4 v[116:119], v9, s[40:43], s76 offen sc1\n" \
"v_rcp_f32 v72, v72\n" \
"s_add_u32 s67, s76, 131072\n" \
"v_mul_f32 v108, v108, v72\n" \
"v_mul_f32 v109, v109, v72\n" \
"v_mul_f32 v110, v110, v72\n" \
"v_mul_f32 v111, v111, v72\n" \
"v_mul_f32 v112, v112, v72\n" \
"v_mul_f32 v113, v113, v72\n" \
"v_mul_f32 v114, v114, v72\n" \
"v_mul_f32 v115, v115, v72\n" \
"v_cvt_pk_bf16_f32 v92, v108, v109\n" \
"v_cvt_pk_bf16_f32 v93, v110, v111\n" \
"v_cvt_pk_bf16_f32 v94, v112, v113\n" \
"v_cvt_pk_bf16_f32 v95, v114, v115\n" \
"buffer_store_dwordx4 v[92:95], v9, s[40:43], s67 offen sc1\n" \
"s_add_u32 s76, s76, 524288\n" \
"s_nop 0\n" \
"s_cmp_ge_u32 s66, 32\n" \
"s_cbranch_scc1 A_fin%=\n" \
"buffer_load_dwordx4 v[112:115], v5, s[40:43], s75 offen offset:0 sc1\n" \
"buffer_load_dwordx4 v[116:119], v5, s[40:43], s75 offen offset:1024 sc1\n" \
"s_waitcnt vmcnt(2)\n" \
"s_barrier \n" \
"s_cmp_lg_u32 s64, 0\n" \
"s_cbranch_scc1 A_s3%=\n" \
"s_mov_b64 exec, 1\n" \
"buffer_store_dword v20, v10, s[40:43], s77 offen offset:32 sc1\n" \
"s_mov_b64 exec, -1\n" \
"A_s3%=:\n" \
"s_add_u32 s77, s77, 4096\n" \
"s_waitcnt vmcnt(0)\n" \
"s_branch A_loop%=\n" \
"A_fin%=:\n" \
"s_waitcnt vmcnt(0)\n" \
"s_barrier \n" \
"s_cmp_lg_u32 s64, 0\n" \
"s_cbranch_scc1 A_s4%=\n" \
"s_mov_b64 exec, 1\n" \
"buffer_store_dword v20, v10, s[40:43], s77 offen offset:32 sc1\n" \
"s_mov_b64 exec, -1\n" \
"A_s4%=:\n" \
"s_add_u32 s77, s77, 4096\n" \
"s_waitcnt vmcnt(0)\n" \
"s_branch ROLE_END%=\n" \
"ROLE_R%=:\n" \
"s_memrealtime s[88:89]\n" \
"s_waitcnt lgkmcnt(0)\n" \
"ds_read_b32 v20, v0 offset:19456\n" \
"ds_read_b32 v21, v0 offset:20480\n" \
"ds_read_b32 v22, v0 offset:21504\n" \
"ds_read_b32 v23, v0 offset:22528\n" \
"ds_read_b32 v24, v0 offset:23552\n" \
"ds_read_b32 v25, v0 offset:24576\n" \
"ds_read_b32 v1, v0 offset:0\n" \
"ds_read_b32 v2, v0 offset:1024\n" \
"ds_read_b32 v3, v0 offset:2048\n" \
"ds_read_b32 v4, v0 offset:3072\n" \
"ds_read_b32 v5, v0 offset:4096\n" \
"ds_read_b32 v6, v0 offset:5120\n" \
"ds_read_b32 v7, v0 offset:6144\n" \
"ds_read_b32 v8, v0 offset:7168\n" \
"ds_read_b32 v9, v0 offset:8192\n" \
"ds_read_b32 v10, v0 offset:9216\n" \
"ds_read_b32 v11, v0 offset:10240\n" \
"ds_read_b32 v12, v0 offset:11264\n" \
"ds_read_b32 v13, v0 offset:12288\n" \
"ds_read_b32 v14, v0 offset:13312\n" \
"ds_read_b32 v15, v0 offset:14336\n" \
"ds_read_b32 v16, v0 offset:15360\n" \
"ds_read_b32 v17, v0 offset:16384\n" \
"ds_read_b32 v18, v0 offset:17408\n" \
"ds_read_b32 v19, v0 offset:18432\n" \
"ds_read_b32 v28, v0 offset:25600\n" \
"ds_read_b32 v29, v0 offset:26624\n" \
"ds_read_b32 v30, v0 offset:27648\n" \
"ds_read_b32 v31, v0 offset:28672\n" \
"v_lshlrev_b32 v26, 2, v0\n" \
"v_and_b32 v26, 1023, v26\n" \
"ds_read_b32 v0, v0 offset:29696\n" \
"s_lshl_b32 s76, s64, 15\n" \
"s_add_u32 s76, s76, 32768\n" \
"s_waitcnt lgkmcnt(15)\n" \
"v_add_u32 v27, 65536, v25\n" \
"s_waitcnt vmcnt(24)\n" \
"ds_read_b64_tr_b16 v[32:33], v7 offset:24576\n" \
"ds_read_b64_tr_b16 v[34:35], v7 offset:24832\n" \
"ds_read_b64_tr_b16 v[48:49], v7 offset:24584\n" \
"ds_read_b64_tr_b16 v[50:51], v7 offset:24840\n" \
"ds_read_b64_tr_b16 v[36:37], v7 offset:26624\n" \
"ds_read_b64_tr_b16 v[38:39], v7 offset:26880\n" \
"ds_read_b64_tr_b16 v[52:53], v7 offset:26632\n" \
"ds_read_b64_tr_b16 v[54:55], v7 offset:26888\n" \
"ds_read_b64_tr_b16 v[40:41], v7 offset:28672\n" \
"ds_read_b64_tr_b16 v[42:43], v7 offset:28928\n" \
"ds_read_b64_tr_b16 v[56:57], v7 offset:28680\n" \
"ds_read_b64_tr_b16 v[58:59], v7 offset:28936\n" \
"ds_read_b64_tr_b16 v[44:45], v7 offset:30720\n" \
"ds_read_b64_tr_b16 v[46:47], v7 offset:30976\n" \
"ds_read_b64_tr_b16 v[60:61], v7 offset:30728\n" \
"ds_read_b64_tr_b16 v[62:63], v7 offset:30984\n" \
"s_memtime s[10:11]\n" \
"s_memrealtime s[12:13]\n" \
"s_waitcnt lgkmcnt(0)\n" \
"s_mov_b32 s9, s10\n" \
"s_lshl_b32 s8, s12, 3\n" \
"s_add_u32 m0, s76, 24576\n" \
"s_mov_b32 s75, 3145728\n" \
"buffer_load_dwordx4 v21, s[56:59], s75 offen lds\n" \
"s_add_u32 m0, s76, 25600\n" \
"s_mov_b32 s75, 3145792\n" \
"buffer_load_dwordx4 v21, s[56:59], s75 offen lds\n" \
"s_add_u32 m0, s76, 26624\n" \
"s_mov_b32 s75, 3407872\n" \
"buffer_load_dwordx4 v21, s[56:59], s75 offen lds\n" \
"s_add_u32 m0, s76, 27648\n" \
"s_mov_b32 s75, 3407936\n" \
"buffer_load_dwordx4 v21, s[56:59], s75 offen lds\n" \
"s_add_u32 m0, s76, 28672\n" \
"s_mov_b32 s75, 3670016\n" \
"buffer_load_dwordx4 v21, s[56:59], s75 offen lds\n" \
"s_add_u32 m0, s76, 29696\n" \
"s_mov_b32 s75, 3670080\n" \
"buffer_load_dwordx4 v21, s[56:59], s75 offen lds\n" \
"s_add_u32 m0, s76, 30720\n" \
"s_mov_b32 s75, 3932160\n" \
"buffer_load_dwordx4 v21, s[56:59], s75 offen lds\n" \
"s_add_u32 m0, s76, 31744\n" \
"s_mov_b32 s75, 3932224\n" \
"buffer_load_dwordx4 v21, s[56:59], s75 offen lds\n" \
"buffer_load_dword v88, v16, s[48:51], 0 offen\n" \
"buffer_load_dword v89, v16, s[52:55], 0 offen\n" \
"s_waitcnt vmcnt(10)\n" \
"s_barrier \n" \
"ds_read_b128 v[96:99], v25 offset:0\n" \
"ds_read_b128 v[100:103], v25 offset:1024\n" \
"ds_read_b128 v[104:107], v25 offset:32768\n" \
"ds_read_b128 v[108:111], v25 offset:33792\n" \
"ds_read_b128 v[112:115], v27 offset:0\n" \
"ds_read_b128 v[116:119], v27 offset:1024\n" \
"ds_read_b128 v[120:123], v27 offset:32768\n" \
"ds_read_b128 v[124:127], v27 offset:33792\n" \
"s_waitcnt lgkmcnt(7)\n" \
"v_mfma_f32_16x16x32_bf16 v[64:67], v[96:99], v[32:35], 0\n" \
"ds_write_b128 v15, v[32:35] offset:0\n" \
"v_mfma_f32_16x16x32_bf16 v[68:71], v[96:99], v[48:51], 0\n" \
"ds_read_b128 v[96:99], v25 offset:2048\n" \
"s_waitcnt lgkmcnt(8)\n" \
"v_mfma_f32_16x16x32_bf16 v[64:67], v[100:103], v[36:39], v[64:67]\n" \
"ds_write_b128 v15, v[36:39] offset:1024\n" \
"v_mfma_f32_16x16x32_bf16 v[68:71], v[100:103], v[52:55], v[68:71]\n" \
"ds_read_b128 v[100:103], v25 offset:3072\n" \
"s_waitcnt lgkmcnt(9)\n" \
"v_mfma_f32_16x16x32_bf16 v[64:67], v[104:107], v[40:43], v[64:67]\n" \
"v_mfma_f32_16x16x32_bf16 v[68:71], v[104:107], v[56:59], v[68:71]\n" \
"ds_read_b128 v[104:107], v25 offset:34816\n" \
"s_waitcnt lgkmcnt(9)\n" \
"v_mfma_f32_16x16x32_bf16 v[64:67], v[108:111], v[44:47], v[64:67]\n" \
"ds_write_b128 v15, v[40:43] offset:2048\n" \
"v_mfma_f32_16x16x32_bf16 v[68:71], v[108:111], v[60:63], v[68:71]\n" \
"ds_read_b128 v[108:111], v25 offset:35840\n" \
"s_waitcnt lgkmcnt(10)\n" \
"v_mfma_f32_16x16x32_bf16 v[72:75], v[112:115], v[32:35], 0\n" \
"ds_write_b128 v15, v[44:47] offset:3072\n" \
"v_mfma_f32_16x16x32_bf16 v[76:79], v[112:115], v[48:51], 0\n" \
"ds_read_b128 v[112:115], v27 offset:2048\n" \
"s_waitcnt lgkmcnt(11)\n" \
"v_mfma_f32_16x16x32_bf16 v[72:75], v[116:119], v[36:39], v[72:75]\n" \
"ds_write_b128 v15, v[48:51] offset:4096\n" \
"v_mfma_f32_16x16x32_bf16 v[76:79], v[116:119], v[52:55], v[76:79]\n" \
"ds_read_b128 v[116:119], v27 offset:3072\n" \
"s_waitcnt lgkmcnt(12)\n" \
"v_mfma_f32_16x16x32_bf16 v[72:75], v[120:123], v[40:43], v[72:75]\n" \
"v_cvt_pk_bf16_f32 v128, v64, v65\n" \
"v_cvt_pk_bf16_f32 v129, v66, v67\n" \
"v_cvt_pk_bf16_f32 v192, v68, v69\n" \
"v_cvt_pk_bf16_f32 v193, v70, v71\n" \
"v_mfma_f32_16x16x32_bf16 v[76:79], v[120:123], v[56:59], v[76:79]\n" \
"ds_read_b128 v[120:123], v27 offset:34816\n" \
"s_waitcnt lgkmcnt(12)\n" \
"v_mfma_f32_16x16x32_bf16 v[72:75], v[124:127], v[44:47], v[72:75]\n" \
"ds_write_b128 v15, v[52:55] offset:5120\n" \
"v_mfma_f32_16x16x32_bf16 v[76:79], v[124:127], v[60:63], v[76:79]\n" \
"ds_read_b128 v[124:127], v27 offset:35840\n" \
"s_waitcnt lgkmcnt(12)\n" \
"v_mfma_f32_16x16x32_bf16 v[80:83], v[96:99], v[32:35], 0\n" \
"ds_write_b128 v15, v[56:59] offset:6144\n" \
"v_mfma_f32_16x16x32_bf16 v[84:87], v[96:99], v[48:51], 0\n" \
"ds_read_b128 v[96:99], v25 offset:4096\n" \
"s_waitcnt lgkmcnt(12)\n" \
"v_mfma_f32_16x16x32_bf16 v[80:83], v[100:103], v[36:39], v[80:83]\n" \
"ds_write_b128 v15, v[60:63] offset:7168\n" \
"v_mfma_f32_16x16x32_bf16 v[84:87], v[100:103], v[52:55], v[84:87]\n" \
"ds_read_b128 v[100:103], v25 offset:5120\n" \
"s_waitcnt lgkmcnt(13)\n" \
"v_mfma_f32_16x16x32_bf16 v[80:83], v[104:107], v[40:43], v[80:83]\n" \
"v_cvt_pk_bf16_f32 v130, v72, v73\n" \
"v_cvt_pk_bf16_f32 v131, v74, v75\n" \
"v_cvt_pk_bf16_f32 v194, v76, v77\n" \
"v_cvt_pk_bf16_f32 v195, v78, v79\n" \
"v_mfma_f32_16x16x32_bf16 v[84:87], v[104:107], v[56:59], v[84:87]\n" \
"ds_read_b128 v[104:107], v25 offset:36864\n" \
"s_waitcnt lgkmcnt(12)\n" \
"v_mfma_f32_16x16x32_bf16 v[80:83], v[108:111], v[44:47], v[80:83]\n" \
"v_mfma_f32_16x16x32_bf16 v[84:87], v[108:111], v[60:63], v[84:87]\n" \
"ds_read_b128 v[108:111], v25 offset:37888\n" \
"s_waitcnt lgkmcnt(11)\n" \
"v_mfma_f32_16x16x32_bf16 v[64:67], v[112:115], v[32:35], 0\n" \
"v_mfma_f32_16x16x32_bf16 v[68:71], v[112:115], v[48:51], 0\n" \
"ds_read_b128 v[112:115], v27 offset:4096\n" \
"s_waitcnt lgkmcnt(10)\n" \
"v_mfma_f32_16x16x32_bf16 v[64:67], v[116:119], v[36:39], v[64:67]\n" \
"v_mfma_f32_16x16x32_bf16 v[68:71], v[116:119], v[52:55], v[68:71]\n" \
"ds_read_b128 v[116:119], v27 offset:5120\n" \
"s_waitcnt lgkmcnt(10)\n" \
"v_mfma_f32_16x16x32_bf16 v[64:67], v[120:123], v[40:43], v[64:67]\n" \
"v_cvt_pk_bf16_f32 v132, v80, v81\n" \
"v_cvt_pk_bf16_f32 v133, v82, v83\n" \
"v_cvt_pk_bf16_f32 v196, v84, v85\n" \
"v_cvt_pk_bf16_f32 v197, v86, v87\n" \
"v_mfma_f32_16x16x32_bf16 v[68:71], v[120:123], v[56:59], v[68:71]\n" \
"ds_read_b128 v[120:123], v27 offset:36864\n" \
"s_waitcnt lgkmcnt(9)\n" \
"v_mfma_f32_16x16x32_bf16 v[64:67], v[124:127], v[44:47], v[64:67]\n" \
"v_mfma_f32_16x16x32_bf16 v[68:71], v[124:127], v[60:63], v[68:71]\n" \
"ds_read_b128 v[124:127], v27 offset:37888\n" \
"s_waitcnt lgkmcnt(8)\n" \
"v_mfma_f32_16x16x32_bf16 v[72:75], v[96:99], v[32:35], 0\n" \
"v_mfma_f32_16x16x32_bf16 v[76:79], v[96:99], v[48:51], 0\n" \
"ds_read_b128 v[96:99], v25 offset:6144\n" \
"s_waitcnt lgkmcnt(7)\n" \
"v_mfma_f32_16x16x32_bf16 v[72:75], v[100:103], v[36:39], v[72:75]\n" \
"v_mfma_f32_16x16x32_bf16 v[76:79], v[100:103], v[52:55], v[76:79]\n" \
"ds_read_b128 v[100:103], v25 offset:7168\n" \
"s_waitcnt lgkmcnt(7)\n" \
"v_mfma_f32_16x16x32_bf16 v[72:75], v[104:107], v[40:43], v[72:75]\n" \
"v_cvt_pk_bf16_f32 v134, v64, v65\n" \
"v_cvt_pk_bf16_f32 v135, v66, v67\n" \
"v_cvt_pk_bf16_f32 v198, v68, v69\n" \
"v_cvt_pk_bf16_f32 v199, v70, v71\n" \
"v_mfma_f32_16x16x32_bf16 v[76:79], v[104:107], v[56:59], v[76:79]\n" \
"ds_read_b128 v[104:107], v25 offset:38912\n" \
"s_waitcnt lgkmcnt(7)\n" \
"v_mfma_f32_16x16x32_bf16 v[72:75], v[108:111], v[44:47], v[72:75]\n" \
"v_mfma_f32_16x16x32_bf16 v[76:79], v[108:111], v[60:63], v[76:79]\n" \
"ds_read_b128 v[108:111], v25 offset:39936\n" \
"s_waitcnt lgkmcnt(7)\n" \
"v_mfma_f32_16x16x32_bf16 v[80:83], v[112:115], v[32:35], 0\n" \
"v_mfma_f32_16x16x32_bf16 v[84:87], v[112:115], v[48:51], 0\n" \
"ds_read_b128 v[112:115], v27 offset:6144\n" \
"s_waitcnt lgkmcnt(7)\n" \
"v_mfma_f32_16x16x32_bf16 v[80:83], v[116:119], v[36:39], v[80:83]\n" \
"v_mfma_f32_16x16x32_bf16 v[84:87], v[116:119], v[52:55], v[84:87]\n" \
"ds_read_b128 v[116:119], v27 offset:7168\n" \
"s_waitcnt lgkmcnt(7)\n" \
"v_mfma_f32_16x16x32_bf16 v[80:83], v[120:123], v[40:43], v[80:83]\n" \
"v_cvt_pk_bf16_f32 v136, v72, v73\n" \
"v_cvt_pk_bf16_f32 v137, v74, v75\n" \
"v_cvt_pk_bf16_f32 v200, v76, v77\n" \
"v_cvt_pk_bf16_f32 v201, v78, v79\n" \
"v_mfma_f32_16x16x32_bf16 v[84:87], v[120:123], v[56:59], v[84:87]\n" \
"ds_read_b128 v[120:123], v27 offset:38912\n" \
"s_waitcnt lgkmcnt(7)\n" \
"v_mfma_f32_16x16x32_bf16 v[80:83], v[124:127], v[44:47], v[80:83]\n" \
"v_mfma_f32_16x16x32_bf16 v[84:87], v[124:127], v[60:63], v[84:87]\n" \
"ds_read_b128 v[124:127], v27 offset:39936\n" \
"s_waitcnt lgkmcnt(7)\n" \
"v_mfma_f32_16x16x32_bf16 v[64:67], v[96:99], v[32:35], 0\n" \
"v_mfma_f32_16x16x32_bf16 v[68:71], v[96:99], v[48:51], 0\n" \
"ds_read_b128 v[96:99], v25 offset:8192\n" \
"s_waitcnt lgkmcnt(7)\n" \
"v_mfma_f32_16x16x32_bf16 v[64:67], v[100:103], v[36:39], v[64:67]\n" \
"v_mfma_f32_16x16x32_bf16 v[68:71], v[100:103], v[52:55], v[68:71]\n" \
"ds_read_b128 v[100:103], v25 offset:9216\n" \
"s_waitcnt lgkmcnt(7)\n" \
"v_mfma_f32_16x16x32_bf16 v[64:67], v[104:107], v[40:43], v[64:67]\n" \
"v_cvt_pk_bf16_f32 v138, v80, v81\n" \
"v_cvt_pk_bf16_f32 v139, v82, v83\n" \
"v_cvt_pk_bf16_f32 v202, v84, v85\n" \
"v_cvt_pk_bf16_f32 v203, v86, v87\n" \
"v_mfma_f32_16x16x32_bf16 v[68:71], v[104:107], v[56:59], v[68:71]\n" \
"ds_read_b128 v[104:107], v25 offset:40960\n" \
"s_waitcnt lgkmcnt(7)\n" \
"v_mfma_f32_16x16x32_bf16 v[64:67], v[108:111], v[44:47], v[64:67]\n" \
"v_mfma_f32_16x16x32_bf16 v[68:71], v[108:111], v[60:63], v[68:71]\n" \
"ds_read_b128 v[108:111], v25 offset:41984\n" \
"s_waitcnt lgkmcnt(7)\n" \
"v_mfma_f32_16x16x32_bf16 v[72:75], v[112:115], v[32:35], 0\n" \
"v_mfma_f32_16x16x32_bf16 v[76:79], v[112:115], v[48:51], 0\n" \
"ds_read_b128 v[112:115], v27 offset:8192\n" \
"s_waitcnt lgkmcnt(7)\n" \
"v_mfma_f32_16x16x32_bf16 v[72:75], v[116:119], v[36:39], v[72:75]\n" \
"v_mfma_f32_16x16x32_bf16 v[76:79], v[116:119], v[52:55], v[76:79]\n" \
"ds_read_b128 v[116:119], v27 offset:9216\n" \
"s_waitcnt lgkmcnt(7)\n" \
"v_mfma_f32_16x16x32_bf16 v[72:75], v[120:123], v[40:43], v[72:75]\n" \
"v_cvt_pk_bf16_f32 v140, v64, v65\n" \
"v_cvt_pk_bf16_f32 v141, v66, v67\n" \
"v_cvt_pk_bf16_f32 v204, v68, v69\n" \
"v_cvt_pk_bf16_f32 v205, v70, v71\n" \
"v_mfma_f32_16x16x32_bf16 v[76:79], v[120:123], v[56:59], v[76:79]\n" \
"ds_read_b128 v[120:123], v27 offset:40960\n" \
"s_waitcnt lgkmcnt(7)\n" \
"v_mfma_f32_16x16x32_bf16 v[72:75], v[124:127], v[44:47], v[72:75]\n" \
"v_mfma_f32_16x16x32_bf16 v[76:79], v[124:127], v[60:63], v[76:79]\n" \
"ds_read_b128 v[124:127], v27 offset:41984\n" \
"s_waitcnt vmcnt(2)\n" \
"s_barrier \n" \
"s_waitcnt lgkmcnt(7)\n" \
"v_mfma_f32_16x16x32_bf16 v[80:83], v[96:99], v[32:35], 0\n" \
"s_add_u32 m0, s76, 0\n" \
"v_mfma_f32_16x16x32_bf16 v[84:87], v[96:99], v[48:51], 0\n" \
"ds_read_b128 v[96:99], v25 offset:10240\n" \
"s_waitcnt lgkmcnt(7)\n" \
"v_mfma_f32_16x16x32_bf16 v[80:83], v[100:103], v[36:39], v[80:83]\n" \
"s_mov_b32 s75, 0\n" \
"v_mfma_f32_16x16x32_bf16 v[84:87], v[100:103], v[52:55], v[84:87]\n" \
"ds_read_b128 v[100:103], v25 offset:11264\n" \
"s_waitcnt lgkmcnt(7)\n" \
"v_mfma_f32_16x16x32_bf16 v[80:83], v[104:107], v[40:43], v[80:83]\n" \
"v_cvt_pk_bf16_f32 v142, v72, v73\n" \
"v_cvt_pk_bf16_f32 v143, v74, v75\n" \
"v_cvt_pk_bf16_f32 v206, v76, v77\n" \
"v_cvt_pk_bf16_f32 v207, v78, v79\n" \
"v_mfma_f32_16x16x32_bf16 v[84:87], v[104:107], v[56:59], v[84:87]\n" \
"ds_read_b128 v[104:107], v25 offset:43008\n" \
"s_waitcnt lgkmcnt(7)\n" \
"v_mfma_f32_16x16x32_bf16 v[80:83], v[108:111], v[44:47], v[80:83]\n" \
"buffer_load_dwordx4 v20, s[48:51], s75 offen lds\n" \
"v_mfma_f32_16x16x32_bf16 v[84:87], v[108:111], v[60:63], v[84:87]\n" \
"ds_read_b128 v[108:111], v25 offset:44032\n" \
"s_waitcnt lgkmcnt(7)\n" \
"v_mfma_f32_16x16x32_bf16 v[64:67], v[112:115], v[32:35], 0\n" \
"s_add_u32 m0, s76, 1024\n" \
"v_mfma_f32_16x16x32_bf16 v[68:71], v[112:115], v[48:51], 0\n" \
"ds_read_b128 v[112:115], v27 offset:10240\n" \
"s_waitcnt lgkmcnt(7)\n" \
"v_mfma_f32_16x16x32_bf16 v[64:67], v[116:119], v[36:39], v[64:67]\n" \
"s_mov_b32 s75, 131072\n" \
"v_mfma_f32_16x16x32_bf16 v[68:71], v[116:119], v[52:55], v[68:71]\n" \
"ds_read_b128 v[116:119], v27 offset:11264\n" \
"s_waitcnt lgkmcnt(7)\n" \
"v_mfma_f32_16x16x32_bf16 v[64:67], v[120:123], v[40:43], v[64:67]\n" \
"v_cvt_pk_bf16_f32 v144, v80, v81\n" \
"v_cvt_pk_bf16_f32 v145, v82, v83\n" \
"v_cvt_pk_bf16_f32 v208, v84, v85\n" \
"v_cvt_pk_bf16_f32 v209, v86, v87\n" \
"v_mfma_f32_16x16x32_bf16 v[68:71], v[120:123], v[56:59], v[68:71]\n" \
"ds_read_b128 v[120:123], v27 offset:43008\n" \
"s_waitcnt lgkmcnt(7)\n" \
"v_mfma_f32_16x16x32_bf16 v[64:67], v[124:127], v[44:47], v[64:67]\n" \
"buffer_load_dwordx4 v20, s[48:51], s75 offen lds\n" \
"v_mfma_f32_16x16x32_bf16 v[68:71], v[124:127], v[60:63], v[68:71]\n" \
"ds_read_b128 v[124:127], v27 offset:44032\n" \
"s_waitcnt lgkmcnt(7)\n" \
"v_mfma_f32_16x16x32_bf16 v[72:75], v[96:99], v[32:35], 0\n" \
"s_add_u32 m0, s76, 2048\n" \
"v_mfma_f32_16x16x32_bf16 v[76:79], v[96:99], v[48:51], 0\n" \
"ds_read_b128 v[96:99], v25 offset:12288\n" \
"s_waitcnt lgkmcnt(7)\n" \
"v_mfma_f32_16x16x32_bf16 v[72:75], v[100:103], v[36:39], v[72:75]\n" \
"s_mov_b32 s75, 262144\n" \
"v_mfma_f32_16x16x32_bf16 v[76:79], v[100:103], v[52:55], v[76:79]\n" \
"ds_read_b128 v[100:103], v25 offset:13312\n" \
"s_waitcnt lgkmcnt(7)\n" \
"v_mfma_f32_16x16x32_bf16 v[72:75], v[104:107], v[40:43], v[72:75]\n" \
"v_cvt_pk_bf16_f32 v146, v64, v65\n" \
"v_cvt_pk_bf16_f32 v147, v66, v67\n" \
"v_cvt_pk_bf16_f32 v210, v68, v69\n" \
"v_cvt_pk_bf16_f32 v211, v70, v71\n" \
"v_mfma_f32_16x16x32_bf16 v[76:79], v[104:107], v[56:59], v[76:79]\n" \
"ds_read_b128 v[104:107], v25 offset:45056\n" \
"s_waitcnt lgkmcnt(7)\n" \
"v_mfma_f32_16x16x32_bf16 v[72:75], v[108:111], v[44:47], v[72:75]\n" \
"buffer_load_dwordx4 v20, s[48:51], s75 offen lds\n" \
"v_mfma_f32_16x16x32_bf16 v[76:79], v[108:111], v[60:63], v[76:79]\n" \
"ds_read_b128 v[108:111], v25 offset:46080\n" \
"s_waitcnt lgkmcnt(7)\n" \
"v_mfma_f32_16x16x32_bf16 v[80:83], v[112:115], v[32:35], 0\n" \
"s_add_u32 m0, s76, 3072\n" \
"v_mfma_f32_16x16x32_bf16 v[84:87], v[112:115], v[48:51], 0\n" \
"ds_read_b128 v[112:115], v27 offset:12288\n" \
"s_waitcnt lgkmcnt(7)\n" \
"v_mfma_f32_16x16x32_bf16 v[80:83], v[116:119], v[36:39], v[80:83]\n" \
"s_mov_b32 s75, 393216\n" \
"v_mfma_f32_16x16x32_bf16 v[84:87], v[116:119], v[52:55], v[84:87]\n" \
"ds_read_b128 v[116:119], v27 offset:13312\n" \
"s_waitcnt lgkmcnt(7)\n" \
"v_mfma_f32_16x16x32_bf16 v[80:83], v[120:123], v[40:43], v[80:83]\n" \
"v_cvt_pk_bf16_f32 v148, v72, v73\n" \
"v_cvt_pk_bf16_f32 v149, v74, v75\n" \
"v_cvt_pk_bf16_f32 v212, v76, v77\n" \
"v_cvt_pk_bf16_f32 v213, v78, v79\n" \
"v_mfma_f32_16x16x32_bf16 v[84:87], v[120:123], v[56:59], v[84:87]\n" \
"ds_read_b128 v[120:123], v27 offset:45056\n" \
"s_waitcnt lgkmcnt(7)\n" \
"v_mfma_f32_16x16x32_bf16 v[80:83], v[124:127], v[44:47], v[80:83]\n" \
"buffer_load_dwordx4 v20, s[48:51], s75 offen lds\n" \
"v_mfma_f32_16x16x32_bf16 v[84:87], v[124:127], v[60:63], v[84:87]\n" \
"ds_read_b128 v[124:127], v27 offset:46080\n" \
"s_waitcnt lgkmcnt(7)\n" \
"v_mfma_f32_16x16x32_bf16 v[64:67], v[96:99], v[32:35], 0\n" \
"s_add_u32 m0, s76, 4096\n" \
"v_mfma_f32_16x16x32_bf16 v[68:71], v[96:99], v[48:51], 0\n" \
"ds_read_b128 v[96:99], v25 offset:14336\n" \
"s_waitcnt lgkmcnt(7)\n" \
"v_mfma_f32_16x16x32_bf16 v[64:67], v[100:103], v[36:39], v[64:67]\n" \
"s_mov_b32 s75, 524288\n" \
"v_mfma_f32_16x16x32_bf16 v[68:71], v[100:103], v[52:55], v[68:71]\n" \
"ds_read_b128 v[100:103], v25 offset:15360\n" \
"s_waitcnt lgkmcnt(7)\n" \
"v_mfma_f32_16x16x32_bf16 v[64:67], v[104:107], v[40:43], v[64:67]\n" \
"v_cvt_pk_bf16_f32 v150, v80, v81\n" \
"v_cvt_pk_bf16_f32 v151, v82, v83\n" \
"v_cvt_pk_bf16_f32 v214, v84, v85\n" \
"v_cvt_pk_bf16_f32 v215, v86, v87\n" \
"v_mfma_f32_16x16x32_bf16 v[68:71], v[104:107], v[56:59], v[68:71]\n" \
"ds_read_b128 v[104:107], v25 offset:47104\n" \
"s_waitcnt lgkmcnt(7)\n" \
"v_mfma_f32_16x16x32_bf16 v[64:67], v[108:111], v[44:47], v[64:67]\n" \
"buffer_load_dwordx4 v20, s[48:51], s75 offen lds\n" \
"v_mfma_f32_16x16x32_bf16 v[68:71], v[108:111], v[60:63], v[68:71]\n" \
"ds_read_b128 v[108:111], v25 offset:48128\n" \
"s_waitcnt lgkmcnt(7)\n" \
"v_mfma_f32_16x16x32_bf16 v[72:75], v[112:115], v[32:35], 0\n" \
"s_add_u32 m0, s76, 5120\n" \
"v_mfma_f32_16x16x32_bf16 v[76:79], v[112:115], v[48:51], 0\n" \
"ds_read_b128 v[112:115], v27 offset:14336\n" \
"s_waitcnt lgkmcnt(7)\n" \
"v_mfma_f32_16x16x32_bf16 v[72:75], v[116:119], v[36:39], v[72:75]\n" \
"s_mov_b32 s75, 655360\n" \
"v_mfma_f32_16x16x32_bf16 v[76:79], v[116:119], v[52:55], v[76:79]\n" \
"ds_read_b128 v[116:119], v27 offset:15360\n" \
"s_waitcnt lgkmcnt(7)\n" \
"v_mfma_f32_16x16x32_bf16 v[72:75], v[120:123], v[40:43], v[72:75]\n" \
"v_cvt_pk_bf16_f32 v152, v64, v65\n" \
"v_cvt_pk_bf16_f32 v153, v66, v67\n" \
"v_cvt_pk_bf16_f32 v216, v68, v69\n" \
"v_cvt_pk_bf16_f32 v217, v70, v71\n" \
"v_mfma_f32_16x16x32_bf16 v[76:79], v[120:123], v[56:59], v[76:79]\n" \
"ds_read_b128 v[120:123], v27 offset:47104\n" \
"s_waitcnt lgkmcnt(7)\n" \
"v_mfma_f32_16x16x32_bf16 v[72:75], v[124:127], v[44:47], v[72:75]\n" \
"buffer_load_dwordx4 v20, s[48:51], s75 offen lds\n" \
"v_mfma_f32_16x16x32_bf16 v[76:79], v[124:127], v[60:63], v[76:79]\n" \
"ds_read_b128 v[124:127], v27 offset:48128\n" \
"s_waitcnt lgkmcnt(7)\n" \
"v_mfma_f32_16x16x32_bf16 v[80:83], v[96:99], v[32:35], 0\n" \
"s_add_u32 m0, s76, 6144\n" \
"v_mfma_f32_16x16x32_bf16 v[84:87], v[96:99], v[48:51], 0\n" \
"ds_read_b128 v[96:99], v25 offset:16384\n" \
"s_waitcnt lgkmcnt(7)\n" \
"v_mfma_f32_16x16x32_bf16 v[80:83], v[100:103], v[36:39], v[80:83]\n" \
"s_mov_b32 s75, 786432\n" \
"v_mfma_f32_16x16x32_bf16 v[84:87], v[100:103], v[52:55], v[84:87]\n" \
"ds_read_b128 v[100:103], v25 offset:17408\n" \
"s_waitcnt lgkmcnt(7)\n" \
"v_mfma_f32_16x16x32_bf16 v[80:83], v[104:107], v[40:43], v[80:83]\n" \
"v_cvt_pk_bf16_f32 v154, v72, v73\n" \
"v_cvt_pk_bf16_f32 v155, v74, v75\n" \
"v_cvt_pk_bf16_f32 v218, v76, v77\n" \
"v_cvt_pk_bf16_f32 v219, v78, v79\n" \
"v_mfma_f32_16x16x32_bf16 v[84:87], v[104:107], v[56:59], v[84:87]\n" \
"ds_read_b128 v[104:107], v25 offset:49152\n" \
"s_waitcnt lgkmcnt(7)\n" \
"v_mfma_f32_16x16x32_bf16 v[80:83], v[108:111], v[44:47], v[80:83]\n" \
"buffer_load_dwordx4 v20, s[48:51], s75 offen lds\n" \
"v_mfma_f32_16x16x32_bf16 v[84:87], v[108:111], v[60:63], v[84:87]\n" \
"ds_read_b128 v[108:111], v25 offset:50176\n" \
"s_waitcnt lgkmcnt(7)\n" \
"v_mfma_f32_16x16x32_bf16 v[64:67], v[112:115], v[32:35], 0\n" \
"s_add_u32 m0, s76, 7168\n" \
"v_mfma_f32_16x16x32_bf16 v[68:71], v[112:115], v[48:51], 0\n" \
"ds_read_b128 v[112:115], v27 offset:16384\n" \
"s_waitcnt lgkmcnt(7)\n" \
"v_mfma_f32_16x16x32_bf16 v[64:67], v[116:119], v[36:39], v[64:67]\n" \
"s_mov_b32 s75, 917504\n" \
"v_mfma_f32_16x16x32_bf16 v[68:71], v[116:119], v[52:55], v[68:71]\n" \
"ds_read_b128 v[116:119], v27 offset:17408\n" \
"s_waitcnt lgkmcnt(7)\n" \
"v_mfma_f32_16x16x32_bf16 v[64:67], v[120:123], v[40:43], v[64:67]\n" \
"v_cvt_pk_bf16_f32 v156, v80, v81\n" \
"v_cvt_pk_bf16_f32 v157, v82, v83\n" \
"v_cvt_pk_bf16_f32 v220, v84, v85\n" \
"v_cvt_pk_bf16_f32 v221, v86, v87\n" \
"v_mfma_f32_16x16x32_bf16 v[68:71], v[120:123], v[56:59], v[68:71]\n" \
"ds_read_b128 v[120:123], v27 offset:49152\n" \
"s_waitcnt lgkmcnt(7)\n" \
"v_mfma_f32_16x16x32_bf16 v[64:67], v[124:127], v[44:47], v[64:67]\n" \
"buffer_load_dwordx4 v20, s[48:51], s75 offen lds\n" \
"v_mfma_f32_16x16x32_bf16 v[68:71], v[124:127], v[60:63], v[68:71]\n" \
"ds_read_b128 v[124:127], v27 offset:50176\n" \
"s_barrier \n" \
"s_waitcnt lgkmcnt(7)\n" \
"v_mfma_f32_16x16x32_bf16 v[72:75], v[96:99], v[32:35], 0\n" \
"s_add_u32 m0, s76, 8192\n" \
"s_mov_b32 s75, 1048576\n" \
"buffer_load_dwordx4 v20, s[48:51], s75 offen lds\n" \
"v_mfma_f32_16x16x32_bf16 v[76:79], v[96:99], v[48:51], 0\n" \
"ds_read_b128 v[96:99], v25 offset:18432\n" \
"s_waitcnt lgkmcnt(7)\n" \
"v_mfma_f32_16x16x32_bf16 v[72:75], v[100:103], v[36:39], v[72:75]\n" \
"s_add_u32 m0, s76, 9216\n" \
"s_mov_b32 s75, 1179648\n" \
"buffer_load_dwordx4 v20, s[48:51], s75 offen lds\n" \
"v_mfma_f32_16x16x32_bf16 v[76:79], v[100:103], v[52:55], v[76:79]\n" \
"ds_read_b128 v[100:103], v25 offset:19456\n" \
"s_waitcnt lgkmcnt(7)\n" \
"v_mfma_f32_16x16x32_bf16 v[72:75], v[104:107], v[40:43], v[72:75]\n" \
"v_cvt_pk_bf16_f32 v158, v64, v65\n" \
"v_cvt_pk_bf16_f32 v159, v66, v67\n" \
"v_cvt_pk_bf16_f32 v222, v68, v69\n" \
"v_cvt_pk_bf16_f32 v223, v70, v71\n" \
"v_mfma_f32_16x16x32_bf16 v[76:79], v[104:107], v[56:59], v[76:79]\n" \
"ds_read_b128 v[104:107], v25 offset:51200\n" \
"s_waitcnt lgkmcnt(7)\n" \
"v_mfma_f32_16x16x32_bf16 v[72:75], v[108:111], v[44:47], v[72:75]\n" \
"s_add_u32 m0, s76, 10240\n" \
"s_mov_b32 s75, 1310720\n" \
"buffer_load_dwordx4 v20, s[48:51], s75 offen lds\n" \
"v_mfma_f32_16x16x32_bf16 v[76:79], v[108:111], v[60:63], v[76:79]\n" \
"ds_read_b128 v[108:111], v25 offset:52224\n" \
"s_waitcnt lgkmcnt(7)\n" \
"v_mfma_f32_16x16x32_bf16 v[80:83], v[112:115], v[32:35], 0\n" \
"s_add_u32 m0, s76, 11264\n" \
"s_mov_b32 s75, 1441792\n" \
"buffer_load_dwordx4 v20, s[48:51], s75 offen lds\n" \
"v_mfma_f32_16x16x32_bf16 v[84:87], v[112:115], v[48:51], 0\n" \
"ds_read_b128 v[112:115], v27 offset:18432\n" \
"s_waitcnt lgkmcnt(7)\n" \
"v_mfma_f32_16x16x32_bf16 v[80:83], v[116:119], v[36:39], v[80:83]\n" \
"s_add_u32 m0, s76, 12288\n" \
"s_mov_b32 s75, 1572864\n" \
"buffer_load_dwordx4 v20, s[48:51], s75 offen lds\n" \
"v_mfma_f32_16x16x32_bf16 v[84:87], v[116:119], v[52:55], v[84:87]\n" \
"ds_read_b128 v[116:119], v27 offset:19456\n" \
"s_waitcnt lgkmcnt(7)\n" \
"v_mfma_f32_16x16x32_bf16 v[80:83], v[120:123], v[40:43], v[80:83]\n" \
"v_cvt_pk_bf16_f32 v160, v72, v73\n" \
"v_cvt_pk_bf16_f32 v161, v74, v75\n" \
"v_cvt_pk_bf16_f32 v224, v76, v77\n" \
"v_cvt_pk_bf16_f32 v225, v78, v79\n" \
"v_mfma_f32_16x16x32_bf16 v[84:87], v[120:123], v[56:59], v[84:87]\n" \
"ds_read_b128 v[120:123], v27 offset:51200\n" \
"s_waitcnt lgkmcnt(7)\n" \
"v_mfma_f32_16x16x32_bf16 v[80:83], v[124:127], v[44:47], v[80:83]\n" \
"s_add_u32 m0, s76, 13312\n" \
"s_mov_b32 s75, 1703936\n" \
"buffer_load_dwordx4 v20, s[48:51], s75 offen lds\n" \
"v_mfma_f32_16x16x32_bf16 v[84:87], v[124:127], v[60:63], v[84:87]\n" \
"ds_read_b128 v[124:127], v27 offset:52224\n" \
"s_waitcnt lgkmcnt(7)\n" \
"v_mfma_f32_16x16x32_bf16 v[64:67], v[96:99], v[32:35], 0\n" \
"s_add_u32 m0, s76, 14336\n" \
"s_mov_b32 s75, 1835008\n" \
"buffer_load_dwordx4 v20, s[48:51], s75 offen lds\n" \
"v_mfma_f32_16x16x32_bf16 v[68:71], v[96:99], v[48:51], 0\n" \
"ds_read_b128 v[96:99], v25 offset:20480\n" \
"s_waitcnt lgkmcnt(7)\n" \
"v_mfma_f32_16x16x32_bf16 v[64:67], v[100:103], v[36:39], v[64:67]\n" \
"s_add_u32 m0, s76, 15360\n" \
"s_mov_b32 s75, 1966080\n" \
"buffer_load_dwordx4 v20, s[48:51], s75 offen lds\n" \
"v_mfma_f32_16x16x32_bf16 v[68:71], v[100:103], v[52:55], v[68:71]\n" \
"ds_read_b128 v[100:103], v25 offset:21504\n" \
"s_waitcnt lgkmcnt(7)\n" \
"v_mfma_f32_16x16x32_bf16 v[64:67], v[104:107], v[40:43], v[64:67]\n" \
"v_cvt_pk_bf16_f32 v162, v80, v81\n" \
"v_cvt_pk_bf16_f32 v163, v82, v83\n" \
"v_cvt_pk_bf16_f32 v226, v84, v85\n" \
"v_cvt_pk_bf16_f32 v227, v86, v87\n" \
"v_mfma_f32_16x16x32_bf16 v[68:71], v[104:107], v[56:59], v[68:71]\n" \
"ds_read_b128 v[104:107], v25 offset:53248\n" \
"s_waitcnt lgkmcnt(7)\n" \
"v_mfma_f32_16x16x32_bf16 v[64:67], v[108:111], v[44:47], v[64:67]\n" \
"s_waitcnt vmcnt(14)\n" \
"ds_read_b64_tr_b16 a[0:1], v23 offset:0\n" \
"ds_read_b64_tr_b16 a[2:3], v23 offset:256\n" \
"v_mfma_f32_16x16x32_bf16 v[68:71], v[108:111], v[60:63], v[68:71]\n" \
"ds_read_b128 v[108:111], v25 offset:54272\n" \
"s_waitcnt lgkmcnt(9)\n" \
"v_mfma_f32_16x16x32_bf16 v[72:75], v[112:115], v[32:35], 0\n" \
"ds_read_b64_tr_b16 a[64:65], v24 offset:0\n" \
"ds_read_b64_tr_b16 a[66:67], v24 offset:256\n" \
"s_waitcnt vmcnt(12)\n" \
"v_mfma_f32_16x16x32_bf16 v[76:79], v[112:115], v[48:51], 0\n" \
"ds_read_b128 v[112:115], v27 offset:20480\n" \
"s_waitcnt lgkmcnt(11)\n" \
"v_mfma_f32_16x16x32_bf16 v[72:75], v[116:119], v[36:39], v[72:75]\n" \
"ds_read_b64_tr_b16 a[4:5], v23 offset:2048\n" \
"ds_read_b64_tr_b16 a[6:7], v23 offset:2304\n" \
"ds_read_b64_tr_b16 a[68:69], v24 offset:2048\n" \
"v_mfma_f32_16x16x32_bf16 v[76:79], v[116:119], v[52:55], v[76:79]\n" \
"ds_read_b128 v[116:119], v27 offset:21504\n" \
"s_waitcnt lgkmcnt(14)\n" \
"v_mfma_f32_16x16x32_bf16 v[72:75], v[120:123], v[40:43], v[72:75]\n" \
"v_cvt_pk_bf16_f32 v164, v64, v65\n" \
"v_cvt_pk_bf16_f32 v165, v66, v67\n" \
"v_cvt_pk_bf16_f32 v228, v68, v69\n" \
"v_cvt_pk_bf16_f32 v229, v70, v71\n" \
"v_mfma_f32_16x16x32_bf16 v[76:79], v[120:123], v[56:59], v[76:79]\n" \
"ds_read_b128 v[120:123], v27 offset:53248\n" \
"s_waitcnt lgkmcnt(14)\n" \
"v_mfma_f32_16x16x32_bf16 v[72:75], v[124:127], v[44:47], v[72:75]\n" \
"ds_read_b64_tr_b16 a[70:71], v24 offset:2304\n" \
"s_waitcnt lgkmcnt(4)\n" \
"s_add_u32 m0, s76, 0\n" \
"v_mfma_f32_16x16x32_bf16 v[76:79], v[124:127], v[60:63], v[76:79]\n" \
"ds_read_b128 v[124:127], v27 offset:54272\n" \
"v_mfma_f32_16x16x32_bf16 v[80:83], v[96:99], v[32:35], 0\n" \
"s_mov_b32 s75, 2097152\n" \
"buffer_load_dwordx4 v20, s[48:51], s75 offen lds\n" \
"s_add_u32 m0, s76, 1024\n" \
"v_mfma_f32_16x16x32_bf16 v[84:87], v[96:99], v[48:51], 0\n" \
"ds_read_b128 v[96:99], v25 offset:22528\n" \
"v_mfma_f32_16x16x32_bf16 v[80:83], v[100:103], v[36:39], v[80:83]\n" \
"s_mov_b32 s75, 2228224\n" \
"buffer_load_dwordx4 v20, s[48:51], s75 offen lds\n" \
"s_waitcnt vmcnt(12)\n" \
"v_mfma_f32_16x16x32_bf16 v[84:87], v[100:103], v[52:55], v[84:87]\n" \
"ds_read_b128 v[100:103], v25 offset:23552\n" \
"v_mfma_f32_16x16x32_bf16 v[80:83], v[104:107], v[40:43], v[80:83]\n" \
"v_cvt_pk_bf16_f32 v166, v72, v73\n" \
"v_cvt_pk_bf16_f32 v167, v74, v75\n" \
"v_cvt_pk_bf16_f32 v230, v76, v77\n" \
"v_cvt_pk_bf16_f32 v231, v78, v79\n" \
"v_mfma_f32_16x16x32_bf16 v[84:87], v[104:107], v[56:59], v[84:87]\n" \
"ds_read_b128 v[104:107], v25 offset:55296\n" \
"v_mfma_f32_16x16x32_bf16 v[80:83], v[108:111], v[44:47], v[80:83]\n" \
"ds_read_b64_tr_b16 a[8:9], v23 offset:4096\n" \
"ds_read_b64_tr_b16 a[10:11], v23 offset:4352\n" \
"ds_read_b64_tr_b16 a[72:73], v24 offset:4096\n" \
"v_mfma_f32_16x16x32_bf16 v[84:87], v[108:111], v[60:63], v[84:87]\n" \
"ds_read_b128 v[108:111], v25 offset:56320\n" \
"v_mfma_f32_16x16x32_bf16 v[64:67], v[112:115], v[32:35], 0\n" \
"ds_read_b64_tr_b16 a[74:75], v24 offset:4352\n" \
"s_waitcnt lgkmcnt(4)\n" \
"s_add_u32 m0, s76, 2048\n" \
"v_mfma_f32_16x16x32_bf16 v[68:71], v[112:115], v[48:51], 0\n" \
"ds_read_b128 v[112:115], v27 offset:22528\n" \
"v_mfma_f32_16x16x32_bf16 v[64:67], v[116:119], v[36:39], v[64:67]\n" \
"s_mov_b32 s75, 2359296\n" \
"buffer_load_dwordx4 v20, s[48:51], s75 offen lds\n" \
"s_add_u32 m0, s76, 3072\n" \
"v_mfma_f32_16x16x32_bf16 v[68:71], v[116:119], v[52:55], v[68:71]\n" \
"ds_read_b128 v[116:119], v27 offset:23552\n" \
"v_mfma_f32_16x16x32_bf16 v[64:67], v[120:123], v[40:43], v[64:67]\n" \
"v_cvt_pk_bf16_f32 v168, v80, v81\n" \
"v_cvt_pk_bf16_f32 v169, v82, v83\n" \
"v_cvt_pk_bf16_f32 v232, v84, v85\n" \
"v_cvt_pk_bf16_f32 v233, v86, v87\n" \
"v_mfma_f32_16x16x32_bf16 v[68:71], v[120:123], v[56:59], v[68:71]\n" \
"ds_read_b128 v[120:123], v27 offset:55296\n" \
"v_mfma_f32_16x16x32_bf16 v[64:67], v[124:127], v[44:47], v[64:67]\n" \
"s_mov_b32 s75, 2490368\n" \
"buffer_load_dwordx4 v20, s[48:51], s75 offen lds\n" \
"s_waitcnt vmcnt(12)\n" \
"v_mfma_f32_16x16x32_bf16 v[68:71], v[124:127], v[60:63], v[68:71]\n" \
"ds_read_b128 v[124:127], v27 offset:56320\n" \
"v_mfma_f32_16x16x32_bf16 v[72:75], v[96:99], v[32:35], 0\n" \
"ds_read_b64_tr_b16 a[12:13], v23 offset:6144\n" \
"ds_read_b64_tr_b16 a[14:15], v23 offset:6400\n" \
"ds_read_b64_tr_b16 a[76:77], v24 offset:6144\n" \
"v_mfma_f32_16x16x32_bf16 v[76:79], v[96:99], v[48:51], 0\n" \
"ds_read_b128 v[96:99], v25 offset:24576\n" \
"v_mfma_f32_16x16x32_bf16 v[72:75], v[100:103], v[36:39], v[72:75]\n" \
"ds_read_b64_tr_b16 a[78:79], v24 offset:6400\n" \
"s_waitcnt lgkmcnt(4)\n" \
"s_add_u32 m0, s76, 4096\n" \
"v_mfma_f32_16x16x32_bf16 v[76:79], v[100:103], v[52:55], v[76:79]\n" \
"ds_read_b128 v[100:103], v25 offset:25600\n" \
"v_mfma_f32_16x16x32_bf16 v[72:75], v[104:107], v[40:43], v[72:75]\n" \
"v_cvt_pk_bf16_f32 v170, v64, v65\n" \
"v_cvt_pk_bf16_f32 v171, v66, v67\n" \
"v_cvt_pk_bf16_f32 v234, v68, v69\n" \
"v_cvt_pk_bf16_f32 v235, v70, v71\n" \
"v_mfma_f32_16x16x32_bf16 v[76:79], v[104:107], v[56:59], v[76:79]\n" \
"ds_read_b128 v[104:107], v25 offset:57344\n" \
"v_mfma_f32_16x16x32_bf16 v[72:75], v[108:111], v[44:47], v[72:75]\n" \
"s_mov_b32 s75, 2621440\n" \
"buffer_load_dwordx4 v20, s[48:51], s75 offen lds\n" \
"s_add_u32 m0, s76, 5120\n" \
"v_mfma_f32_16x16x32_bf16 v[76:79], v[108:111], v[60:63], v[76:79]\n" \
"ds_read_b128 v[108:111], v25 offset:58368\n" \
"v_mfma_f32_16x16x32_bf16 v[80:83], v[112:115], v[32:35], 0\n" \
"s_mov_b32 s75, 2752512\n" \
"buffer_load_dwordx4 v20, s[48:51], s75 offen lds\n" \
"s_waitcnt lgkmcnt(0)\n" \
"v_mfma_f32_16x16x32_bf16 v[84:87], v[112:115], v[48:51], 0\n" \
"ds_read_b128 v[112:115], v27 offset:24576\n" \
"v_mfma_f32_16x16x32_bf16 v[80:83], v[116:119], v[36:39], v[80:83]\n" \
"s_add_u32 m0, s76, 6144\n" \
"s_mov_b32 s75, 2883584\n" \
"buffer_load_dwordx4 v20, s[48:51], s75 offen lds\n" \
"v_mfma_f32_16x16x32_bf16 v[84:87], v[116:119], v[52:55], v[84:87]\n" \
"ds_read_b128 v[116:119], v27 offset:25600\n" \
"v_mfma_f32_16x16x32_bf16 v[80:83], v[120:123], v[40:43], v[80:83]\n" \
"v_cvt_pk_bf16_f32 v172, v72, v73\n" \
"v_cvt_pk_bf16_f32 v173, v74, v75\n" \
"v_cvt_pk_bf16_f32 v236, v76, v77\n" \
"v_cvt_pk_bf16_f32 v237, v78, v79\n" \
"v_mfma_f32_16x16x32_bf16 v[84:87], v[120:123], v[56:59], v[84:87]\n" \
"ds_read_b128 v[120:123], v27 offset:57344\n" \
"v_mfma_f32_16x16x32_bf16 v[80:83], v[124:127], v[44:47], v[80:83]\n" \
"s_add_u32 m0, s76, 7168\n" \
"s_mov_b32 s75, 3014656\n" \
"buffer_load_dwordx4 v20, s[48:51], s75 offen lds\n" \
"v_mfma_f32_16x16x32_bf16 v[84:87], v[124:127], v[60:63], v[84:87]\n" \
"ds_read_b128 v[124:127], v27 offset:58368\n" \
"s_barrier \n" \
"v_mfma_f32_16x16x32_bf16 v[64:67], v[96:99], v[32:35], 0\n" \
"s_add_u32 m0, s76, 16384\n" \
"s_mov_b32 s75, 3145728\n" \
"buffer_load_dwordx4 v20, s[48:51], s75 offen lds\n" \
"s_add_u32 m0, s76, 17408\n" \
"s_mov_b32 s75, 3276800\n" \
"v_mfma_f32_16x16x32_bf16 v[68:71], v[96:99], v[48:51], 0\n" \
"ds_read_b128 v[96:99], v25 offset:26624\n" \
"v_mfma_f32_16x16x32_bf16 v[64:67], v[100:103], v[36:39], v[64:67]\n" \
"buffer_load_dwordx4 v20, s[48:51], s75 offen lds\n" \
"s_add_u32 m0, s76, 18432\n" \
"s_mov_b32 s75, 3407872\n" \
"buffer_load_dwordx4 v20, s[48:51], s75 offen lds\n" \
"s_add_u32 m0, s76, 19456\n" \
"v_mfma_f32_16x16x32_bf16 v[68:71], v[100:103], v[52:55], v[68:71]\n" \
"ds_read_b128 v[100:103], v25 offset:27648\n" \
"v_mfma_f32_16x16x32_bf16 v[64:67], v[104:107], v[40:43], v[64:67]\n" \
"v_cvt_pk_bf16_f32 v174, v80, v81\n" \
"v_cvt_pk_bf16_f32 v175, v82, v83\n" \
"v_cvt_pk_bf16_f32 v238, v84, v85\n" \
"v_cvt_pk_bf16_f32 v239, v86, v87\n" \
"v_mfma_f32_16x16x32_bf16 v[68:71], v[104:107], v[56:59], v[68:71]\n" \
"ds_read_b128 v[104:107], v25 offset:59392\n" \
"v_mfma_f32_16x16x32_bf16 v[64:67], v[108:111], v[44:47], v[64:67]\n" \
"s_mov_b32 s75, 3538944\n" \
"buffer_load_dwordx4 v20, s[48:51], s75 offen lds\n" \
"s_add_u32 m0, s76, 20480\n" \
"s_mov_b32 s75, 3670016\n" \
"buffer_load_dwordx4 v20, s[48:51], s75 offen lds\n" \
"v_mfma_f32_16x16x32_bf16 v[68:71], v[108:111], v[60:63], v[68:71]\n" \
"ds_read_b128 v[108:111], v25 offset:60416\n" \
"s_waitcnt lgkmcnt(7)\n" \
"v_mfma_f32_16x16x32_bf16 v[72:75], v[112:115], v[32:35], 0\n" \
"s_add_u32 m0, s76, 21504\n" \
"s_mov_b32 s75, 3801088\n" \
"buffer_load_dwordx4 v20, s[48:51], s75 offen lds\n" \
"s_add_u32 m0, s76, 22528\n" \
"s_mov_b32 s75, 3932160\n" \
"v_mfma_f32_16x16x32_bf16 v[76:79], v[112:115], v[48:51], 0\n" \
"ds_read_b128 v[112:115], v27 offset:26624\n" \
"s_waitcnt lgkmcnt(7)\n" \
"v_mfma_f32_16x16x32_bf16 v[72:75], v[116:119], v[36:39], v[72:75]\n" \
"buffer_load_dwordx4 v20, s[48:51], s75 offen lds\n" \
"s_add_u32 m0, s76, 23552\n" \
"s_mov_b32 s75, 4063232\n" \
"buffer_load_dwordx4 v20, s[48:51], s75 offen lds\n" \
"s_waitcnt vmcnt(22)\n" \
"v_mfma_f32_16x16x32_bf16 v[76:79], v[116:119], v[52:55], v[76:79]\n" \
"ds_read_b128 v[116:119], v27 offset:27648\n" \
"s_waitcnt lgkmcnt(7)\n" \
"v_mfma_f32_16x16x32_bf16 v[72:75], v[120:123], v[40:43], v[72:75]\n" \
"v_cvt_pk_bf16_f32 v176, v64, v65\n" \
"v_cvt_pk_bf16_f32 v177, v66, v67\n" \
"v_cvt_pk_bf16_f32 v240, v68, v69\n" \
"v_cvt_pk_bf16_f32 v241, v70, v71\n" \
"v_mfma_f32_16x16x32_bf16 v[76:79], v[120:123], v[56:59], v[76:79]\n" \
"ds_read_b128 v[120:123], v27 offset:59392\n" \
"s_waitcnt lgkmcnt(7)\n" \
"v_mfma_f32_16x16x32_bf16 v[72:75], v[124:127], v[44:47], v[72:75]\n" \
"ds_read_b64_tr_b16 a[16:17], v23 offset:8192\n" \
"ds_read_b64_tr_b16 a[18:19], v23 offset:8448\n" \
"ds_read_b64_tr_b16 a[80:81], v24 offset:8192\n" \
"ds_read_b64_tr_b16 a[82:83], v24 offset:8448\n" \
"s_waitcnt vmcnt(20)\n" \
"v_mfma_f32_16x16x32_bf16 v[76:79], v[124:127], v[60:63], v[76:79]\n" \
"ds_read_b128 v[124:127], v27 offset:60416\n" \
"s_waitcnt lgkmcnt(11)\n" \
"v_mfma_f32_16x16x32_bf16 v[80:83], v[96:99], v[32:35], 0\n" \
"ds_read_b64_tr_b16 a[20:21], v23 offset:10240\n" \
"ds_read_b64_tr_b16 a[22:23], v23 offset:10496\n" \
"ds_read_b64_tr_b16 a[84:85], v24 offset:10240\n" \
"ds_read_b64_tr_b16 a[86:87], v24 offset:10496\n" \
"s_waitcnt lgkmcnt(4)\n" \
"v_mfma_f32_16x16x32_bf16 v[84:87], v[96:99], v[48:51], 0\n" \
"ds_read_b128 v[96:99], v25 offset:28672\n" \
"v_mfma_f32_16x16x32_bf16 v[80:83], v[100:103], v[36:39], v[80:83]\n" \
"s_add_u32 m0, s76, 8192\n" \
"s_mov_b32 s75, 0\n" \
"buffer_load_dwordx4 v20, s[52:55], s75 offen lds\n" \
"s_add_u32 m0, s76, 9216\n" \
"s_mov_b32 s75, 131072\n" \
"v_mfma_f32_16x16x32_bf16 v[84:87], v[100:103], v[52:55], v[84:87]\n" \
"ds_read_b128 v[100:103], v25 offset:29696\n" \
"v_mfma_f32_16x16x32_bf16 v[80:83], v[104:107], v[40:43], v[80:83]\n" \
"v_cvt_pk_bf16_f32 v178, v72, v73\n" \
"v_cvt_pk_bf16_f32 v179, v74, v75\n" \
"v_cvt_pk_bf16_f32 v242, v76, v77\n" \
"v_cvt_pk_bf16_f32 v243, v78, v79\n" \
"v_mfma_f32_16x16x32_bf16 v[84:87], v[104:107], v[56:59], v[84:87]\n" \
"ds_read_b128 v[104:107], v25 offset:61440\n" \
"v_mfma_f32_16x16x32_bf16 v[80:83], v[108:111], v[44:47], v[80:83]\n" \
"buffer_load_dwordx4 v20, s[52:55], s75 offen lds\n" \
"s_waitcnt vmcnt(20)\n" \
"ds_read_b64_tr_b16 a[24:25], v23 offset:12288\n" \
"ds_read_b64_tr_b16 a[26:27], v23 offset:12544\n" \
"ds_read_b64_tr_b16 a[88:89], v24 offset:12288\n" \
"v_mfma_f32_16x16x32_bf16 v[84:87], v[108:111], v[60:63], v[84:87]\n" \
"ds_read_b128 v[108:111], v25 offset:62464\n" \
"v_mfma_f32_16x16x32_bf16 v[64:67], v[112:115], v[32:35], 0\n" \
"ds_read_b64_tr_b16 a[90:91], v24 offset:12544\n" \
"s_waitcnt lgkmcnt(4)\n" \
"s_add_u32 m0, s76, 10240\n" \
"s_mov_b32 s75, 262144\n" \
"buffer_load_dwordx4 v20, s[52:55], s75 offen lds\n" \
"v_mfma_f32_16x16x32_bf16 v[68:71], v[112:115], v[48:51], 0\n" \
"ds_read_b128 v[112:115], v27 offset:28672\n" \
"v_mfma_f32_16x16x32_bf16 v[64:67], v[116:119], v[36:39], v[64:67]\n" \
"s_add_u32 m0, s76, 11264\n" \
"s_mov_b32 s75, 393216\n" \
"buffer_load_dwordx4 v20, s[52:55], s75 offen lds\n" \
"s_waitcnt vmcnt(20)\n" \
"ds_read_b64_tr_b16 a[28:29], v23 offset:14336\n" \
"v_mfma_f32_16x16x32_bf16 v[68:71], v[116:119], v[52:55], v[68:71]\n" \
"ds_read_b128 v[116:119], v27 offset:29696\n" \
"v_mfma_f32_16x16x32_bf16 v[64:67], v[120:123], v[40:43], v[64:67]\n" \
"v_cvt_pk_bf16_f32 v180, v80, v81\n" \
"v_cvt_pk_bf16_f32 v181, v82, v83\n" \
"v_cvt_pk_bf16_f32 v244, v84, v85\n" \
"v_cvt_pk_bf16_f32 v245, v86, v87\n" \
"v_mfma_f32_16x16x32_bf16 v[68:71], v[120:123], v[56:59], v[68:71]\n" \
"ds_read_b128 v[120:123], v27 offset:61440\n" \
"v_mfma_f32_16x16x32_bf16 v[64:67], v[124:127], v[44:47], v[64:67]\n" \
"ds_read_b64_tr_b16 a[30:31], v23 offset:14592\n" \
"ds_read_b64_tr_b16 a[92:93], v24 offset:14336\n" \
"ds_read_b64_tr_b16 a[94:95], v24 offset:14592\n" \
"s_waitcnt lgkmcnt(4)\n" \
"s_add_u32 m0, s76, 12288\n" \
"v_mfma_f32_16x16x32_bf16 v[68:71], v[124:127], v[60:63], v[68:71]\n" \
"ds_read_b128 v[124:127], v27 offset:62464\n" \
"v_mfma_f32_16x16x32_bf16 v[72:75], v[96:99], v[32:35], 0\n" \
"s_mov_b32 s75, 524288\n" \
"buffer_load_dwordx4 v20, s[52:55], s75 offen lds\n" \
"s_add_u32 m0, s76, 13312\n" \
"s_mov_b32 s75, 655360\n" \
"buffer_load_dwordx4 v20, s[52:55], s75 offen lds\n" \
"v_mfma_f32_16x16x32_bf16 v[76:79], v[96:99], v[48:51], 0\n" \
"ds_read_b128 v[96:99], v25 offset:30720\n" \
"v_mfma_f32_16x16x32_bf16 v[72:75], v[100:103], v[36:39], v[72:75]\n" \
"s_waitcnt vmcnt(20)\n" \
"ds_read_b64_tr_b16 a[32:33], v23 offset:0\n" \
"ds_read_b64_tr_b16 a[34:35], v23 offset:256\n" \
"ds_read_b64_tr_b16 a[96:97], v24 offset:0\n" \
"ds_read_b64_tr_b16 a[98:99], v24 offset:256\n" \
"v_mfma_f32_16x16x32_bf16 v[76:79], v[100:103], v[52:55], v[76:79]\n" \
"ds_read_b128 v[100:103], v25 offset:31744\n" \
"v_mfma_f32_16x16x32_bf16 v[72:75], v[104:107], v[40:43], v[72:75]\n" \
"v_cvt_pk_bf16_f32 v182, v64, v65\n" \
"v_cvt_pk_bf16_f32 v183, v66, v67\n" \
"v_cvt_pk_bf16_f32 v246, v68, v69\n" \
"v_cvt_pk_bf16_f32 v247, v70, v71\n" \
"v_mfma_f32_16x16x32_bf16 v[76:79], v[104:107], v[56:59], v[76:79]\n" \
"ds_read_b128 v[104:107], v25 offset:63488\n" \
"v_mfma_f32_16x16x32_bf16 v[72:75], v[108:111], v[44:47], v[72:75]\n" \
"s_waitcnt lgkmcnt(4)\n" \
"s_add_u32 m0, s76, 14336\n" \
"s_mov_b32 s75, 786432\n" \
"buffer_load_dwordx4 v20, s[52:55], s75 offen lds\n" \
"s_add_u32 m0, s76, 15360\n" \
"v_mfma_f32_16x16x32_bf16 v[76:79], v[108:111], v[60:63], v[76:79]\n" \
"ds_read_b128 v[108:111], v25 offset:64512\n" \
"v_mfma_f32_16x16x32_bf16 v[80:83], v[112:115], v[32:35], 0\n" \
"s_mov_b32 s75, 917504\n" \
"buffer_load_dwordx4 v20, s[52:55], s75 offen lds\n" \
"s_waitcnt vmcnt(20)\n" \
"ds_read_b64_tr_b16 a[36:37], v23 offset:2048\n" \
"ds_read_b64_tr_b16 a[38:39], v23 offset:2304\n" \
"v_mfma_f32_16x16x32_bf16 v[84:87], v[112:115], v[48:51], 0\n" \
"ds_read_b128 v[112:115], v27 offset:30720\n" \
"v_mfma_f32_16x16x32_bf16 v[80:83], v[116:119], v[36:39], v[80:83]\n" \
"ds_read_b64_tr_b16 a[100:101], v24 offset:2048\n" \
"ds_read_b64_tr_b16 a[102:103], v24 offset:2304\n" \
"s_waitcnt lgkmcnt(4)\n" \
"s_add_u32 m0, s76, 0\n" \
"s_mov_b32 s75, 1048576\n" \
"v_mfma_f32_16x16x32_bf16 v[84:87], v[116:119], v[52:55], v[84:87]\n" \
"ds_read_b128 v[116:119], v27 offset:31744\n" \
"v_mfma_f32_16x16x32_bf16 v[80:83], v[120:123], v[40:43], v[80:83]\n" \
"v_cvt_pk_bf16_f32 v184, v72, v73\n" \
"v_cvt_pk_bf16_f32 v185, v74, v75\n" \
"v_cvt_pk_bf16_f32 v248, v76, v77\n" \
"v_cvt_pk_bf16_f32 v249, v78, v79\n" \
"v_mfma_f32_16x16x32_bf16 v[84:87], v[120:123], v[56:59], v[84:87]\n" \
"ds_read_b128 v[120:123], v27 offset:63488\n" \
"v_mfma_f32_16x16x32_bf16 v[80:83], v[124:127], v[44:47], v[80:83]\n" \
"buffer_load_dwordx4 v20, s[52:55], s75 offen lds\n" \
"s_add_u32 m0, s76, 1024\n" \
"s_mov_b32 s75, 1179648\n" \
"buffer_load_dwordx4 v20, s[52:55], s75 offen lds\n" \
"s_waitcnt vmcnt(20)\n" \
"v_mfma_f32_16x16x32_bf16 v[84:87], v[124:127], v[60:63], v[84:87]\n" \
"ds_read_b128 v[124:127], v27 offset:64512\n" \
"v_mfma_f32_16x16x32_bf16 v[64:67], v[96:99], v[32:35], 0\n" \
"ds_read_b64_tr_b16 a[40:41], v23 offset:4096\n" \
"ds_read_b64_tr_b16 a[42:43], v23 offset:4352\n" \
"ds_read_b64_tr_b16 a[104:105], v24 offset:4096\n" \
"ds_read_b64_tr_b16 a[106:107], v24 offset:4352\n" \
"s_waitcnt lgkmcnt(4)\n" \
"v_mfma_f32_16x16x32_bf16 v[68:71], v[96:99], v[48:51], 0\n" \
"v_mfma_f32_16x16x32_bf16 v[64:67], v[100:103], v[36:39], v[64:67]\n" \
"s_add_u32 m0, s76, 2048\n" \
"s_mov_b32 s75, 1310720\n" \
"buffer_load_dwordx4 v20, s[52:55], s75 offen lds\n" \
"s_add_u32 m0, s76, 3072\n" \
"s_mov_b32 s75, 1441792\n" \
"v_mfma_f32_16x16x32_bf16 v[68:71], v[100:103], v[52:55], v[68:71]\n" \
"v_mfma_f32_16x16x32_bf16 v[64:67], v[104:107], v[40:43], v[64:67]\n" \
"v_cvt_pk_bf16_f32 v186, v80, v81\n" \
"v_cvt_pk_bf16_f32 v187, v82, v83\n" \
"v_cvt_pk_bf16_f32 v250, v84, v85\n" \
"v_cvt_pk_bf16_f32 v251, v86, v87\n" \
"v_mfma_f32_16x16x32_bf16 v[68:71], v[104:107], v[56:59], v[68:71]\n" \
"v_mfma_f32_16x16x32_bf16 v[64:67], v[108:111], v[44:47], v[64:67]\n" \
"buffer_load_dwordx4 v20, s[52:55], s75 offen lds\n" \
"s_waitcnt vmcnt(20)\n" \
"ds_read_b64_tr_b16 a[44:45], v23 offset:6144\n" \
"ds_read_b64_tr_b16 a[46:47], v23 offset:6400\n" \
"ds_read_b64_tr_b16 a[108:109], v24 offset:6144\n" \
"v_mfma_f32_16x16x32_bf16 v[68:71], v[108:111], v[60:63], v[68:71]\n" \
"v_mfma_f32_16x16x32_bf16 v[72:75], v[112:115], v[32:35], 0\n" \
"ds_read_b64_tr_b16 a[110:111], v24 offset:6400\n" \
"s_waitcnt lgkmcnt(4)\n" \
"s_add_u32 m0, s76, 4096\n" \
"s_mov_b32 s75, 1572864\n" \
"buffer_load_dwordx4 v20, s[52:55], s75 offen lds\n" \
"v_mfma_f32_16x16x32_bf16 v[76:79], v[112:115], v[48:51], 0\n" \
"v_mfma_f32_16x16x32_bf16 v[72:75], v[116:119], v[36:39], v[72:75]\n" \
"s_add_u32 m0, s76, 5120\n" \
"s_mov_b32 s75, 1703936\n" \
"buffer_load_dwordx4 v20, s[52:55], s75 offen lds\n" \
"s_waitcnt lgkmcnt(0)\n" \
"s_add_u32 m0, s76, 6144\n" \
"v_mfma_f32_16x16x32_bf16 v[76:79], v[116:119], v[52:55], v[76:79]\n" \
"v_mfma_f32_16x16x32_bf16 v[72:75], v[120:123], v[40:43], v[72:75]\n" \
"v_cvt_pk_bf16_f32 v188, v64, v65\n" \
"v_cvt_pk_bf16_f32 v189, v66, v67\n" \
"v_cvt_pk_bf16_f32 v252, v68, v69\n" \
"v_cvt_pk_bf16_f32 v253, v70, v71\n" \
"v_mfma_f32_16x16x32_bf16 v[76:79], v[120:123], v[56:59], v[76:79]\n" \
"v_mfma_f32_16x16x32_bf16 v[72:75], v[124:127], v[44:47], v[72:75]\n" \
"s_mov_b32 s75, 1835008\n" \
"buffer_load_dwordx4 v20, s[52:55], s75 offen lds\n" \
"s_add_u32 m0, s76, 7168\n" \
"s_mov_b32 s75, 1966080\n" \
"buffer_load_dwordx4 v20, s[52:55], s75 offen lds\n" \
"v_mfma_f32_16x16x32_bf16 v[76:79], v[124:127], v[60:63], v[76:79]\n" \
"s_nop 1\n" \
"v_cvt_pk_bf16_f32 v190, v72, v73\n" \
"v_cvt_pk_bf16_f32 v191, v74, v75\n" \
"s_nop 3\n" \
"v_cvt_pk_bf16_f32 v254, v76, v77\n" \
"v_cvt_pk_bf16_f32 v255, v78, v79\n" \
"s_barrier \n" \
"s_memrealtime s[14:15]\n" \
"s_waitcnt lgkmcnt(0)\n" \
"s_lshl_b32 s37, s14, 3\n" \
"s_add_u32 s37, s37, 1900\n" \
"s_pack_ll_b32_b16 s75, s37, s37\n" \
"s_min_u32 s75, s75, -2\n" \
"v_mov_b32 v96, s75\n" \
"s_mov_b32 s35, 512000\n" \
"s_sub_u32 s25, s37, 8000\n" \
"s_cmp_lg_u32 s64, 0\n" \
"s_cbranch_scc1 R_ns%=\n" \
"buffer_store_dword v96, v19, s[40:43], s35 offen sc1\n" \
"v_or_b32 v19, 0x40000000, v19\n" \
"R_ns%=:\n" \
"s_add_u32 m0, s76, 24576\n" \
"s_mov_b32 s75, 2097152\n" \
"buffer_load_dwordx4 v20, s[52:55], s75 offen lds\n" \
"s_add_u32 m0, s76, 25600\n" \
"s_mov_b32 s75, 2228224\n" \
"buffer_load_dwordx4 v20, s[52:55], s75 offen lds\n" \
"s_add_u32 m0, s76, 26624\n" \
"s_mov_b32 s75, 2359296\n" \
"buffer_load_dwordx4 v20, s[52:55], s75 offen lds\n" \
"s_add_u32 m0, s76, 27648\n" \
"s_mov_b32 s75, 2490368\n" \
"buffer_load_dwordx4 v20, s[52:55], s75 offen lds\n" \
"s_add_u32 m0, s76, 28672\n" \
"s_mov_b32 s75, 2621440\n" \
"buffer_load_dwordx4 v20, s[52:55], s75 offen lds\n" \
"s_add_u32 m0, s76, 29696\n" \
"s_mov_b32 s75, 2752512\n" \
"buffer_load_dwordx4 v20, s[52:55], s75 offen lds\n" \
"s_add_u32 m0, s76, 30720\n" \
"s_mov_b32 s75, 2883584\n" \
"buffer_load_dwordx4 v20, s[52:55], s75 offen lds\n" \
"s_add_u32 m0, s76, 31744\n" \
"s_mov_b32 s75, 3014656\n" \
"buffer_load_dwordx4 v20, s[52:55], s75 offen lds\n" \
"s_waitcnt vmcnt(30)\n" \
"ds_read_b64_tr_b16 a[48:49], v23 offset:16384\n" \
"ds_read_b64_tr_b16 a[50:51], v23 offset:16640\n" \
"ds_read_b64_tr_b16 a[112:113], v24 offset:16384\n" \
"ds_read_b64_tr_b16 a[114:115], v24 offset:16640\n" \
"s_waitcnt vmcnt(28)\n" \
"ds_read_b64_tr_b16 a[52:53], v23 offset:18432\n" \
"ds_read_b64_tr_b16 a[54:55], v23 offset:18688\n" \
"ds_read_b64_tr_b16 a[116:117], v24 offset:18432\n" \
"ds_read_b64_tr_b16 a[118:119], v24 offset:18688\n" \
"s_waitcnt lgkmcnt(4)\n" \
"s_add_u32 m0, s76, 16384\n" \
"s_mov_b32 s75, 3145728\n" \
"buffer_load_dwordx4 v20, s[52:55], s75 offen lds\n" \
"s_add_u32 m0, s76, 17408\n" \
"s_mov_b32 s75, 3276800\n" \
"buffer_load_dwordx4 v20, s[52:55], s75 offen lds\n" \
"s_waitcnt vmcnt(28)\n" \
"ds_read_b64_tr_b16 a[56:57], v23 offset:20480\n" \
"ds_read_b64_tr_b16 a[58:59], v23 offset:20736\n" \
"ds_read_b64_tr_b16 a[120:121], v24 offset:20480\n" \
"ds_read_b64_tr_b16 a[122:123], v24 offset:20736\n" \
"s_waitcnt lgkmcnt(4)\n" \
"s_add_u32 m0, s76, 18432\n" \
"s_mov_b32 s75, 3407872\n" \
"buffer_load_dwordx4 v20, s[52:55], s75 offen lds\n" \
"s_add_u32 m0, s76, 19456\n" \
"s_mov_b32 s75, 3538944\n" \
"buffer_load_dwordx4 v20, s[52:55], s75 offen lds\n" \
"s_waitcnt vmcnt(28)\n" \
"ds_read_b64_tr_b16 a[60:61], v23 offset:22528\n" \
"ds_read_b64_tr_b16 a[62:63], v23 offset:22784\n" \
"ds_read_b64_tr_b16 a[124:125], v24 offset:22528\n" \
"ds_read_b64_tr_b16 a[126:127], v24 offset:22784\n" \
"s_waitcnt lgkmcnt(4)\n" \
"s_add_u32 m0, s76, 20480\n" \
"s_mov_b32 s75, 3670016\n" \
"buffer_load_dwordx4 v20, s[52:55], s75 offen lds\n" \
"s_add_u32 m0, s76, 21504\n" \
"s_mov_b32 s75, 3801088\n" \
"buffer_load_dwordx4 v20, s[52:55], s75 offen lds\n" \
"s_waitcnt vmcnt(28)\n" \
"ds_read_b64_tr_b16 a[128:129], v23 offset:8192\n" \
"ds_read_b64_tr_b16 a[130:131], v23 offset:8448\n" \
"ds_read_b64_tr_b16 a[192:193], v24 offset:8192\n" \
"ds_read_b64_tr_b16 a[194:195], v24 offset:8448\n" \
"s_waitcnt lgkmcnt(4)\n" \
"s_add_u32 m0, s76, 22528\n" \
"s_mov_b32 s75, 3932160\n" \
"buffer_load_dwordx4 v20, s[52:55], s75 offen lds\n" \
"s_add_u32 m0, s76, 23552\n" \
"s_mov_b32 s75, 4063232\n" \
"buffer_load_dwordx4 v20, s[52:55], s75 offen lds\n" \
"s_mov_b32 s31, 0\n" \
"s_mov_b32 s77, 0\n" \
"s_mov_b32 s74, 0\n" \
"buffer_load_dword v16, v14, s[44:47], 0 offen sc1\n" \
"buffer_load_dword v17, v14, s[44:47], 0 offen offset:2048 sc1\n" \
"buffer_load_dwordx4 v[100:103], v1, s[40:43], 0 offen offset:0 sc1\n" \
"buffer_load_dwordx4 v[104:107], v1, s[40:43], 0 offen offset:1024 sc1\n" \
"v_mov_b32 v27, v22\n" \
"s_waitcnt vmcnt(32)\n" \
"ds_read_b64_tr_b16 a[132:133], v23 offset:10240\n" \
"ds_read_b64_tr_b16 a[134:135], v23 offset:10496\n" \
"ds_read_b64_tr_b16 a[196:197], v24 offset:10240\n" \
"ds_read_b64_tr_b16 a[198:199], v24 offset:10496\n" \
"s_waitcnt vmcnt(30)\n" \
"ds_read_b64_tr_b16 a[136:137], v23 offset:12288\n" \
"ds_read_b64_tr_b16 a[138:139], v23 offset:12544\n" \
"ds_read_b64_tr_b16 a[200:201], v24 offset:12288\n" \
"ds_read_b64_tr_b16 a[202:203], v24 offset:12544\n" \
"s_waitcnt vmcnt(28)\n" \
"ds_read_b64_tr_b16 a[140:141], v23 offset:14336\n" \
"ds_read_b64_tr_b16 a[142:143], v23 offset:14592\n" \
"ds_read_b64_tr_b16 a[204:205], v24 offset:14336\n" \
"ds_read_b64_tr_b16 a[206:207], v24 offset:14592\n" \
"s_waitcnt vmcnt(26)\n" \
"ds_read_b64_tr_b16 a[144:145], v23 offset:0\n" \
"ds_read_b64_tr_b16 a[146:147], v23 offset:256\n" \
"ds_read_b64_tr_b16 a[208:209], v24 offset:0\n" \
"ds_read_b64_tr_b16 a[210:211], v24 offset:256\n" \
"s_waitcnt vmcnt(24)\n" \
"ds_read_b64_tr_b16 a[148:149], v23 offset:2048\n" \
"ds_read_b64_tr_b16 a[150:151], v23 offset:2304\n" \
"ds_read_b64_tr_b16 a[212:213], v24 offset:2048\n" \
"ds_read_b64_tr_b16 a[214:215], v24 offset:2304\n" \
"s_waitcnt vmcnt(22)\n" \
"ds_read_b64_tr_b16 a[152:153], v23 offset:4096\n" \
"ds_read_b64_tr_b16 a[154:155], v23 offset:4352\n" \
"ds_read_b64_tr_b16 a[216:217], v24 offset:4096\n" \
"ds_read_b64_tr_b16 a[218:219], v24 offset:4352\n" \
"s_waitcnt vmcnt(20)\n" \
"ds_read_b64_tr_b16 a[156:157], v23 offset:6144\n" \
"ds_read_b64_tr_b16 a[158:159], v23 offset:6400\n" \
"ds_read_b64_tr_b16 a[220:221], v24 offset:6144\n" \
"ds_read_b64_tr_b16 a[222:223], v24 offset:6400\n" \
"s_waitcnt vmcnt(18)\n" \
"ds_read_b64_tr_b16 a[160:161], v23 offset:24576\n" \
"ds_read_b64_tr_b16 a[162:163], v23 offset:24832\n" \
"ds_read_b64_tr_b16 a[224:225], v24 offset:24576\n" \
"ds_read_b64_tr_b16 a[226:227], v24 offset:24832\n" \
"s_waitcnt vmcnt(16)\n" \
"ds_read_b64_tr_b16 a[164:165], v23 offset:26624\n" \
"ds_read_b64_tr_b16 a[166:167], v23 offset:26880\n" \
"ds_read_b64_tr_b16 a[228:229], v24 offset:26624\n" \
"ds_read_b64_tr_b16 a[230:231], v24 offset:26880\n" \
"s_waitcnt vmcnt(14)\n" \
"ds_read_b64_tr_b16 a[168:169], v23 offset:28672\n" \
"ds_read_b64_tr_b16 a[170:171], v23 offset:28928\n" \
"ds_read_b64_tr_b16 a[232:233], v24 offset:28672\n" \
"ds_read_b64_tr_b16 a[234:235], v24 offset:28928\n" \
"s_waitcnt vmcnt(12)\n" \
"ds_read_b64_tr_b16 a[172:173], v23 offset:30720\n" \
"ds_read_b64_tr_b16 a[174:175], v23 offset:30976\n" \
"ds_read_b64_tr_b16 a[236:237], v24 offset:30720\n" \
"ds_read_b64_tr_b16 a[238:239], v24 offset:30976\n" \
"s_waitcnt vmcnt(10)\n" \
"ds_read_b64_tr_b16 a[176:177], v23 offset:16384\n" \
"ds_read_b64_tr_b16 a[178:179], v23 offset:16640\n" \
"ds_read_b64_tr_b16 a[240:241], v24 offset:16384\n" \
"ds_read_b64_tr_b16 a[242:243], v24 offset:16640\n" \
"s_waitcnt vmcnt(8)\n" \
"ds_read_b64_tr_b16 a[180:181], v23 offset:18432\n" \
"ds_read_b64_tr_b16 a[182:183], v23 offset:18688\n" \
"ds_read_b64_tr_b16 a[244:245], v24 offset:18432\n" \
"ds_read_b64_tr_b16 a[246:247], v24 offset:18688\n" \
"s_waitcnt vmcnt(6)\n" \
"ds_read_b64_tr_b16 a[184:185], v23 offset:20480\n" \
"ds_read_b64_tr_b16 a[186:187], v23 offset:20736\n" \
"ds_read_b64_tr_b16 a[248:249], v24 offset:20480\n" \
"ds_read_b64_tr_b16 a[250:251], v24 offset:20736\n" \
"s_waitcnt vmcnt(4)\n" \
"ds_read_b64_tr_b16 a[188:189], v23 offset:22528\n" \
"ds_read_b64_tr_b16 a[190:191], v23 offset:22784\n" \
"ds_read_b64_tr_b16 a[252:253], v24 offset:22528\n" \
"ds_read_b64_tr_b16 a[254:255], v24 offset:22784\n" \
"s_waitcnt vmcnt(0)\n" \
"s_cmp_eq_u32 s64, 0\n" \
"s_cbranch_scc1 R_st0%=\n" \
"s_mov_b32 s83, 0\n" \
"R_f0%=:\n" \
"v_readfirstlane_b32 s75, v16\n" \
"s_nop 0\n" \
"s_cmp_eq_u32 s75, 0\n" \
"s_cbranch_scc1 R_f0w%=\n" \
"R_f0x%=:\n" \
"s_add_u32 s31, s31, 2048\n" \
"s_min_u32 s31, s31, 126976\n" \
"v_mov_b32 v16, v17\n" \
"s_nop 0\n" \
"s_mov_b32 s83, 0\n" \
"R_f1%=:\n" \
"v_readfirstlane_b32 s75, v16\n" \
"s_nop 0\n" \
"s_cmp_eq_u32 s75, 0\n" \
"s_cbranch_scc1 R_f1w%=\n" \
"R_f1x%=:\n" \
"s_add_u32 s31, s31, 2048\n" \
"s_min_u32 s31, s31, 126976\n" \
"s_cmp_eq_u32 s64, 1\n" \
"s_cbranch_scc1 R_kv1%=\n" \
"buffer_load_dwordx4 v[32:35], v5, s[44:47], s74 offen offset:0 sc1\n" \
"buffer_load_dwordx4 v[36:39], v5, s[44:47], s74 offen offset:1024 sc1\n" \
"buffer_load_dwordx4 v[40:43], v5, s[44:47], s74 offen offset:2048 sc1\n" \
"buffer_load_dwordx4 v[44:47], v5, s[44:47], s74 offen offset:3072 sc1\n" \
"s_add_u32 s74, s74, 524288\n" \
"s_min_u32 s74, s74, 32505856\n" \
"buffer_load_dwordx4 v[68:71], v5, s[44:47], s74 offen offset:0 sc1\n" \
"buffer_load_dwordx4 v[72:75], v5, s[44:47], s74 offen offset:1024 sc1\n" \
"buffer_load_dwordx4 v[76:79], v5, s[44:47], s74 offen offset:2048 sc1\n" \
"buffer_load_dwordx4 v[80:83], v5, s[44:47], s74 offen offset:3072 sc1\n" \
"s_add_u32 s74, s74, 524288\n" \
"s_min_u32 s74, s74, 32505856\n" \
"s_branch R_kvi%=\n" \
"R_f0w%=:\n" \
"s_add_u32 s83, s83, 1\n" \
"s_cmp_gt_u32 s83, 65536\n" \
"s_cselect_b32 s77, 1, s77\n" \
"s_cmp_lg_u32 s77, 0\n" \
"s_cbranch_scc1 R_f0x%=\n" \
"s_sleep 5\n" \
"buffer_load_dword v16, v14, s[44:47], s31 offen sc1\n" \
"s_waitcnt vmcnt(0)\n" \
"s_branch R_f0%=\n" \
"R_f1w%=:\n" \
"s_add_u32 s83, s83, 1\n" \
"s_cmp_gt_u32 s83, 65536\n" \
"s_cselect_b32 s77, 1, s77\n" \
"s_cmp_lg_u32 s77, 0\n" \
"s_cbranch_scc1 R_f1x%=\n" \
"s_sleep 5\n" \
"buffer_load_dword v16, v14, s[44:47], s31 offen sc1\n" \
"s_waitcnt vmcnt(0)\n" \
"s_branch R_f1%=\n" \
"R_st0%=:\n" \
"s_waitcnt lgkmcnt(0)\n" \
"s_mov_b32 s44, s8\n" \
"s_mov_b32 s45, s9\n" \
"s_memtime s[84:85]\n" \
"s_memrealtime s[48:49]\n" \
"v_mov_b32 v7, v7\n" \
"s_nop 0\n" \
"s_memrealtime s[50:51]\n" \
"v_mov_b32 v7, v7\n" \
"s_nop 0\n" \
"s_memrealtime s[52:53]\n" \
"v_mov_b32 v7, v7\n" \
"s_nop 0\n" \
"s_memrealtime s[54:55]\n" \
"v_mov_b32 v7, v7\n" \
"s_memrealtime s[56:57]\n" \
"v_mov_b32 v7, v7\n" \
"s_nop 0\n" \
"s_memrealtime s[58:59]\n" \
"v_mov_b32 v7, v7\n" \
"s_nop 0\n" \
"s_memrealtime s[60:61]\n" \
"v_mov_b32 v7, v7\n" \
"s_nop 0\n" \
"s_memrealtime s[62:63]\n" \
"v_mov_b32 v7, v7\n" \
"s_nop 0\n" \
"v_mov_b32 v7, v7\n" \
"s_nop 0\n" \
"v_mov_b32 v7, v7\n" \
"s_nop 0\n" \
"v_mov_b32 v7, v7\n" \
"s_nop 0\n" \
"s_waitcnt lgkmcnt(0)\n" \
"s_add_u32 s8, s48, s50\n" \
"s_add_u32 s8, s8, s52\n" \
"s_add_u32 s8, s8, s54\n" \
"s_add_u32 s8, s8, s56\n" \
"s_add_u32 s8, s8, s58\n" \
"s_add_u32 s8, s8, s60\n" \
"s_add_u32 s8, s8, s62\n" \
"s_mov_b32 s9, s84\n" \
"s_sub_u32 s37, s8, s44\n" \
"s_sub_u32 s38, s9, s45\n" \
"s_mov_b32 s44, s8\n" \
"s_mov_b32 s45, s9\n" \
"v_cvt_f32_u32 v44, s37\n" \
"v_cvt_f32_u32 v45, s38\n" \
"v_rcp_f32 v44, v44\n" \
"s_nop 0\n" \
"v_mul_f32 v44, v44, v45\n" \
"v_med3_f32 v44, v44, 2.0, 4.0\n" \
"v_rcp_f32 v45, v44\n" \
"v_mul_f32 v44, 0x43800000, v44\n" \
"v_mul_f32 v45, 0x46800000, v45\n" \
"v_cvt_u32_f32 v44, v44\n" \
"v_cvt_u32_f32 v45, v45\n" \
"v_readfirstlane_b32 s10, v44\n" \
"v_readfirstlane_b32 s11, v45\n" \
"s_getpc_b64 s[0:1]\n" \
"R0_jpc%=:\n" \
"s_add_u32 s0, s0, R0_w1x%=-R0_jpc%=\n" \
"s_addc_u32 s1, s1, 0\n" \
"s_mov_b32 s31, 65536\n" \
"v_mov_b64 v[16:17], -1\n" \
"s_mov_b32 s47, 3200\n" \
"s_mov_b32 s17, 0\n" \
"s_mov_b32 s6, 0\n" \
"s_branch R_kvi%=\n" \
"R_kv1%=:\n" \
"s_waitcnt lgkmcnt(0)\n" \
"buffer_load_dwordx4 v[32:35], v5, s[44:47], s74 offen offset:0 sc1\n" \
"buffer_load_dwordx4 v[36:39], v5, s[44:47], s74 offen offset:1024 sc1\n" \
"buffer_load_dwordx4 v[40:43], v5, s[44:47], s74 offen offset:2048 sc1\n" \
"buffer_load_dwordx4 v[44:47], v5, s[44:47], s74 offen offset:3072 sc1\n" \
"s_add_u32 s75, s74, 4096\n" \
"buffer_load_dwordx4 v[48:51], v5, s[44:47], s75 offen offset:0 sc1\n" \
"buffer_load_dwordx4 v[52:55], v5, s[44:47], s75 offen offset:1024 sc1\n" \
"buffer_load_dwordx4 v[56:59], v5, s[44:47], s75 offen offset:2048 sc1\n" \
"buffer_load_dwordx4 v[60:63], v5, s[44:47], s75 offen offset:3072 sc1\n" \
"s_add_u32 s74, s74, 524288\n" \
"s_min_u32 s74, s74, 32505856\n" \
"buffer_load_dwordx4 v[68:71], v5, s[44:47], s74 offen offset:0 sc1\n" \
"buffer_load_dwordx4 v[72:75], v5, s[44:47], s74 offen offset:1024 sc1\n" \
"buffer_load_dwordx4 v[76:79], v5, s[44:47], s74 offen offset:2048 sc1\n" \
"buffer_load_dwordx4 v[80:83], v5, s[44:47], s74 offen offset:3072 sc1\n" \
"s_add_u32 s75, s74, 4096\n" \
"buffer_load_dwordx4 v[84:87], v5, s[44:47], s75 offen offset:0 sc1\n" \
"buffer_load_dwordx4 v[88:91], v5, s[44:47], s75 offen offset:1024 sc1\n" \
"buffer_load_dwordx4 v[92:95], v5, s[44:47], s75 offen offset:2048 sc1\n" \
"buffer_load_dwordx4 v[20:23], v5, s[44:47], s75 offen offset:3072 sc1\n" \
"s_add_u32 s74, s74, 524288\n" \
"s_min_u32 s74, s74, 32505856\n" \
"R_kvi%=:\n" \
"s_waitcnt lgkmcnt(0)\n" \
"s_barrier \n" \
"v_add_u32 v26, 86016, v26\n" \
"ds_write_b128 v26, v[28:31]\n" \
"s_cmp_eq_u32 s64, 0\n" \
"s_cbranch_scc1 R_kvw%=\n" \
"s_waitcnt vmcnt(0)\n" \
"s_cmp_eq_u32 s64, 1\n" \
"s_cbranch_scc1 R_kw1%=\n" \
"ds_write_b128 v4, v[32:35] offset:0\n" \
"ds_write_b128 v4, v[36:39] offset:1024\n" \
"ds_write_b128 v4, v[40:43] offset:2048\n" \
"ds_write_b128 v4, v[44:47] offset:3072\n" \
"v_xor_b32 v4, 16384, v4\n" \
"v_mov_b64 v[116:117], v[68:69]\n" \
"v_mov_b64 v[118:119], v[70:71]\n" \
"v_mov_b64 v[120:121], v[72:73]\n" \
"v_mov_b64 v[122:123], v[74:75]\n" \
"v_mov_b64 v[124:125], v[76:77]\n" \
"v_mov_b64 v[126:127], v[78:79]\n" \
"v_mov_b64 v[16:17], v[80:81]\n" \
"v_mov_b64 v[18:19], v[82:83]\n" \
"s_branch R_kvw%=\n" \
"R_kw1%=:\n" \
"ds_write_b128 v4, v[32:35] offset:0\n" \
"ds_write_b128 v4, v[36:39] offset:1024\n" \
"ds_write_b128 v4, v[40:43] offset:2048\n" \
"ds_write_b128 v4, v[44:47] offset:3072\n" \
"ds_write_b128 v4, v[48:51] offset:4096\n" \
"ds_write_b128 v4, v[52:55] offset:5120\n" \
"ds_write_b128 v4, v[56:59] offset:6144\n" \
"ds_write_b128 v4, v[60:63] offset:7168\n" \
"v_xor_b32 v4, 16384, v4\n" \
"v_mov_b64 v[44:45], v[68:69]\n" \
"v_mov_b64 v[46:47], v[70:71]\n" \
"v_mov_b64 v[48:49], v[72:73]\n" \
"v_mov_b64 v[50:51], v[74:75]\n" \
"v_mov_b64 v[52:53], v[76:77]\n" \
"v_mov_b64 v[54:55], v[78:79]\n" \
"v_mov_b64 v[56:57], v[80:81]\n" \
"v_mov_b64 v[58:59], v[82:83]\n" \
"v_mov_b64 v[60:61], v[84:85]\n" \
"v_mov_b64 v[62:63], v[86:87]\n" \
"v_mov_b64 v[64:65], v[88:89]\n" \
"v_mov_b64 v[66:67], v[90:91]\n" \
"v_mov_b64 v[68:69], v[92:93]\n" \
"v_mov_b64 v[70:71], v[94:95]\n" \
"v_mov_b64 v[72:73], v[20:21]\n" \
"v_mov_b64 v[74:75], v[22:23]\n" \
"R_kvw%=:\n" \
"s_mov_b32 s70, 0\n" \
"s_mov_b32 s73, 0\n" \
"s_mov_b32 s78, 0\n" \
"s_mov_b32 s79, 0\n" \
"s_mov_b32 s27, 0\n" \
"s_mov_b32 s36, 0\n" \
"s_mov_b32 s82, 0\n" \
"s_mov_b32 s39, 0\n" \
"s_mov_b32 s28, 835\n" \
"s_mov_b32 s29, 330\n" \
"s_sub_u32 s75, s65, s7\n" \
"s_mul_i32 s12, s75, 3\n" \
"s_sub_u32 s75, s66, s7\n" \
"s_mul_i32 s13, s75, 3\n" \
"v_mov_b32 v7, 71680\n" \
"s_mov_b32 s71, 0\n" \
"s_mov_b32 s72, s98\n" \
"s_mov_b32 s80, 0x3e0293ee\n" \
"s_mov_b32 s81, 0xbe0293ee\n" \
"s_mov_b32 s92, 0\n" \
"s_mov_b32 s93, 0\n" \
"s_mov_b32 s94, 0\n" \
"s_mov_b32 s95, 0\n" \
"s_mov_b32 s96, 0\n" \
"v_mov_b32 v100, 0\n" \
"ds_write_b32 v7, v100 offset:16\n" \
"s_waitcnt lgkmcnt(0)\n" \
"s_barrier \n" \
"ds_read_b128 v[76:79], v27 offset:0\n" \
"ds_read_b128 v[80:83], v27 offset:128\n" \
"ds_read_b128 v[84:87], v15 offset:0\n" \
"ds_read_b128 v[88:91], v15 offset:4096\n" \
"ds_read_b128 v[92:95], v27 offset:1024\n" \
"ds_read_b128 v[96:99], v27 offset:1152\n" \
"ds_read_b128 v[100:103], v15 offset:1024\n" \
"ds_read_b128 v[104:107], v15 offset:5120\n" \
"s_waitcnt lgkmcnt(5)\n" \
"v_mfma_f32_16x16x32_bf16 v[36:39], v[76:79], v[84:87], 0\n" \
"s_waitcnt lgkmcnt(4)\n" \
"v_mfma_f32_16x16x32_bf16 v[40:43], v[76:79], v[88:91], 0\n" \
"v_mfma_f32_16x16x32_bf16 v[108:111], v[80:83], v[84:87], 0\n" \
"v_mfma_f32_16x16x32_bf16 v[112:115], v[80:83], v[88:91], 0\n" \
"ds_read_b128 v[76:79], v27 offset:2048\n" \
"ds_read_b128 v[80:83], v27 offset:2176\n" \
"ds_read_b128 v[84:87], v15 offset:2048\n" \
"ds_read_b128 v[88:91], v15 offset:6144\n" \
"s_waitcnt lgkmcnt(5)\n" \
"v_mfma_f32_16x16x32_bf16 v[36:39], v[92:95], v[100:103], v[36:39]\n" \
"s_waitcnt lgkmcnt(4)\n" \
"v_mfma_f32_16x16x32_bf16 v[40:43], v[92:95], v[104:107], v[40:43]\n" \
"v_mfma_f32_16x16x32_bf16 v[108:111], v[96:99], v[100:103], v[108:111]\n" \
"v_mfma_f32_16x16x32_bf16 v[112:115], v[96:99], v[104:107], v[112:115]\n" \
"ds_read_b128 v[92:95], v27 offset:3072\n" \
"ds_read_b128 v[96:99], v27 offset:3200\n" \
"ds_read_b128 v[100:103], v15 offset:3072\n" \
"ds_read_b128 v[104:107], v15 offset:7168\n" \
"s_waitcnt lgkmcnt(5)\n" \
"v_mfma_f32_16x16x32_bf16 v[36:39], v[76:79], v[84:87], v[36:39]\n" \
"s_waitcnt lgkmcnt(4)\n" \
"v_mfma_f32_16x16x32_bf16 v[40:43], v[76:79], v[88:91], v[40:43]\n" \
"v_mfma_f32_16x16x32_bf16 v[108:111], v[80:83], v[84:87], v[108:111]\n" \
"v_mfma_f32_16x16x32_bf16 v[112:115], v[80:83], v[88:91], v[112:115]\n" \
"s_waitcnt lgkmcnt(1)\n" \
"v_mfma_f32_16x16x32_bf16 v[36:39], v[92:95], v[100:103], v[36:39]\n" \
"s_waitcnt lgkmcnt(0)\n" \
"v_mfma_f32_16x16x32_bf16 v[40:43], v[92:95], v[104:107], v[40:43]\n" \
"v_mfma_f32_16x16x32_bf16 v[108:111], v[96:99], v[100:103], v[108:111]\n" \
"v_mfma_f32_16x16x32_bf16 v[112:115], v[96:99], v[104:107], v[112:115]\n" \
"v_cvt_pk_bf16_f32 v20, v36, v37\n" \
"v_cvt_pk_bf16_f32 v21, v38, v39\n" \
"s_nop 1\n" \
"v_cvt_pk_bf16_f32 v24, v108, v109\n" \
"v_cvt_pk_bf16_f32 v25, v110, v111\n" \
"v_cvt_pk_bf16_f32 v28, v40, v41\n" \
"v_cvt_pk_bf16_f32 v29, v42, v43\n" \
"v_cvt_pk_bf16_f32 v32, v112, v113\n" \
"v_cvt_pk_bf16_f32 v33, v114, v115\n" \
"s_cmp_gt_u32 s64, 1\n" \
"s_cbranch_scc1 R_hi%=\n" \
"ds_write_b64 v11, v[20:21] offset:0\n" \
"ds_write_b64 v11, v[24:25] offset:8\n" \
"ds_write_b64 v11, v[28:29] offset:1024\n" \
"ds_write_b64 v11, v[32:33] offset:1032\n" \
"s_waitcnt lgkmcnt(0)\n" \
"s_cmp_eq_u32 s64, 1\n" \
"s_cbranch_scc1 R1_loop%=\n" \
"s_branch R0_entry%=\n" \
"R_hi%=:\n" \
"v_subrev_u32 v15, 16384, v15\n" \
"s_mov_b32 s5, s99\n" \
"s_mov_b32 s6, s97\n" \
"v_xor_b32 v11, 16384, v11\n" \
"s_nop 1\n" \
"s_cmp_eq_u32 s64, 2\n" \
"s_cbranch_scc1 R2_loop%=\n" \
"s_branch R3_loop%=\n" \
"R0_entry%=:\n" \
"buffer_load_dwordx4 v[36:39], v1, s[40:43], s71 offen offset:0 sc1\n" \
"buffer_load_dwordx4 v[40:43], v1, s[40:43], s71 offen offset:1024 sc1\n" \
"s_branch R0_in%=\n" \
"R0_top%=:\n" \
"R0_w2%=:\n" \
"s_memtime s[14:15]\n" \
"s_getpc_b64 s[90:91]\n" \
"R0_w2pc%=:\n" \
"s_add_u32 s90, s90, R0_w2x%=-R0_w2pc%=\n" \
"s_addc_u32 s91, s91, 0\n" \
"v_mov_b32 v7, v7\n" \
"s_nop 0\n" \
"v_mov_b32 v7, v7\n" \
"s_nop 0\n" \
"v_mov_b32 v7, v7\n" \
"s_nop 0\n" \
"s_waitcnt lgkmcnt(0)\n" \
"s_sub_u32 s75, s34, s14\n" \
"v_mov_b32 v7, v7\n" \
"s_cmp_gt_i32 s75, 384\n" \
"s_cbranch_scc1 R0_w2%=\n" \
"s_max_i32 s75, s75, 0\n" \
"s_and_b32 s75, s75, -4\n" \
"s_sub_u32 s90, s90, s75\n" \
"s_subb_u32 s91, s91, 0\n" \
"v_mov_b32 v7, v7\n" \
"s_setpc_b64 s[90:91]\n" \
"v_mov_b32_e32 v7, v7\n" \
"v_mov_b32_e32 v7, v7\n" \
"v_mov_b32_e32 v7, v7\n" \
"v_mov_b32_e32 v7, v7\n" \
"v_mov_b32_e32 v7, v7\n" \
"v_mov_b32_e32 v7, v7\n" \
"v_mov_b32_e32 v7, v7\n" \
"v_mov_b32_e32 v7, v7\n" \
"v_mov_b32_e32 v7, v7\n" \
"v_mov_b32_e32 v7, v7\n" \
"v_mov_b32_e32 v7, v7\n" \
"v_mov_b32_e32 v7, v7\n" \
"v_mov_b32_e32 v7, v7\n" \
"v_mov_b32_e32 v7, v7\n" \
"v_mov_b32_e32 v7, v7\n" \
"v_mov_b32_e32 v7, v7\n" \
"v_mov_b32_e32 v7, v7\n" \
"v_mov_b32_e32 v7, v7\n" \
"v_mov_b32_e32 v7, v7\n" \
"v_mov_b32_e32 v7, v7\n" \
"v_mov_b32_e32 v7, v7\n" \
"v_mov_b32_e32 v7, v7\n" \
"v_mov_b32_e32 v7, v7\n" \
"v_mov_b32_e32 v7, v7\n" \
"v_mov_b32_e32 v7, v7\n" \
"v_mov_b32_e32 v7, v7\n" \
"v_mov_b32_e32 v7, v7\n" \
"v_mov_b32_e32 v7, v7\n" \
"v_mov_b32_e32 v7, v7\n" \
"v_mov_b32_e32 v7, v7\n" \
"v_mov_b32_e32 v7, v7\n" \
"v_mov_b32_e32 v7, v7\n" \
"v_mov_b32_e32 v7, v7\n" \
"v_mov_b32_e32 v7, v7\n" \
"v_mov_b32_e32 v7, v7\n" \
"v_mov_b32_e32 v7, v7\n" \
"v_mov_b32_e32 v7, v7\n" \
"v_mov_b32_e32 v7, v7\n" \
"v_mov_b32_e32 v7, v7\n" \
"v_mov_b32_e32 v7, v7\n" \
"v_mov_b32_e32 v7, v7\n" \
"v_mov_b32_e32 v7, v7\n" \
"v_mov_b32_e32 v7, v7\n" \
"v_mov_b32_e32 v7, v7\n" \
"v_mov_b32_e32 v7, v7\n" \
"v_mov_b32_e32 v7, v7\n" \
"v_mov_b32_e32 v7, v7\n" \
"v_mov_b32_e32 v7, v7\n" \
"v_mov_b32_e32 v7, v7\n" \
"v_mov_b32_e32 v7, v7\n" \
"v_mov_b32_e32 v7, v7\n" \
"v_mov_b32_e32 v7, v7\n" \
"v_mov_b32_e32 v7, v7\n" \
"v_mov_b32_e32 v7, v7\n" \
"v_mov_b32_e32 v7, v7\n" \
"v_mov_b32_e32 v7, v7\n" \
"v_mov_b32_e32 v7, v7\n" \
"v_mov_b32_e32 v7, v7\n" \
"v_mov_b32_e32 v7, v7\n" \
"v_mov_b32_e32 v7, v7\n" \
"v_mov_b32_e32 v7, v7\n" \
"v_mov_b32_e32 v7, v7\n" \
"v_mov_b32_e32 v7, v7\n" \
"v_mov_b32_e32 v7, v7\n" \
"v_mov_b32_e32 v7, v7\n" \
"v_mov_b32_e32 v7, v7\n" \
"v_mov_b32_e32 v7, v7\n" \
"v_mov_b32_e32 v7, v7\n" \
"v_mov_b32_e32 v7, v7\n" \
"v_mov_b32_e32 v7, v7\n" \
"v_mov_b32_e32 v7, v7\n" \
"v_mov_b32_e32 v7, v7\n" \
"v_mov_b32_e32 v7, v7\n" \
"v_mov_b32_e32 v7, v7\n" \
"v_mov_b32_e32 v7, v7\n" \
"v_mov_b32_e32 v7, v7\n" \
"v_mov_b32_e32 v7, v7\n" \
"v_mov_b32_e32 v7, v7\n" \
"v_mov_b32_e32 v7, v7\n" \
"v_mov_b32_e32 v7, v7\n" \
"v_mov_b32_e32 v7, v7\n" \
"v_mov_b32_e32 v7, v7\n" \
"v_mov_b32_e32 v7, v7\n" \
"v_mov_b32_e32 v7, v7\n" \
"v_mov_b32_e32 v7, v7\n" \
"v_mov_b32_e32 v7, v7\n" \
"v_mov_b32_e32 v7, v7\n" \
"v_mov_b32_e32 v7, v7\n" \
"v_mov_b32_e32 v7, v7\n" \
"v_mov_b32_e32 v7, v7\n" \
"v_mov_b32_e32 v7, v7\n" \
"v_mov_b32_e32 v7, v7\n" \
"v_mov_b32_e32 v7, v7\n" \
"v_mov_b32_e32 v7, v7\n" \
"v_mov_b32_e32 v7, v7\n" \
"v_mov_b32_e32 v7, v7\n" \
"R0_w2x%=:\n" \
"R0_loop%=:\n" \
"buffer_load_dwordx4 v[36:39], v1, s[40:43], s71 offen offset:0 sc1\n" \
"buffer_load_dwordx4 v[40:43], v1, s[40:43], s71 offen offset:1024 sc1\n" \
"s_sub_u32 s37, s22, s9\n" \
"s_mul_i32 s37, s37, s11\n" \
"s_ashr_i32 s37, s37, 14\n" \
"s_add_u32 s37, s37, s8\n" \
"s_pack_ll_b32_b16 s75, s30, s37\n" \
"s_min_u32 s75, s75, -2\n" \
"v_mov_b32 v14, s75\n" \
"v_and_b32 v19, 0xbfffffff, v19\n" \
"s_mov_b32 s35, s36\n" \
"s_lshr_b32 s75, s70, 1\n" \
"s_lshl_b32 s75, s75, 14\n" \
"s_and_b32 s36, s70, 1\n" \
"s_lshl_b32 s36, s36, 12\n" \
"s_or_b32 s36, s36, s75\n" \
"s_add_u32 s73, s73, 524288\n" \
"s_and_b32 s73, s73, 2097151\n" \
"s_lshr_b32 s75, s73, 7\n" \
"s_and_b32 s75, s75, 0x1000\n" \
"s_xor_b32 s75, s75, 0x5000\n" \
"v_xor_b32 v2, s75, v2\n" \
"v_xor_b32 v3, s75, v3\n" \
"R0_in%=:\n" \
"v_mov_b64 v[100:101], 0\n" \
"v_mov_b64 v[102:103], 0\n" \
"v_mov_b64 v[104:105], 0\n" \
"v_mov_b64 v[106:107], 0\n" \
"v_mov_b64 v[108:109], 0\n" \
"v_mov_b64 v[110:111], 0\n" \
"v_mov_b64 v[112:113], 0\n" \
"v_mov_b64 v[114:115], 0\n" \
"v_mov_b64 v[116:117], 0\n" \
"v_mov_b64 v[118:119], 0\n" \
"v_mov_b64 v[120:121], 0\n" \
"v_mov_b64 v[122:123], 0\n" \
"s_mov_b32 s46, 3\n" \
"s_sub_u32 s37, s2, s26\n" \
"s_cmp_lg_u32 s27, 0\n" \
"s_cselect_b32 s37, s37, s28\n" \
"s_sub_u32 s37, s37, s28\n" \
"s_cmp_gt_i32 s37, 70\n" \
"s_cselect_b32 s37, 24, s37\n" \
"s_add_u32 s75, s37, 12\n" \
"s_ashr_i32 s75, s75, s46\n" \
"s_min_i32 s75, s75, -1\n" \
"s_max_i32 s75, s75, s37\n" \
"s_add_u32 s28, s28, s75\n" \
"s_add_u32 s2, s2, s65\n" \
"s_sub_u32 s38, s2, s24\n" \
"s_cmp_lg_u32 s27, 0\n" \
"s_cselect_b32 s75, s7, -150\n" \
"s_cmp_le_i32 s38, s75\n" \
"s_cselect_b32 s38, 1, 0\n" \
"s_cmp_ge_u32 s70, 2\n" \
"s_cselect_b32 s38, s38, 0\n" \
"s_cmp_lg_u32 s38, 0\n" \
"s_cselect_b32 s6, s67, s6\n" \
"s_sub_u32 s37, s3, s24\n" \
"s_max_i32 s75, s37, s29\n" \
"s_cmp_lg_u32 s27, 0\n" \
"s_cselect_b32 s37, s37, s75\n" \
"s_cmp_lg_u32 s38, 0\n" \
"s_cselect_b32 s37, s37, s29\n" \
"s_sub_u32 s37, s37, s29\n" \
"s_cmp_gt_i32 s37, 70\n" \
"s_cselect_b32 s37, 24, s37\n" \
"s_add_u32 s75, s37, 12\n" \
"s_ashr_i32 s75, s75, s46\n" \
"s_min_i32 s75, s75, -1\n" \
"s_max_i32 s75, s75, s37\n" \
"s_add_u32 s29, s29, s75\n" \
"s_mov_b32 s24, s18\n" \
"s_mov_b32 s26, s5\n" \
"s_mov_b32 s27, s17\n" \
"s_add_u32 s18, s25, s28\n" \
"s_add_u32 s18, s18, s65\n" \
"s_cmp_lt_u32 s70, 6\n" \
"s_cselect_b32 s75, 1, 3\n" \
"s_lshr_b32 s75, s47, s75\n" \
"s_sub_u32 s47, s47, s75\n" \
"s_sub_u32 s47, s47, 1\n" \
"s_max_i32 s47, s47, 0\n" \
"s_lshr_b32 s75, s47, 2\n" \
"s_add_u32 s18, s18, s75\n" \
"s_sub_u32 s37, s8, s44\n" \
"s_sub_u32 s38, s9, s45\n" \
"s_mov_b32 s44, s8\n" \
"s_mov_b32 s45, s9\n" \
"s_mul_i32 s37, s37, s10\n" \
"s_lshl_b32 s38, s38, 8\n" \
"s_sub_u32 s37, s38, s37\n" \
"s_ashr_i32 s37, s37, 12\n" \
"s_add_u32 s10, s10, s37\n" \
"s_max_i32 s10, s10, 400\n" \
"s_min_i32 s10, s10, 900\n" \
"s_mul_i32 s37, s10, s11\n" \
"s_sub_u32 s37, 4194304, s37\n" \
"s_ashr_i32 s37, s37, 10\n" \
"s_add_u32 s11, s11, s37\n" \
"s_waitcnt vmcnt(1)\n" \
"ds_write_b128 v0, v[36:39] offset:0\n" \
"s_waitcnt vmcnt(0)\n" \
"ds_write_b128 v0, v[40:43] offset:1024\n" \
"v_smfmac_f32_16x16x64_bf16 v[100:103], v[36:39], a[0:7], v6\n" \
"v_smfmac_f32_16x16x64_bf16 v[100:103], v[40:43], a[8:15], v6\n" \
"v_smfmac_f32_16x16x64_bf16 v[108:111], v[36:39], a[128:135], v6\n" \
"buffer_store_dword v14, v19, s[40:43], s35 offen sc1\n" \
"v_smfmac_f32_16x16x64_bf16 v[104:107], v[36:39], a[64:71], v6\n" \
"v_cmp_u_f32 vcc, v100, v101\n" \
"v_cmp_u_f32 s[84:85], v102, v103\n" \
"v_smfmac_f32_16x16x64_bf16 v[112:115], v[36:39], a[192:199], v6\n" \
"s_or_b64 vcc, vcc, s[84:85]\n" \
"s_cbranch_vccnz R0_slowE%=\n" \
"R0_okE%=:\n" \
"s_waitcnt lgkmcnt(0)\n" \
"s_barrier \n" \
"ds_read_b128 v[44:47], v0 offset:2048\n" \
"ds_read_b128 v[48:51], v0 offset:3072\n" \
"v_smfmac_f32_16x16x64_bf16 v[108:111], v[40:43], a[136:143], v6\n" \
"ds_read_b128 v[52:55], v0 offset:4096\n" \
"ds_read_b128 v[56:59], v0 offset:5120\n" \
"v_smfmac_f32_16x16x64_bf16 v[104:107], v[40:43], a[72:79], v6\n" \
"ds_read_b128 v[60:63], v0 offset:6144\n" \
"ds_read_b128 v[64:67], v0 offset:7168\n" \
"v_smfmac_f32_16x16x64_bf16 v[112:115], v[40:43], a[200:207], v6\n" \
"v_smfmac_f32_16x16x64_bf16 v[116:119], v[36:39], v[128:135], v6\n" \
"v_smfmac_f32_16x16x64_bf16 v[120:123], v[36:39], v[192:199], v6\n" \
"s_waitcnt lgkmcnt(5)\n" \
"v_smfmac_f32_16x16x64_bf16 v[100:103], v[44:47], a[16:23], v6\n" \
"buffer_store_dwordx2 v[16:17], v5, s[40:43], s31 offen sc1\n" \
"v_smfmac_f32_16x16x64_bf16 v[108:111], v[44:47], a[144:151], v6\n" \
"ds_read_b128 v[68:71], v10 offset:0\n" \
"v_smfmac_f32_16x16x64_bf16 v[104:107], v[44:47], a[80:87], v6\n" \
"ds_read_b128 v[72:75], v10 offset:4096\n" \
"v_smfmac_f32_16x16x64_bf16 v[112:115], v[44:47], a[208:215], v6\n" \
"ds_read_b128 v[76:79], v10 offset:1024\n" \
"s_waitcnt lgkmcnt(7)\n" \
"v_smfmac_f32_16x16x64_bf16 v[100:103], v[48:51], a[24:31], v6\n" \
"ds_read_b128 v[80:83], v10 offset:5120\n" \
"v_smfmac_f32_16x16x64_bf16 v[108:111], v[48:51], a[152:159], v6\n" \
"ds_read_b128 v[84:87], v10 offset:2048\n" \
"v_smfmac_f32_16x16x64_bf16 v[104:107], v[48:51], a[88:95], v6\n" \
"ds_read_b128 v[88:91], v10 offset:6144\n" \
"v_smfmac_f32_16x16x64_bf16 v[112:115], v[48:51], a[216:223], v6\n" \
"ds_read_b128 v[92:95], v10 offset:3072\n" \
"s_waitcnt lgkmcnt(10)\n" \
"v_smfmac_f32_16x16x64_bf16 v[100:103], v[52:55], a[32:39], v6\n" \
"ds_read_b128 v[96:99], v10 offset:7168\n" \
"v_smfmac_f32_16x16x64_bf16 v[108:111], v[52:55], a[160:167], v6\n" \
"ds_read_b64 v[20:21], v11 offset:0\n" \
"v_smfmac_f32_16x16x64_bf16 v[104:107], v[52:55], a[96:103], v6\n" \
"ds_read_b64 v[24:25], v11 offset:8\n" \
"v_smfmac_f32_16x16x64_bf16 v[112:115], v[52:55], a[224:231], v6\n" \
"ds_read_b64 v[28:29], v11 offset:1024\n" \
"s_waitcnt lgkmcnt(13)\n" \
"v_smfmac_f32_16x16x64_bf16 v[100:103], v[56:59], a[40:47], v6\n" \
"ds_read_b64 v[32:33], v11 offset:1032\n" \
"v_smfmac_f32_16x16x64_bf16 v[108:111], v[56:59], a[168:175], v6\n" \
"v_xor_b32 v10, 16384, v10\n" \
"v_smfmac_f32_16x16x64_bf16 v[104:107], v[56:59], a[104:111], v6\n" \
"s_mov_b32 vcc_lo, 0x33333333\n" \
"v_smfmac_f32_16x16x64_bf16 v[112:115], v[56:59], a[232:239], v6\n" \
"s_mov_b32 vcc_hi, 0x33333333\n" \
"s_waitcnt lgkmcnt(13)\n" \
"v_smfmac_f32_16x16x64_bf16 v[100:103], v[60:63], a[48:55], v6\n" \
"v_smfmac_f32_16x16x64_bf16 v[108:111], v[60:63], a[176:183], v6\n" \
"v_smfmac_f32_16x16x64_bf16 v[104:107], v[60:63], a[112:119], v6\n" \
"v_smfmac_f32_16x16x64_bf16 v[112:115], v[60:63], a[240:247], v6\n" \
"s_waitcnt lgkmcnt(12)\n" \
"v_smfmac_f32_16x16x64_bf16 v[100:103], v[64:67], a[56:63], v6\n" \
"v_smfmac_f32_16x16x64_bf16 v[108:111], v[64:67], a[184:191], v6\n" \
"v_smfmac_f32_16x16x64_bf16 v[104:107], v[64:67], a[120:127], v6\n" \
"v_smfmac_f32_16x16x64_bf16 v[112:115], v[64:67], a[248:255], v6\n" \
"v_add_f32 v100, v100, v102\n" \
"v_add_f32 v101, v101, v103\n" \
"v_cvt_pk_bf16_f32 v124, v100, v101\n" \
"v_add_f32 v108, v108, v110\n" \
"v_add_f32 v109, v109, v111\n" \
"v_cvt_pk_bf16_f32 v125, v108, v109\n" \
"v_pk_add_f32 v[104:105], v[104:105], v[106:107]\n" \
"v_cvt_pk_bf16_f32 v126, v104, v105\n" \
"v_pk_add_f32 v[112:113], v[112:113], v[114:115]\n" \
"v_cvt_pk_bf16_f32 v127, v112, v113\n" \
"v_smfmac_f32_16x16x64_bf16 v[116:119], v[40:43], v[136:143], v6\n" \
"ds_write_b128 v8, v[124:127]\n" \
"v_smfmac_f32_16x16x64_bf16 v[120:123], v[40:43], v[200:207], v6\n" \
"v_smfmac_f32_16x16x64_bf16 v[116:119], v[44:47], v[144:151], v6\n" \
"v_smfmac_f32_16x16x64_bf16 v[120:123], v[44:47], v[208:215], v6\n" \
"v_smfmac_f32_16x16x64_bf16 v[116:119], v[48:51], v[152:159], v6\n" \
"s_waitcnt lgkmcnt(0)\n" \
"s_barrier \n" \
"ds_read_b64_tr_b16 v[36:37], v9 offset:0\n" \
"ds_read_b64_tr_b16 v[38:39], v9 offset:64\n" \
"v_smfmac_f32_16x16x64_bf16 v[120:123], v[48:51], v[216:223], v6\n" \
"ds_read_b64_tr_b16 v[40:41], v9 offset:256\n" \
"ds_read_b64_tr_b16 v[42:43], v9 offset:320\n" \
"v_smfmac_f32_16x16x64_bf16 v[116:119], v[52:55], v[160:167], v6\n" \
"ds_read_b64_tr_b16 v[124:125], v9 offset:512\n" \
"ds_read_b64_tr_b16 v[126:127], v9 offset:576\n" \
"v_smfmac_f32_16x16x64_bf16 v[120:123], v[52:55], v[224:231], v6\n" \
"ds_read_b64_tr_b16 v[112:113], v9 offset:768\n" \
"ds_read_b64_tr_b16 v[114:115], v9 offset:832\n" \
"v_smfmac_f32_16x16x64_bf16 v[116:119], v[56:59], v[168:175], v6\n" \
"s_waitcnt lgkmcnt(6)\n" \
"v_mfma_f32_16x16x32_bf16 v[100:103], v[68:71], v[36:39], 0\n" \
"buffer_load_dword v16, v18, s[40:43], s35 offen sc1\n" \
"v_mfma_f32_16x16x32_bf16 v[104:107], v[72:75], v[36:39], 0\n" \
"v_mov_b32 v68, 0x3f803f80\n" \
"v_mov_b32 v69, 0x3f803f80\n" \
"buffer_load_dword v17, v18, s[40:43], s35 offen offset:2048 sc1\n" \
"v_mfma_f32_16x16x32_bf16 v[108:111], v[36:39], v[36:39], 0\n" \
"v_mov_b32 v70, 0x3f80\n" \
"v_mov_b32 v71, 0x3f80\n" \
"s_waitcnt lgkmcnt(4)\n" \
"v_mfma_f32_16x16x32_bf16 v[100:103], v[76:79], v[40:43], v[100:103]\n" \
"v_mov_b32 v72, 0x3f803f80\n" \
"v_mov_b32 v73, 0x3f803f80\n" \
"v_mfma_f32_16x16x32_bf16 v[104:107], v[80:83], v[40:43], v[104:107]\n" \
"v_mov_b32 v74, 0x3f80\n" \
"v_mov_b32 v75, 0x3f80\n" \
"v_mfma_f32_16x16x32_bf16 v[108:111], v[40:43], v[40:43], v[108:111]\n" \
"v_mov_b64 v[76:77], 0\n" \
"v_mov_b64 v[78:79], 0\n" \
"s_waitcnt lgkmcnt(2)\n" \
"v_mfma_f32_16x16x32_bf16 v[100:103], v[84:87], v[124:127], v[100:103]\n" \
"v_mov_b64 v[80:81], 0\n" \
"v_mov_b64 v[82:83], 0\n" \
"v_mfma_f32_16x16x32_bf16 v[104:107], v[88:91], v[124:127], v[104:107]\n" \
"v_mov_b64 v[84:85], 0\n" \
"v_mov_b64 v[86:87], 0\n" \
"v_mfma_f32_16x16x32_bf16 v[108:111], v[124:127], v[124:127], v[108:111]\n" \
"s_waitcnt lgkmcnt(0)\n" \
"v_mfma_f32_16x16x32_bf16 v[100:103], v[92:95], v[112:115], v[100:103]\n" \
"v_mfma_f32_16x16x32_bf16 v[104:107], v[96:99], v[112:115], v[104:107]\n" \
"v_mfma_f32_16x16x32_bf16 v[108:111], v[112:115], v[112:115], v[108:111]\n" \
"v_cndmask_b32_dpp v100, v102, v100, vcc quad_perm:[0,1,0,1] row_mask:0xf bank_mask:0xf\n" \
"v_cndmask_b32_dpp v101, v103, v101, vcc quad_perm:[0,1,0,1] row_mask:0xf bank_mask:0xf\n" \
"v_smfmac_f32_16x16x64_bf16 v[120:123], v[56:59], v[232:239], v6\n" \
"v_cndmask_b32_dpp v104, v106, v104, vcc quad_perm:[0,1,0,1] row_mask:0xf bank_mask:0xf\n" \
"v_cndmask_b32_dpp v105, v107, v105, vcc quad_perm:[0,1,0,1] row_mask:0xf bank_mask:0xf\n" \
"v_max3_f32 v92, v100, v101, v104\n" \
"s_nop 0\n" \
"v_cndmask_b32_dpp v110, v111, v110, vcc quad_perm:[0,1,0,1] row_mask:0xf bank_mask:0xf\n" \
"v_max3_f32 v92, v92, v105, v110\n" \
"v_smfmac_f32_16x16x64_bf16 v[116:119], v[60:63], v[176:183], v6\n" \
"s_nop 0\n" \
"v_max_f32_dpp v93, v92, v92 quad_perm:[2,3,0,1] row_mask:0xf bank_mask:0xf\n" \
"v_max_f32_dpp v92, v92, v92 quad_perm:[2,3,0,1] row_mask:0xf bank_mask:0xf\n" \
"v_smfmac_f32_16x16x64_bf16 v[120:123], v[60:63], v[240:247], v6\n" \
"s_nop 0\n" \
"v_permlane16_swap_b32 v92, v93\n" \
"v_max_f32 v94, v92, v93\n" \
"v_max_f32 v92, v92, v93\n" \
"v_smfmac_f32_16x16x64_bf16 v[116:119], v[64:67], v[184:191], v6\n" \
"s_nop 0\n" \
"v_permlane32_swap_b32 v92, v94\n" \
"v_max_f32 v92, v92, v94\n" \
"v_mul_f32 v94, s81, v92\n" \
"v_fma_f32 v110, v110, s80, v94\n" \
"v_pk_fma_f32 v[100:101], v[100:101], s[80:81], v[94:95] op_sel_hi:[1,0,0]\n" \
"v_pk_fma_f32 v[104:105], v[104:105], s[80:81], v[94:95] op_sel_hi:[1,0,0]\n" \
"v_smfmac_f32_16x16x64_bf16 v[120:123], v[64:67], v[248:255], v6\n" \
"v_exp_f32 v110, v110\n" \
"v_exp_f32 v100, v100\n" \
"v_exp_f32 v101, v101\n" \
"v_exp_f32 v104, v104\n" \
"v_exp_f32 v105, v105\n" \
"v_cvt_pk_bf16_f32 v91, v110, v110\n" \
"v_cvt_pk_bf16_f32 v88, v100, v101\n" \
"v_and_b32 v89, v12, v91\n" \
"v_and_b32 v91, v13, v91\n" \
"v_cvt_pk_bf16_f32 v90, v104, v105\n" \
"v_cvt_pk_bf16_f32 v22, v116, v118\n" \
"v_cvt_pk_bf16_f32 v23, v117, v119\n" \
"v_smfmac_f32_16x16x64_bf16 v[76:79], v[88:91], v[68:75], v6\n" \
"v_mov_b64 v[26:27], v[22:23]\n" \
"v_cvt_pk_bf16_f32 v30, v120, v122\n" \
"v_cvt_pk_bf16_f32 v31, v121, v123\n" \
"v_smfmac_f32_16x16x64_bf16 v[80:83], v[88:91], v[20:27], v6\n" \
"v_mov_b64 v[34:35], v[30:31]\n" \
"s_nop 1\n" \
"v_smfmac_f32_16x16x64_bf16 v[84:87], v[88:91], v[28:35], v6\n" \
"v_pk_add_f32 v[76:77], v[76:77], v[78:79]\n" \
"v_rcp_f32 v76, v76\n" \
"v_rcp_f32 v77, v77\n" \
"s_nop 0\n" \
"v_pk_add_f32 v[80:81], v[80:81], v[82:83]\n" \
"v_pk_mul_f32 v[80:81], v[80:81], v[76:77]\n" \
"v_cvt_pk_bf16_f32 v116, v80, v81\n" \
"v_min_u32 v116, -2, v116\n" \
"v_pk_add_f32 v[84:85], v[84:85], v[86:87]\n" \
"v_pk_mul_f32 v[84:85], v[84:85], v[76:77]\n" \
"v_cvt_pk_bf16_f32 v117, v84, v85\n" \
"v_min_u32 v117, -2, v117\n" \
"buffer_store_dwordx2 v[116:117], v2, s[40:43], s73 offen sc1\n" \
"s_memtime s[20:21]\n" \
"s_memrealtime s[48:49]\n" \
"v_mov_b32 v7, v7\n" \
"s_add_u32 s71, s71, 65536\n" \
"s_memrealtime s[50:51]\n" \
"v_mov_b32 v7, v7\n" \
"s_add_u32 s70, s70, 1\n" \
"s_memrealtime s[52:53]\n" \
"v_mov_b32 v7, v7\n" \
"s_mov_b64 s[90:91], s[0:1]\n" \
"s_memrealtime s[54:55]\n" \
"v_mov_b32 v7, v7\n" \
"s_memrealtime s[56:57]\n" \
"v_mov_b32 v7, v7\n" \
"s_nop 0\n" \
"s_memrealtime s[58:59]\n" \
"v_mov_b32 v7, v7\n" \
"s_nop 0\n" \
"s_memrealtime s[60:61]\n" \
"v_mov_b32 v7, v7\n" \
"s_nop 0\n" \
"s_memrealtime s[62:63]\n" \
"v_mov_b32 v7, v7\n" \
"s_nop 0\n" \
"v_mov_b32 v7, v7\n" \
"s_nop 0\n" \
"v_mov_b32 v7, v7\n" \
"s_nop 0\n" \
"v_mov_b32 v7, v7\n" \
"s_nop 0\n" \
"s_waitcnt lgkmcnt(0)\n" \
"s_add_u32 s8, s48, s50\n" \
"s_add_u32 s8, s8, s52\n" \
"s_add_u32 s8, s8, s54\n" \
"s_add_u32 s8, s8, s56\n" \
"s_add_u32 s8, s8, s58\n" \
"s_add_u32 s8, s8, s60\n" \
"s_add_u32 s8, s8, s62\n" \
"s_mov_b32 s9, s20\n" \
"s_sub_u32 s75, s18, s8\n" \
"s_mul_i32 s75, s75, s10\n" \
"s_ashr_i32 s75, s75, 8\n" \
"s_sub_u32 s75, s75, 132\n" \
"s_add_u32 s37, s75, 120\n" \
"s_cmp_gt_u32 s37, 504\n" \
"v_mov_b32 v7, v7\n" \
"s_cbranch_scc1 R0_off1%=\n" \
"s_max_i32 s75, s75, 32\n" \
"s_and_b32 s75, s75, -4\n" \
"s_sub_u32 s90, s90, s75\n" \
"s_subb_u32 s91, s91, 0\n" \
"v_mov_b32 v7, v7\n" \
"s_setpc_b64 s[90:91]\n" \
"R0_w1t%=:\n" \
"s_waitcnt lgkmcnt(0)\n" \
"R0_w1%=:\n" \
"s_memtime s[14:15]\n" \
"s_getpc_b64 s[90:91]\n" \
"R0_w1pc%=:\n" \
"s_add_u32 s90, s90, R0_w1x%=-R0_w1pc%=\n" \
"s_addc_u32 s91, s91, 0\n" \
"v_mov_b32 v7, v7\n" \
"s_nop 0\n" \
"v_mov_b32 v7, v7\n" \
"s_nop 0\n" \
"v_mov_b32 v7, v7\n" \
"s_nop 0\n" \
"s_waitcnt lgkmcnt(0)\n" \
"s_sub_u32 s75, s16, s14\n" \
"v_mov_b32 v7, v7\n" \
"s_cmp_gt_i32 s75, 384\n" \
"s_cbranch_scc1 R0_w1%=\n" \
"s_max_i32 s75, s75, 0\n" \
"s_and_b32 s75, s75, -4\n" \
"s_sub_u32 s90, s90, s75\n" \
"s_subb_u32 s91, s91, 0\n" \
"v_mov_b32 v7, v7\n" \
"s_setpc_b64 s[90:91]\n" \
"v_mov_b32_e32 v7, v7\n" \
"v_mov_b32_e32 v7, v7\n" \
"v_mov_b32_e32 v7, v7\n" \
"v_mov_b32_e32 v7, v7\n" \
"v_mov_b32_e32 v7, v7\n" \
"v_mov_b32_e32 v7, v7\n" \
"v_mov_b32_e32 v7, v7\n" \
"v_mov_b32_e32 v7, v7\n" \
"v_mov_b32_e32 v7, v7\n" \
"v_mov_b32_e32 v7, v7\n" \
"v_mov_b32_e32 v7, v7\n" \
"v_mov_b32_e32 v7, v7\n" \
"v_mov_b32_e32 v7, v7\n" \
"v_mov_b32_e32 v7, v7\n" \
"v_mov_b32_e32 v7, v7\n" \
"v_mov_b32_e32 v7, v7\n" \
"v_mov_b32_e32 v7, v7\n" \
"v_mov_b32_e32 v7, v7\n" \
"v_mov_b32_e32 v7, v7\n" \
"v_mov_b32_e32 v7, v7\n" \
"v_mov_b32_e32 v7, v7\n" \
"v_mov_b32_e32 v7, v7\n" \
"v_mov_b32_e32 v7, v7\n" \
"v_mov_b32_e32 v7, v7\n" \
"v_mov_b32_e32 v7, v7\n" \
"v_mov_b32_e32 v7, v7\n" \
"v_mov_b32_e32 v7, v7\n" \
"v_mov_b32_e32 v7, v7\n" \
"v_mov_b32_e32 v7, v7\n" \
"v_mov_b32_e32 v7, v7\n" \
"v_mov_b32_e32 v7, v7\n" \
"v_mov_b32_e32 v7, v7\n" \
"v_mov_b32_e32 v7, v7\n" \
"v_mov_b32_e32 v7, v7\n" \
"v_mov_b32_e32 v7, v7\n" \
"v_mov_b32_e32 v7, v7\n" \
"v_mov_b32_e32 v7, v7\n" \
"v_mov_b32_e32 v7, v7\n" \
"v_mov_b32_e32 v7, v7\n" \
"v_mov_b32_e32 v7, v7\n" \
"v_mov_b32_e32 v7, v7\n" \
"v_mov_b32_e32 v7, v7\n" \
"v_mov_b32_e32 v7, v7\n" \
"v_mov_b32_e32 v7, v7\n" \
"v_mov_b32_e32 v7, v7\n" \
"v_mov_b32_e32 v7, v7\n" \
"v_mov_b32_e32 v7, v7\n" \
"v_mov_b32_e32 v7, v7\n" \
"v_mov_b32_e32 v7, v7\n" \
"v_mov_b32_e32 v7, v7\n" \
"v_mov_b32_e32 v7, v7\n" \
"v_mov_b32_e32 v7, v7\n" \
"v_mov_b32_e32 v7, v7\n" \
"v_mov_b32_e32 v7, v7\n" \
"v_mov_b32_e32 v7, v7\n" \
"v_mov_b32_e32 v7, v7\n" \
"v_mov_b32_e32 v7, v7\n" \
"v_mov_b32_e32 v7, v7\n" \
"v_mov_b32_e32 v7, v7\n" \
"v_mov_b32_e32 v7, v7\n" \
"v_mov_b32_e32 v7, v7\n" \
"v_mov_b32_e32 v7, v7\n" \
"v_mov_b32_e32 v7, v7\n" \
"v_mov_b32_e32 v7, v7\n" \
"v_mov_b32_e32 v7, v7\n" \
"v_mov_b32_e32 v7, v7\n" \
"v_mov_b32_e32 v7, v7\n" \
"v_mov_b32_e32 v7, v7\n" \
"v_mov_b32_e32 v7, v7\n" \
"v_mov_b32_e32 v7, v7\n" \
"v_mov_b32_e32 v7, v7\n" \
"v_mov_b32_e32 v7, v7\n" \
"v_mov_b32_e32 v7, v7\n" \
"v_mov_b32_e32 v7, v7\n" \
"v_mov_b32_e32 v7, v7\n" \
"v_mov_b32_e32 v7, v7\n" \
"v_mov_b32_e32 v7, v7\n" \
"v_mov_b32_e32 v7, v7\n" \
"v_mov_b32_e32 v7, v7\n" \
"v_mov_b32_e32 v7, v7\n" \
"v_mov_b32_e32 v7, v7\n" \
"v_mov_b32_e32 v7, v7\n" \
"v_mov_b32_e32 v7, v7\n" \
"v_mov_b32_e32 v7, v7\n" \
"v_mov_b32_e32 v7, v7\n" \
"v_mov_b32_e32 v7, v7\n" \
"v_mov_b32_e32 v7, v7\n" \
"v_mov_b32_e32 v7, v7\n" \
"v_mov_b32_e32 v7, v7\n" \
"v_mov_b32_e32 v7, v7\n" \
"v_mov_b32_e32 v7, v7\n" \
"v_mov_b32_e32 v7, v7\n" \
"v_mov_b32_e32 v7, v7\n" \
"v_mov_b32_e32 v7, v7\n" \
"v_mov_b32_e32 v7, v7\n" \
"v_mov_b32_e32 v7, v7\n" \
"R0_w1x%=:\n" \
"buffer_load_dwordx4 v[36:39], v3, s[40:43], s73 offen sc1\n" \
"buffer_load_dwordx4 v[40:43], v3, s[40:43], s73 offen offset:1024 sc1\n" \
"ds_read_b128 v[56:59], v0 offset:12288\n" \
"s_sub_u32 s30, s20, s9\n" \
"s_mul_i32 s30, s30, s11\n" \
"s_ashr_i32 s30, s30, 14\n" \
"s_add_u32 s30, s30, s8\n" \
"s_lshl_b32 s31, s70, 16\n" \
"s_sub_u32 s75, s70, 1\n" \
"s_xor_b32 s75, s75, s70\n" \
"s_bitcmp1_b32 s75, 5\n" \
"s_cselect_b32 s76, s72, 0\n" \
"s_lshl_b32 s38, s75, 11\n" \
"s_and_b32 s38, s38, 0x4000\n" \
"s_xor_b32 s76, s76, s38\n" \
"s_lshl_b32 s38, s75, 8\n" \
"s_and_b32 s38, s38, 0x1000\n" \
"s_xor_b32 s76, s76, s38\n" \
"v_xor_b32 v1, s76, v1\n" \
"v_xor_b32 v4, s76, v4\n" \
"v_xor_b32 v5, s76, v5\n" \
"s_waitcnt vmcnt(3)\n" \
"v_max_u32 v49, v16, v17\n" \
"v_cmp_eq_u32 vcc, -1, v49\n" \
"s_mov_b32 s19, -1\n" \
"s_cbranch_vccnz R0_nopub%=\n" \
"s_lshl_b32 s37, s8, 16\n" \
"v_subrev_u32 v68, s8, v16\n" \
"v_subrev_u32 v69, s8, v17\n" \
"v_subrev_u32 v70, s37, v16\n" \
"v_subrev_u32 v71, s37, v17\n" \
"v_bfe_i32 v68, v68, 0, 16\n" \
"v_bfe_i32 v69, v69, 0, 16\n" \
"v_max_i32 v70, v70, v71\n" \
"v_max_i32 v68, v68, v69\n" \
"s_nop 0\n" \
"v_max_i32_dpp v70, v70, v70 quad_perm:[1,0,3,2] row_mask:0xf bank_mask:0xf\n" \
"v_max_i32_dpp v68, v68, v68 quad_perm:[1,0,3,2] row_mask:0xf bank_mask:0xf\n" \
"s_nop 0\n" \
"v_max_i32_dpp v70, v70, v70 quad_perm:[2,3,0,1] row_mask:0xf bank_mask:0xf\n" \
"v_max_i32_dpp v68, v68, v68 quad_perm:[2,3,0,1] row_mask:0xf bank_mask:0xf\n" \
"s_nop 0\n" \
"v_max_i32_dpp v70, v70, v70 row_half_mirror row_mask:0xf bank_mask:0xf\n" \
"v_max_i32_dpp v68, v68, v68 row_half_mirror row_mask:0xf bank_mask:0xf\n" \
"s_nop 0\n" \
"v_max_i32_dpp v70, v70, v70 row_mirror row_mask:0xf bank_mask:0xf\n" \
"v_max_i32_dpp v68, v68, v68 row_mirror row_mask:0xf bank_mask:0xf\n" \
"s_nop 0\n" \
"v_max_i32_dpp v70, v70, v70 row_bcast:15 row_mask:0xa bank_mask:0xf\n" \
"v_max_i32_dpp v68, v68, v68 row_bcast:15 row_mask:0xa bank_mask:0xf\n" \
"s_nop 0\n" \
"v_max_i32_dpp v70, v70, v70 row_bcast:31 row_mask:0xc bank_mask:0xf\n" \
"v_max_i32_dpp v68, v68, v68 row_bcast:31 row_mask:0xc bank_mask:0xf\n" \
"v_readlane_b32 s3, v70, 63\n" \
"v_readlane_b32 s2, v68, 63\n" \
"s_ashr_i32 s3, s3, 16\n" \
"s_add_u32 s3, s3, s8\n" \
"s_add_u32 s2, s2, s8\n" \
"s_add_u32 s37, s3, s66\n" \
"s_sub_u32 s38, s37, s25\n" \
"s_cmp_le_i32 s38, s7\n" \
"s_mov_b32 s17, 1\n" \
"s_cbranch_scc0 R0_resynca%=\n" \
"s_mov_b32 s6, s67\n" \
"R0_sua%=:\n" \
"s_mov_b32 s5, s25\n" \
"s_add_u32 s25, s18, s29\n" \
"s_add_u32 s25, s25, s66\n" \
"s_lshr_b32 s75, s47, 2\n" \
"s_add_u32 s25, s25, s75\n" \
"s_sub_u32 s34, s25, s8\n" \
"s_mul_i32 s34, s34, s10\n" \
"s_ashr_i32 s19, s34, 8\n" \
"s_add_u32 s34, s19, s9\n" \
"s_cmp_lt_i32 s19, 0\n" \
"s_cselect_b32 s19, -2, s19\n" \
"R0_nopub%=:\n" \
"s_cmp_gt_u32 s19, 2000\n" \
"s_cselect_b32 s75, 0, s70\n" \
"v_mov_b32 v44, s75\n" \
"v_mov_b32 v45, s34\n" \
"s_waitcnt lgkmcnt(0)\n" \
"R0_pw%=:\n" \
"s_getreg_b32 s75, hwreg(HW_REG_IB_STS, 0, 4)\n" \
"s_cmp_le_u32 s75, 1\n" \
"v_mov_b32 v7, v7\n" \
"s_cbranch_scc1 R0_redoB%=\n" \
"s_getreg_b32 s75, hwreg(HW_REG_IB_STS, 0, 4)\n" \
"s_cmp_le_u32 s75, 1\n" \
"v_mov_b32 v7, v7\n" \
"s_cbranch_scc1 R0_redoB%=\n" \
"s_getreg_b32 s75, hwreg(HW_REG_IB_STS, 0, 4)\n" \
"s_cmp_le_u32 s75, 1\n" \
"v_mov_b32 v7, v7\n" \
"s_cbranch_scc1 R0_redoB%=\n" \
"s_getreg_b32 s75, hwreg(HW_REG_IB_STS, 0, 4)\n" \
"s_cmp_le_u32 s75, 1\n" \
"v_mov_b32 v7, v7\n" \
"s_cbranch_scc1 R0_redoB%=\n" \
"s_getreg_b32 s75, hwreg(HW_REG_IB_STS, 0, 4)\n" \
"s_cmp_le_u32 s75, 1\n" \
"v_mov_b32 v7, v7\n" \
"s_cbranch_scc1 R0_redoB%=\n" \
"s_getreg_b32 s75, hwreg(HW_REG_IB_STS, 0, 4)\n" \
"s_cmp_le_u32 s75, 1\n" \
"v_mov_b32 v7, v7\n" \
"s_cbranch_scc1 R0_redoB%=\n" \
"s_getreg_b32 s75, hwreg(HW_REG_IB_STS, 0, 4)\n" \
"s_cmp_le_u32 s75, 1\n" \
"v_mov_b32 v7, v7\n" \
"s_cbranch_scc1 R0_redoB%=\n" \
"s_getreg_b32 s75, hwreg(HW_REG_IB_STS, 0, 4)\n" \
"s_cmp_le_u32 s75, 1\n" \
"v_mov_b32 v7, v7\n" \
"s_cbranch_scc0 R0_pw%=\n" \
"R0_redoB%=:\n" \
"s_waitcnt vmcnt(1)\n" \
"v_mfma_f32_16x16x32_bf16 v[48:51], v[56:59], v[36:39], 0\n" \
"s_waitcnt vmcnt(0)\n" \
"v_mfma_f32_16x16x32_bf16 v[48:51], v[56:59], v[40:43], v[48:51]\n" \
"s_nop 7\n" \
"v_cmp_u_f32 vcc, v48, v48\n" \
"v_add_f32_dpp v48, v48, v48 row_ror:4 row_mask:0xf bank_mask:0xf\n" \
"v_add_f32_dpp v49, v49, v49 row_ror:4 row_mask:0xf bank_mask:0xf\n" \
"v_add_f32_dpp v50, v50, v50 row_ror:4 row_mask:0xf bank_mask:0xf\n" \
"v_add_f32_dpp v51, v51, v51 row_ror:4 row_mask:0xf bank_mask:0xf\n" \
"s_cbranch_vccnz R0_slowB%=\n" \
"R0_okB%=:\n" \
"v_add_f32_dpp v48, v48, v48 row_ror:8 row_mask:0xf bank_mask:0xf\n" \
"v_add_f32_dpp v49, v49, v49 row_ror:8 row_mask:0xf bank_mask:0xf\n" \
"v_add_f32_dpp v50, v50, v50 row_ror:8 row_mask:0xf bank_mask:0xf\n" \
"v_add_f32_dpp v51, v51, v51 row_ror:8 row_mask:0xf bank_mask:0xf\n" \
"v_cvt_pk_bf16_f32 v52, v48, v49\n" \
"v_cvt_pk_bf16_f32 v53, v50, v51\n" \
"v_min_u32 v52, -2, v52\n" \
"v_min_u32 v53, -2, v53\n" \
"ds_write_b64 v7, v[44:45] offset:16\n" \
"buffer_store_dwordx2 v[52:53], v4, s[40:43], s31 offen sc1\n" \
"s_memtime s[22:23]\n" \
"s_cmp_gt_u32 s19, 2000\n" \
"s_cbranch_scc1 R0_off2%=\n" \
"R0_g2%=:\n" \
"v_mov_b64 v[16:17], v[52:53]\n" \
"s_cmp_lt_u32 s70, 63\n" \
"s_cbranch_scc1 R0_top%=\n" \
"buffer_store_dwordx2 v[16:17], v5, s[40:43], s31 offen sc1\n" \
"s_cmp_lt_u32 s70, 63\n" \
"s_cbranch_scc1 R0_top%=\n" \
"s_waitcnt lgkmcnt(0)\n" \
"s_sub_u32 s37, s22, s9\n" \
"s_mul_i32 s37, s37, s11\n" \
"s_ashr_i32 s37, s37, 14\n" \
"s_add_u32 s37, s37, s8\n" \
"s_pack_ll_b32_b16 s75, s30, s37\n" \
"s_min_u32 s75, s75, -2\n" \
"v_mov_b32 v14, s75\n" \
"v_and_b32 v19, 0xbfffffff, v19\n" \
"s_mov_b32 s35, s36\n" \
"s_lshr_b32 s75, s70, 1\n" \
"s_lshl_b32 s75, s75, 14\n" \
"s_and_b32 s36, s70, 1\n" \
"s_lshl_b32 s36, s36, 12\n" \
"s_or_b32 s36, s36, s75\n" \
"s_add_u32 s73, s73, 524288\n" \
"s_and_b32 s73, s73, 2097151\n" \
"s_lshr_b32 s75, s73, 7\n" \
"s_and_b32 s75, s75, 0x1000\n" \
"s_xor_b32 s75, s75, 0x5000\n" \
"v_xor_b32 v2, s75, v2\n" \
"v_xor_b32 v3, s75, v3\n" \
"buffer_store_dword v14, v19, s[40:43], s35 offen sc1\n" \
"s_branch R_done%=\n" \
"R0_slowE%=:\n" \
"s_nop 7\n" \
"s_mov_b32 s83, 0\n" \
"R0_slowE1%=:\n" \
"v_max3_u32 v52, v36, v37, v38\n" \
"v_max3_u32 v52, v52, v39, v40\n" \
"v_max3_u32 v52, v52, v41, v42\n" \
"v_max_u32 v52, v52, v43\n" \
"v_cmp_eq_u32 vcc, -1, v52\n" \
"s_cbranch_vccnz R0_slowE2%=\n" \
"s_cmp_eq_u32 s83, 0\n" \
"s_cbranch_scc1 R0_okE%=\n" \
"ds_write_b128 v0, v[36:39] offset:0\n" \
"ds_write_b128 v0, v[40:43] offset:1024\n" \
"v_mov_b64 v[100:101], 0\n" \
"v_mov_b64 v[102:103], 0\n" \
"v_mov_b64 v[104:105], 0\n" \
"v_mov_b64 v[106:107], 0\n" \
"v_mov_b64 v[108:109], 0\n" \
"v_mov_b64 v[110:111], 0\n" \
"v_mov_b64 v[112:113], 0\n" \
"v_mov_b64 v[114:115], 0\n" \
"v_smfmac_f32_16x16x64_bf16 v[100:103], v[36:39], a[0:7], v6\n" \
"v_smfmac_f32_16x16x64_bf16 v[100:103], v[40:43], a[8:15], v6\n" \
"v_smfmac_f32_16x16x64_bf16 v[108:111], v[36:39], a[128:135], v6\n" \
"v_smfmac_f32_16x16x64_bf16 v[104:107], v[36:39], a[64:71], v6\n" \
"v_smfmac_f32_16x16x64_bf16 v[112:115], v[36:39], a[192:199], v6\n" \
"s_branch R0_okE%=\n" \
"R0_slowE2%=:\n" \
"s_add_u32 s83, s83, 1\n" \
"s_add_u32 s78, s78, 1\n" \
"s_cmp_gt_u32 s83, 65536\n" \
"s_cselect_b32 s77, 1, s77\n" \
"s_cmp_lg_u32 s77, 0\n" \
"s_cbranch_scc1 R0_okE%=\n" \
"s_sleep 2\n" \
"s_mov_b64 exec, vcc\n" \
"buffer_load_dwordx4 v[36:39], v1, s[40:43], s71 offen offset:0 sc1\n" \
"buffer_load_dwordx4 v[40:43], v1, s[40:43], s71 offen offset:1024 sc1\n" \
"s_mov_b64 exec, -1\n" \
"s_waitcnt vmcnt(0)\n" \
"s_branch R0_slowE1%=\n" \
"R0_slowB%=:\n" \
"s_mov_b32 s83, 0\n" \
"R0_slowB1%=:\n" \
"v_max3_u32 v54, v36, v37, v38\n" \
"v_max3_u32 v54, v54, v39, v40\n" \
"v_max3_u32 v54, v54, v41, v42\n" \
"v_max_u32 v54, v54, v43\n" \
"v_cmp_eq_u32 vcc, -1, v54\n" \
"s_cbranch_vccnz R0_slowB2%=\n" \
"s_cmp_eq_u32 s83, 0\n" \
"s_cbranch_scc1 R0_okB%=\n" \
"s_branch R0_redoB%=\n" \
"R0_slowB2%=:\n" \
"s_add_u32 s83, s83, 1\n" \
"s_add_u32 s79, s79, 1\n" \
"s_cmp_gt_u32 s83, 65536\n" \
"s_cselect_b32 s77, 1, s77\n" \
"s_cmp_lg_u32 s77, 0\n" \
"s_cbranch_scc1 R0_okB%=\n" \
"s_cmp_eq_u32 s19, -1\n" \
"s_cselect_b32 s19, -1, -2\n" \
"v_mov_b32 v44, 0\n" \
"s_sleep 2\n" \
"s_mov_b64 exec, vcc\n" \
"buffer_load_dwordx4 v[36:39], v3, s[40:43], s73 offen sc1\n" \
"buffer_load_dwordx4 v[40:43], v3, s[40:43], s73 offen offset:1024 sc1\n" \
"s_mov_b64 exec, -1\n" \
"s_waitcnt vmcnt(0)\n" \
"s_branch R0_slowB1%=\n" \
"R0_resynca%=:\n" \
"s_cmp_gt_i32 s38, 110\n" \
"s_cbranch_scc1 R0_harda%=\n" \
"s_add_u32 s18, s18, s38\n" \
"s_add_u32 s25, s25, s38\n" \
"s_add_u32 s39, s39, 1000\n" \
"s_branch R0_sua%=\n" \
"R0_harda%=:\n" \
"s_mov_b32 s17, 0\n" \
"s_add_u32 s39, s39, 1\n" \
"s_add_u32 s18, s37, s6\n" \
"s_add_u32 s18, s18, s28\n" \
"s_add_u32 s18, s18, s65\n" \
"s_add_u32 s6, s6, s67\n" \
"s_min_u32 s6, s6, 1500\n" \
"s_add_u32 s75, s2, s65\n" \
"s_sub_u32 s75, s75, s24\n" \
"s_mul_i32 s38, s27, 8\n" \
"s_cmp_le_i32 s75, s7\n" \
"s_cselect_b32 s75, s38, 0\n" \
"s_cselect_b32 s38, 0, s38\n" \
"s_add_u32 s65, s65, s75\n" \
"s_add_u32 s66, s66, s38\n" \
"s_mul_i32 s75, s27, 0\n" \
"s_cmp_lt_u32 s47, 320\n" \
"s_cselect_b32 s75, s75, 0\n" \
"s_add_u32 s47, s47, s75\n" \
"s_branch R0_sua%=\n" \
"R0_resyncb%=:\n" \
"s_cmp_gt_i32 s38, 110\n" \
"s_cbranch_scc1 R0_hardb%=\n" \
"s_add_u32 s18, s18, s38\n" \
"s_add_u32 s25, s25, s38\n" \
"s_add_u32 s39, s39, 1000\n" \
"s_branch R0_sub%=\n" \
"R0_hardb%=:\n" \
"s_mov_b32 s17, 0\n" \
"s_add_u32 s39, s39, 1\n" \
"s_add_u32 s18, s37, s6\n" \
"s_add_u32 s18, s18, s28\n" \
"s_add_u32 s18, s18, s65\n" \
"s_add_u32 s6, s6, s67\n" \
"s_min_u32 s6, s6, 1500\n" \
"s_add_u32 s75, s2, s65\n" \
"s_sub_u32 s75, s75, s24\n" \
"s_mul_i32 s38, s27, 8\n" \
"s_cmp_le_i32 s75, s7\n" \
"s_cselect_b32 s75, s38, 0\n" \
"s_cselect_b32 s38, 0, s38\n" \
"s_add_u32 s65, s65, s75\n" \
"s_add_u32 s66, s66, s38\n" \
"s_mul_i32 s75, s27, 0\n" \
"s_cmp_lt_u32 s47, 320\n" \
"s_cselect_b32 s75, s75, 0\n" \
"s_add_u32 s47, s47, s75\n" \
"s_branch R0_sub%=\n" \
"R0_off1%=:\n" \
"s_mov_b32 s16, s20\n" \
"s_mov_b32 s30, s18\n" \
"s_cmp_gt_i32 s75, 0\n" \
"s_cbranch_scc1 R0_off1b%=\n" \
"R0_tw1%=:\n" \
"s_waitcnt vmcnt(0)\n" \
"v_max_u32 v49, v16, v17\n" \
"v_cmp_eq_u32 vcc, -1, v49\n" \
"s_cbranch_vccz R0_tw1x%=\n" \
"s_add_u32 s82, s82, 1\n" \
"s_cmp_gt_u32 s82, 65536\n" \
"s_cselect_b32 s77, 1, s77\n" \
"s_cmp_lg_u32 s77, 0\n" \
"s_cbranch_scc1 R0_tw1x%=\n" \
"s_sleep 6\n" \
"s_mov_b64 exec, vcc\n" \
"buffer_load_dword v16, v18, s[40:43], s35 offen sc1\n" \
"buffer_load_dword v17, v18, s[40:43], s35 offen offset:2048 sc1\n" \
"s_mov_b64 exec, -1\n" \
"s_branch R0_tw1%=\n" \
"R0_tw1x%=:\n" \
"s_lshl_b32 s37, s8, 16\n" \
"s_waitcnt vmcnt(1)\n" \
"v_subrev_u32 v68, s8, v16\n" \
"s_waitcnt vmcnt(0)\n" \
"v_subrev_u32 v69, s8, v17\n" \
"v_subrev_u32 v70, s37, v16\n" \
"v_subrev_u32 v71, s37, v17\n" \
"v_bfe_i32 v68, v68, 0, 16\n" \
"v_bfe_i32 v69, v69, 0, 16\n" \
"v_max_i32 v70, v70, v71\n" \
"v_max_i32 v68, v68, v69\n" \
"s_nop 0\n" \
"v_max_i32_dpp v70, v70, v70 quad_perm:[1,0,3,2] row_mask:0xf bank_mask:0xf\n" \
"v_max_i32_dpp v68, v68, v68 quad_perm:[1,0,3,2] row_mask:0xf bank_mask:0xf\n" \
"s_nop 0\n" \
"v_max_i32_dpp v70, v70, v70 quad_perm:[2,3,0,1] row_mask:0xf bank_mask:0xf\n" \
"v_max_i32_dpp v68, v68, v68 quad_perm:[2,3,0,1] row_mask:0xf bank_mask:0xf\n" \
"s_nop 0\n" \
"v_max_i32_dpp v70, v70, v70 row_half_mirror row_mask:0xf bank_mask:0xf\n" \
"v_max_i32_dpp v68, v68, v68 row_half_mirror row_mask:0xf bank_mask:0xf\n" \
"s_nop 0\n" \
"v_max_i32_dpp v70, v70, v70 row_mirror row_mask:0xf bank_mask:0xf\n" \
"v_max_i32_dpp v68, v68, v68 row_mirror row_mask:0xf bank_mask:0xf\n" \
"s_nop 0\n" \
"v_max_i32_dpp v70, v70, v70 row_bcast:15 row_mask:0xa bank_mask:0xf\n" \
"v_max_i32_dpp v68, v68, v68 row_bcast:15 row_mask:0xa bank_mask:0xf\n" \
"s_nop 0\n" \
"v_max_i32_dpp v70, v70, v70 row_bcast:31 row_mask:0xc bank_mask:0xf\n" \
"v_max_i32_dpp v68, v68, v68 row_bcast:31 row_mask:0xc bank_mask:0xf\n" \
"v_readlane_b32 s3, v70, 63\n" \
"v_readlane_b32 s2, v68, 63\n" \
"s_ashr_i32 s3, s3, 16\n" \
"s_add_u32 s3, s3, s8\n" \
"s_add_u32 s2, s2, s8\n" \
"s_add_u32 s37, s3, s66\n" \
"s_sub_u32 s38, s37, s25\n" \
"s_cmp_le_i32 s38, s7\n" \
"s_cbranch_scc1 R0_off1c%=\n" \
"s_add_u32 s30, s18, s38\n" \
"s_cmp_le_i32 s38, 110\n" \
"s_cbranch_scc1 R0_off1b%=\n" \
"s_add_u32 s30, s37, s6\n" \
"s_add_u32 s30, s30, s28\n" \
"s_add_u32 s30, s30, s65\n" \
"R0_off1b%=:\n" \
"s_mov_b32 s83, 0\n" \
"s_branch R0_ap1c%=\n" \
"R0_ap1%=:\n" \
"s_memtime s[84:85]\n" \
"s_memrealtime s[48:49]\n" \
"v_mov_b32 v7, v7\n" \
"s_nop 0\n" \
"s_memrealtime s[50:51]\n" \
"v_mov_b32 v7, v7\n" \
"s_nop 0\n" \
"s_memrealtime s[52:53]\n" \
"v_mov_b32 v7, v7\n" \
"s_nop 0\n" \
"s_memrealtime s[54:55]\n" \
"v_mov_b32 v7, v7\n" \
"s_memrealtime s[56:57]\n" \
"v_mov_b32 v7, v7\n" \
"s_nop 0\n" \
"s_memrealtime s[58:59]\n" \
"v_mov_b32 v7, v7\n" \
"s_nop 0\n" \
"s_memrealtime s[60:61]\n" \
"v_mov_b32 v7, v7\n" \
"s_nop 0\n" \
"s_memrealtime s[62:63]\n" \
"v_mov_b32 v7, v7\n" \
"s_nop 0\n" \
"v_mov_b32 v7, v7\n" \
"s_nop 0\n" \
"v_mov_b32 v7, v7\n" \
"s_nop 0\n" \
"v_mov_b32 v7, v7\n" \
"s_nop 0\n" \
"s_waitcnt lgkmcnt(0)\n" \
"s_add_u32 s8, s48, s50\n" \
"s_add_u32 s8, s8, s52\n" \
"s_add_u32 s8, s8, s54\n" \
"s_add_u32 s8, s8, s56\n" \
"s_add_u32 s8, s8, s58\n" \
"s_add_u32 s8, s8, s60\n" \
"s_add_u32 s8, s8, s62\n" \
"s_mov_b32 s9, s84\n" \
"R0_ap1c%=:\n" \
"s_sub_u32 s16, s30, s8\n" \
"s_mul_i32 s16, s16, s10\n" \
"s_ashr_i32 s75, s16, 8\n" \
"s_add_u32 s16, s75, s9\n" \
"s_add_u32 s83, s83, 1\n" \
"s_cmp_gt_u32 s83, 80\n" \
"s_cselect_b32 s75, 0, s75\n" \
"s_cmp_gt_i32 s75, 900\n" \
"s_cbranch_scc1 R0_ap1%=\n" \
"R0_off1c%=:\n" \
"s_sub_u32 s16, s16, s20\n" \
"s_max_i32 s16, s16, s12\n" \
"s_min_i32 s16, s16, 16384\n" \
"s_add_u32 s16, s16, s20\n" \
"s_branch R0_w1t%=\n" \
"R0_off2%=:\n" \
"s_waitcnt lgkmcnt(0)\n" \
"s_mov_b32 s16, s22\n" \
"s_cmp_lg_u32 s19, -1\n" \
"s_cbranch_scc1 R0_off2b%=\n" \
"R0_tw2%=:\n" \
"s_waitcnt vmcnt(0)\n" \
"v_max_u32 v49, v16, v17\n" \
"v_cmp_eq_u32 vcc, -1, v49\n" \
"s_cbranch_vccz R0_tw2x%=\n" \
"s_add_u32 s82, s82, 1\n" \
"s_cmp_gt_u32 s82, 65536\n" \
"s_cselect_b32 s77, 1, s77\n" \
"s_cmp_lg_u32 s77, 0\n" \
"s_cbranch_scc1 R0_tw2x%=\n" \
"s_sleep 6\n" \
"s_mov_b64 exec, vcc\n" \
"buffer_load_dword v16, v18, s[40:43], s35 offen sc1\n" \
"buffer_load_dword v17, v18, s[40:43], s35 offen offset:2048 sc1\n" \
"s_mov_b64 exec, -1\n" \
"s_branch R0_tw2%=\n" \
"R0_tw2x%=:\n" \
"s_lshl_b32 s37, s8, 16\n" \
"s_waitcnt vmcnt(1)\n" \
"v_subrev_u32 v68, s8, v16\n" \
"s_waitcnt vmcnt(0)\n" \
"v_subrev_u32 v69, s8, v17\n" \
"v_subrev_u32 v70, s37, v16\n" \
"v_subrev_u32 v71, s37, v17\n" \
"v_bfe_i32 v68, v68, 0, 16\n" \
"v_bfe_i32 v69, v69, 0, 16\n" \
"v_max_i32 v70, v70, v71\n" \
"v_max_i32 v68, v68, v69\n" \
"s_nop 0\n" \
"v_max_i32_dpp v70, v70, v70 quad_perm:[1,0,3,2] row_mask:0xf bank_mask:0xf\n" \
"v_max_i32_dpp v68, v68, v68 quad_perm:[1,0,3,2] row_mask:0xf bank_mask:0xf\n" \
"s_nop 0\n" \
"v_max_i32_dpp v70, v70, v70 quad_perm:[2,3,0,1] row_mask:0xf bank_mask:0xf\n" \
"v_max_i32_dpp v68, v68, v68 quad_perm:[2,3,0,1] row_mask:0xf bank_mask:0xf\n" \
"s_nop 0\n" \
"v_max_i32_dpp v70, v70, v70 row_half_mirror row_mask:0xf bank_mask:0xf\n" \
"v_max_i32_dpp v68, v68, v68 row_half_mirror row_mask:0xf bank_mask:0xf\n" \
"s_nop 0\n" \
"v_max_i32_dpp v70, v70, v70 row_mirror row_mask:0xf bank_mask:0xf\n" \
"v_max_i32_dpp v68, v68, v68 row_mirror row_mask:0xf bank_mask:0xf\n" \
"s_nop 0\n" \
"v_max_i32_dpp v70, v70, v70 row_bcast:15 row_mask:0xa bank_mask:0xf\n" \
"v_max_i32_dpp v68, v68, v68 row_bcast:15 row_mask:0xa bank_mask:0xf\n" \
"s_nop 0\n" \
"v_max_i32_dpp v70, v70, v70 row_bcast:31 row_mask:0xc bank_mask:0xf\n" \
"v_max_i32_dpp v68, v68, v68 row_bcast:31 row_mask:0xc bank_mask:0xf\n" \
"v_readlane_b32 s3, v70, 63\n" \
"v_readlane_b32 s2, v68, 63\n" \
"s_ashr_i32 s3, s3, 16\n" \
"s_add_u32 s3, s3, s8\n" \
"s_add_u32 s2, s2, s8\n" \
"s_add_u32 s37, s3, s66\n" \
"s_sub_u32 s38, s37, s25\n" \
"s_cmp_le_i32 s38, s7\n" \
"s_mov_b32 s17, 1\n" \
"s_cbranch_scc0 R0_resyncb%=\n" \
"s_mov_b32 s6, s67\n" \
"R0_sub%=:\n" \
"s_mov_b32 s5, s25\n" \
"s_add_u32 s25, s18, s29\n" \
"s_add_u32 s25, s25, s66\n" \
"s_lshr_b32 s75, s47, 2\n" \
"s_add_u32 s25, s25, s75\n" \
"R0_off2b%=:\n" \
"s_mov_b32 s83, 0\n" \
"s_branch R0_ap2c%=\n" \
"R0_ap2%=:\n" \
"s_memtime s[84:85]\n" \
"s_memrealtime s[48:49]\n" \
"v_mov_b32 v7, v7\n" \
"s_nop 0\n" \
"s_memrealtime s[50:51]\n" \
"v_mov_b32 v7, v7\n" \
"s_nop 0\n" \
"s_memrealtime s[52:53]\n" \
"v_mov_b32 v7, v7\n" \
"s_nop 0\n" \
"s_memrealtime s[54:55]\n" \
"v_mov_b32 v7, v7\n" \
"s_memrealtime s[56:57]\n" \
"v_mov_b32 v7, v7\n" \
"s_nop 0\n" \
"s_memrealtime s[58:59]\n" \
"v_mov_b32 v7, v7\n" \
"s_nop 0\n" \
"s_memrealtime s[60:61]\n" \
"v_mov_b32 v7, v7\n" \
"s_nop 0\n" \
"s_memrealtime s[62:63]\n" \
"v_mov_b32 v7, v7\n" \
"s_nop 0\n" \
"v_mov_b32 v7, v7\n" \
"s_nop 0\n" \
"v_mov_b32 v7, v7\n" \
"s_nop 0\n" \
"v_mov_b32 v7, v7\n" \
"s_nop 0\n" \
"s_waitcnt lgkmcnt(0)\n" \
"s_add_u32 s8, s48, s50\n" \
"s_add_u32 s8, s8, s52\n" \
"s_add_u32 s8, s8, s54\n" \
"s_add_u32 s8, s8, s56\n" \
"s_add_u32 s8, s8, s58\n" \
"s_add_u32 s8, s8, s60\n" \
"s_add_u32 s8, s8, s62\n" \
"s_mov_b32 s9, s84\n" \
"R0_ap2c%=:\n" \
"s_sub_u32 s34, s25, s8\n" \
"s_mul_i32 s34, s34, s10\n" \
"s_ashr_i32 s75, s34, 8\n" \
"s_add_u32 s34, s75, s9\n" \
"s_add_u32 s83, s83, 1\n" \
"s_cmp_gt_u32 s83, 80\n" \
"s_cselect_b32 s75, 0, s75\n" \
"s_cmp_gt_i32 s75, 1200\n" \
"s_cbranch_scc1 R0_ap2%=\n" \
"s_mul_i32 s38, s67, 2\n" \
"s_sub_u32 s75, s34, s16\n" \
"s_cmp_ge_i32 s75, s13\n" \
"s_cselect_b32 s38, 0, s38\n" \
"s_sub_u32 s34, s34, s16\n" \
"s_max_i32 s34, s34, s38\n" \
"s_min_i32 s34, s34, 16384\n" \
"s_add_u32 s34, s34, s16\n" \
"v_mov_b32 v44, s70\n" \
"v_mov_b32 v45, s34\n" \
"ds_write_b64 v7, v[44:45] offset:16\n" \
"s_waitcnt lgkmcnt(0)\n" \
"s_mov_b32 s22, s16\n" \
"s_branch R0_g2%=\n" \
"R1_top%=:\n" \
"R1_w2%=:\n" \
"s_memtime s[14:15]\n" \
"s_getpc_b64 s[90:91]\n" \
"R1_w2pc%=:\n" \
"s_add_u32 s90, s90, R1_w2x%=-R1_w2pc%=\n" \
"s_addc_u32 s91, s91, 0\n" \
"v_mov_b32 v7, v7\n" \
"s_nop 0\n" \
"v_mov_b32 v7, v7\n" \
"s_nop 0\n" \
"v_mov_b32 v7, v7\n" \
"s_nop 0\n" \
"s_waitcnt lgkmcnt(0)\n" \
"s_sub_u32 s75, s34, s14\n" \
"v_mov_b32 v7, v7\n" \
"s_cmp_gt_i32 s75, 384\n" \
"s_cbranch_scc1 R1_w2%=\n" \
"s_max_i32 s75, s75, 0\n" \
"s_and_b32 s75, s75, -4\n" \
"s_sub_u32 s90, s90, s75\n" \
"s_subb_u32 s91, s91, 0\n" \
"v_mov_b32 v7, v7\n" \
"s_setpc_b64 s[90:91]\n" \
"v_mov_b32_e32 v7, v7\n" \
"v_mov_b32_e32 v7, v7\n" \
"v_mov_b32_e32 v7, v7\n" \
"v_mov_b32_e32 v7, v7\n" \
"v_mov_b32_e32 v7, v7\n" \
"v_mov_b32_e32 v7, v7\n" \
"v_mov_b32_e32 v7, v7\n" \
"v_mov_b32_e32 v7, v7\n" \
"v_mov_b32_e32 v7, v7\n" \
"v_mov_b32_e32 v7, v7\n" \
"v_mov_b32_e32 v7, v7\n" \
"v_mov_b32_e32 v7, v7\n" \
"v_mov_b32_e32 v7, v7\n" \
"v_mov_b32_e32 v7, v7\n" \
"v_mov_b32_e32 v7, v7\n" \
"v_mov_b32_e32 v7, v7\n" \
"v_mov_b32_e32 v7, v7\n" \
"v_mov_b32_e32 v7, v7\n" \
"v_mov_b32_e32 v7, v7\n" \
"v_mov_b32_e32 v7, v7\n" \
"v_mov_b32_e32 v7, v7\n" \
"v_mov_b32_e32 v7, v7\n" \
"v_mov_b32_e32 v7, v7\n" \
"v_mov_b32_e32 v7, v7\n" \
"v_mov_b32_e32 v7, v7\n" \
"v_mov_b32_e32 v7, v7\n" \
"v_mov_b32_e32 v7, v7\n" \
"v_mov_b32_e32 v7, v7\n" \
"v_mov_b32_e32 v7, v7\n" \
"v_mov_b32_e32 v7, v7\n" \
"v_mov_b32_e32 v7, v7\n" \
"v_mov_b32_e32 v7, v7\n" \
"v_mov_b32_e32 v7, v7\n" \
"v_mov_b32_e32 v7, v7\n" \
"v_mov_b32_e32 v7, v7\n" \
"v_mov_b32_e32 v7, v7\n" \
"v_mov_b32_e32 v7, v7\n" \
"v_mov_b32_e32 v7, v7\n" \
"v_mov_b32_e32 v7, v7\n" \
"v_mov_b32_e32 v7, v7\n" \
"v_mov_b32_e32 v7, v7\n" \
"v_mov_b32_e32 v7, v7\n" \
"v_mov_b32_e32 v7, v7\n" \
"v_mov_b32_e32 v7, v7\n" \
"v_mov_b32_e32 v7, v7\n" \
"v_mov_b32_e32 v7, v7\n" \
"v_mov_b32_e32 v7, v7\n" \
"v_mov_b32_e32 v7, v7\n" \
"v_mov_b32_e32 v7, v7\n" \
"v_mov_b32_e32 v7, v7\n" \
"v_mov_b32_e32 v7, v7\n" \
"v_mov_b32_e32 v7, v7\n" \
"v_mov_b32_e32 v7, v7\n" \
"v_mov_b32_e32 v7, v7\n" \
"v_mov_b32_e32 v7, v7\n" \
"v_mov_b32_e32 v7, v7\n" \
"v_mov_b32_e32 v7, v7\n" \
"v_mov_b32_e32 v7, v7\n" \
"v_mov_b32_e32 v7, v7\n" \
"v_mov_b32_e32 v7, v7\n" \
"v_mov_b32_e32 v7, v7\n" \
"v_mov_b32_e32 v7, v7\n" \
"v_mov_b32_e32 v7, v7\n" \
"v_mov_b32_e32 v7, v7\n" \
"v_mov_b32_e32 v7, v7\n" \
"v_mov_b32_e32 v7, v7\n" \
"v_mov_b32_e32 v7, v7\n" \
"v_mov_b32_e32 v7, v7\n" \
"v_mov_b32_e32 v7, v7\n" \
"v_mov_b32_e32 v7, v7\n" \
"v_mov_b32_e32 v7, v7\n" \
"v_mov_b32_e32 v7, v7\n" \
"v_mov_b32_e32 v7, v7\n" \
"v_mov_b32_e32 v7, v7\n" \
"v_mov_b32_e32 v7, v7\n" \
"v_mov_b32_e32 v7, v7\n" \
"v_mov_b32_e32 v7, v7\n" \
"v_mov_b32_e32 v7, v7\n" \
"v_mov_b32_e32 v7, v7\n" \
"v_mov_b32_e32 v7, v7\n" \
"v_mov_b32_e32 v7, v7\n" \
"v_mov_b32_e32 v7, v7\n" \
"v_mov_b32_e32 v7, v7\n" \
"v_mov_b32_e32 v7, v7\n" \
"v_mov_b32_e32 v7, v7\n" \
"v_mov_b32_e32 v7, v7\n" \
"v_mov_b32_e32 v7, v7\n" \
"v_mov_b32_e32 v7, v7\n" \
"v_mov_b32_e32 v7, v7\n" \
"v_mov_b32_e32 v7, v7\n" \
"v_mov_b32_e32 v7, v7\n" \
"v_mov_b32_e32 v7, v7\n" \
"v_mov_b32_e32 v7, v7\n" \
"v_mov_b32_e32 v7, v7\n" \
"v_mov_b32_e32 v7, v7\n" \
"v_mov_b32_e32 v7, v7\n" \
"R1_w2x%=:\n" \
"R1_loop%=:\n" \
"buffer_load_dwordx4 v[36:39], v1, s[40:43], s71 offen offset:0 sc1\n" \
"buffer_load_dwordx4 v[40:43], v1, s[40:43], s71 offen offset:1024 sc1\n" \
"s_waitcnt vmcnt(2)\n" \
"ds_write_b128 v4, v[44:47] offset:0\n" \
"ds_write_b128 v4, v[48:51] offset:1024\n" \
"ds_write_b128 v4, v[52:55] offset:2048\n" \
"ds_write_b128 v4, v[56:59] offset:3072\n" \
"ds_write_b128 v4, v[60:63] offset:4096\n" \
"ds_write_b128 v4, v[64:67] offset:5120\n" \
"ds_write_b128 v4, v[68:71] offset:6144\n" \
"ds_write_b128 v4, v[72:75] offset:7168\n" \
"v_xor_b32 v4, 16384, v4\n" \
"s_cmp_lt_u32 s70, 1\n" \
"s_cselect_b32 s37, 0, s74\n" \
"s_add_u32 s76, s37, 8192\n" \
"v_mov_b64 v[100:101], 0\n" \
"v_mov_b64 v[102:103], 0\n" \
"v_mov_b64 v[104:105], 0\n" \
"v_mov_b64 v[106:107], 0\n" \
"v_mov_b64 v[108:109], 0\n" \
"v_mov_b64 v[110:111], 0\n" \
"v_mov_b64 v[112:113], 0\n" \
"v_mov_b64 v[114:115], 0\n" \
"v_mov_b64 v[116:117], 0\n" \
"v_mov_b64 v[118:119], 0\n" \
"v_mov_b64 v[120:121], 0\n" \
"v_mov_b64 v[122:123], 0\n" \
"s_waitcnt vmcnt(1)\n" \
"ds_write_b128 v0, v[36:39] offset:2048\n" \
"s_waitcnt vmcnt(0)\n" \
"ds_write_b128 v0, v[40:43] offset:3072\n" \
"v_smfmac_f32_16x16x64_bf16 v[100:103], v[36:39], a[16:23], v6\n" \
"v_smfmac_f32_16x16x64_bf16 v[100:103], v[40:43], a[24:31], v6\n" \
"v_smfmac_f32_16x16x64_bf16 v[108:111], v[36:39], a[144:151], v6\n" \
"v_smfmac_f32_16x16x64_bf16 v[104:107], v[36:39], a[80:87], v6\n" \
"v_cmp_u_f32 vcc, v100, v101\n" \
"v_cmp_u_f32 s[84:85], v102, v103\n" \
"v_smfmac_f32_16x16x64_bf16 v[112:115], v[36:39], a[208:215], v6\n" \
"s_or_b64 vcc, vcc, s[84:85]\n" \
"s_cbranch_vccnz R1_slowE%=\n" \
"R1_okE%=:\n" \
"s_waitcnt lgkmcnt(0)\n" \
"s_barrier \n" \
"ds_read_b128 v[44:47], v0 offset:4096\n" \
"ds_read_b128 v[48:51], v0 offset:5120\n" \
"v_smfmac_f32_16x16x64_bf16 v[108:111], v[40:43], a[152:159], v6\n" \
"ds_read_b128 v[52:55], v0 offset:6144\n" \
"ds_read_b128 v[56:59], v0 offset:7168\n" \
"v_smfmac_f32_16x16x64_bf16 v[104:107], v[40:43], a[88:95], v6\n" \
"ds_read_b128 v[60:63], v0 offset:0\n" \
"ds_read_b128 v[64:67], v0 offset:1024\n" \
"v_smfmac_f32_16x16x64_bf16 v[112:115], v[40:43], a[216:223], v6\n" \
"v_smfmac_f32_16x16x64_bf16 v[116:119], v[36:39], v[144:151], v6\n" \
"v_smfmac_f32_16x16x64_bf16 v[120:123], v[36:39], v[208:215], v6\n" \
"s_waitcnt lgkmcnt(5)\n" \
"v_smfmac_f32_16x16x64_bf16 v[100:103], v[44:47], a[32:39], v6\n" \
"ds_read_b128 v[68:71], v10 offset:0\n" \
"v_smfmac_f32_16x16x64_bf16 v[108:111], v[44:47], a[160:167], v6\n" \
"ds_read_b128 v[72:75], v10 offset:4096\n" \
"v_smfmac_f32_16x16x64_bf16 v[104:107], v[44:47], a[96:103], v6\n" \
"ds_read_b128 v[76:79], v10 offset:1024\n" \
"v_smfmac_f32_16x16x64_bf16 v[112:115], v[44:47], a[224:231], v6\n" \
"ds_read_b128 v[80:83], v10 offset:5120\n" \
"s_waitcnt lgkmcnt(8)\n" \
"v_smfmac_f32_16x16x64_bf16 v[100:103], v[48:51], a[40:47], v6\n" \
"ds_read_b128 v[84:87], v10 offset:2048\n" \
"v_smfmac_f32_16x16x64_bf16 v[108:111], v[48:51], a[168:175], v6\n" \
"ds_read_b128 v[88:91], v10 offset:6144\n" \
"v_smfmac_f32_16x16x64_bf16 v[104:107], v[48:51], a[104:111], v6\n" \
"ds_read_b128 v[92:95], v10 offset:3072\n" \
"v_smfmac_f32_16x16x64_bf16 v[112:115], v[48:51], a[232:239], v6\n" \
"ds_read_b128 v[96:99], v10 offset:7168\n" \
"s_waitcnt lgkmcnt(11)\n" \
"v_smfmac_f32_16x16x64_bf16 v[100:103], v[52:55], a[48:55], v6\n" \
"ds_read_b64 v[20:21], v11 offset:0\n" \
"v_smfmac_f32_16x16x64_bf16 v[108:111], v[52:55], a[176:183], v6\n" \
"ds_read_b64 v[24:25], v11 offset:8\n" \
"v_smfmac_f32_16x16x64_bf16 v[104:107], v[52:55], a[112:119], v6\n" \
"ds_read_b64 v[28:29], v11 offset:1024\n" \
"v_smfmac_f32_16x16x64_bf16 v[112:115], v[52:55], a[240:247], v6\n" \
"ds_read_b64 v[32:33], v11 offset:1032\n" \
"s_waitcnt lgkmcnt(14)\n" \
"v_smfmac_f32_16x16x64_bf16 v[100:103], v[56:59], a[56:63], v6\n" \
"v_xor_b32 v10, 16384, v10\n" \
"v_smfmac_f32_16x16x64_bf16 v[108:111], v[56:59], a[184:191], v6\n" \
"s_mov_b32 vcc_lo, 0x33333333\n" \
"v_smfmac_f32_16x16x64_bf16 v[104:107], v[56:59], a[120:127], v6\n" \
"s_mov_b32 vcc_hi, 0x33333333\n" \
"v_smfmac_f32_16x16x64_bf16 v[112:115], v[56:59], a[248:255], v6\n" \
"buffer_load_dword v17, v3, s[44:47], s37 offen sc1\n" \
"s_waitcnt lgkmcnt(13)\n" \
"v_smfmac_f32_16x16x64_bf16 v[100:103], v[60:63], a[0:7], v6\n" \
"buffer_load_dword v18, v3, s[44:47], s76 offen sc1\n" \
"v_smfmac_f32_16x16x64_bf16 v[108:111], v[60:63], a[128:135], v6\n" \
"buffer_load_dword v16, v14, s[44:47], s31 offen sc1\n" \
"v_smfmac_f32_16x16x64_bf16 v[104:107], v[60:63], a[64:71], v6\n" \
"v_smfmac_f32_16x16x64_bf16 v[112:115], v[60:63], a[192:199], v6\n" \
"s_waitcnt lgkmcnt(12)\n" \
"v_smfmac_f32_16x16x64_bf16 v[100:103], v[64:67], a[8:15], v6\n" \
"v_smfmac_f32_16x16x64_bf16 v[108:111], v[64:67], a[136:143], v6\n" \
"v_smfmac_f32_16x16x64_bf16 v[104:107], v[64:67], a[72:79], v6\n" \
"v_smfmac_f32_16x16x64_bf16 v[112:115], v[64:67], a[200:207], v6\n" \
"v_add_f32 v100, v100, v102\n" \
"v_add_f32 v101, v101, v103\n" \
"v_cvt_pk_bf16_f32 v124, v100, v101\n" \
"v_add_f32 v108, v108, v110\n" \
"v_add_f32 v109, v109, v111\n" \
"v_cvt_pk_bf16_f32 v125, v108, v109\n" \
"v_pk_add_f32 v[104:105], v[104:105], v[106:107]\n" \
"v_cvt_pk_bf16_f32 v126, v104, v105\n" \
"v_pk_add_f32 v[112:113], v[112:113], v[114:115]\n" \
"v_cvt_pk_bf16_f32 v127, v112, v113\n" \
"v_smfmac_f32_16x16x64_bf16 v[116:119], v[40:43], v[152:159], v6\n" \
"ds_write_b128 v8, v[124:127]\n" \
"v_smfmac_f32_16x16x64_bf16 v[120:123], v[40:43], v[216:223], v6\n" \
"v_smfmac_f32_16x16x64_bf16 v[116:119], v[44:47], v[160:167], v6\n" \
"v_smfmac_f32_16x16x64_bf16 v[120:123], v[44:47], v[224:231], v6\n" \
"v_smfmac_f32_16x16x64_bf16 v[116:119], v[48:51], v[168:175], v6\n" \
"s_waitcnt lgkmcnt(0)\n" \
"s_barrier \n" \
"ds_read_b64_tr_b16 v[36:37], v9 offset:0\n" \
"ds_read_b64_tr_b16 v[38:39], v9 offset:64\n" \
"v_smfmac_f32_16x16x64_bf16 v[120:123], v[48:51], v[232:239], v6\n" \
"ds_read_b64_tr_b16 v[40:41], v9 offset:256\n" \
"ds_read_b64_tr_b16 v[42:43], v9 offset:320\n" \
"v_smfmac_f32_16x16x64_bf16 v[116:119], v[52:55], v[176:183], v6\n" \
"ds_read_b64_tr_b16 v[124:125], v9 offset:512\n" \
"ds_read_b64_tr_b16 v[126:127], v9 offset:576\n" \
"v_smfmac_f32_16x16x64_bf16 v[120:123], v[52:55], v[240:247], v6\n" \
"ds_read_b64_tr_b16 v[112:113], v9 offset:768\n" \
"ds_read_b64_tr_b16 v[114:115], v9 offset:832\n" \
"v_smfmac_f32_16x16x64_bf16 v[116:119], v[56:59], v[184:191], v6\n" \
"s_waitcnt lgkmcnt(6)\n" \
"v_mfma_f32_16x16x32_bf16 v[100:103], v[68:71], v[36:39], 0\n" \
"v_mfma_f32_16x16x32_bf16 v[104:107], v[72:75], v[36:39], 0\n" \
"v_mov_b32 v68, 0x3f803f80\n" \
"v_mov_b32 v69, 0x3f803f80\n" \
"v_mfma_f32_16x16x32_bf16 v[108:111], v[36:39], v[36:39], 0\n" \
"v_mov_b32 v70, 0x3f80\n" \
"v_mov_b32 v71, 0x3f80\n" \
"s_waitcnt lgkmcnt(4)\n" \
"v_mfma_f32_16x16x32_bf16 v[100:103], v[76:79], v[40:43], v[100:103]\n" \
"v_mov_b32 v72, 0x3f803f80\n" \
"v_mov_b32 v73, 0x3f803f80\n" \
"v_mfma_f32_16x16x32_bf16 v[104:107], v[80:83], v[40:43], v[104:107]\n" \
"v_mov_b32 v74, 0x3f80\n" \
"v_mov_b32 v75, 0x3f80\n" \
"v_mfma_f32_16x16x32_bf16 v[108:111], v[40:43], v[40:43], v[108:111]\n" \
"v_mov_b64 v[76:77], 0\n" \
"v_mov_b64 v[78:79], 0\n" \
"s_waitcnt lgkmcnt(2)\n" \
"v_mfma_f32_16x16x32_bf16 v[100:103], v[84:87], v[124:127], v[100:103]\n" \
"v_mov_b64 v[80:81], 0\n" \
"v_mov_b64 v[82:83], 0\n" \
"v_mfma_f32_16x16x32_bf16 v[104:107], v[88:91], v[124:127], v[104:107]\n" \
"v_mov_b64 v[84:85], 0\n" \
"v_mov_b64 v[86:87], 0\n" \
"v_mfma_f32_16x16x32_bf16 v[108:111], v[124:127], v[124:127], v[108:111]\n" \
"s_waitcnt lgkmcnt(0)\n" \
"v_mfma_f32_16x16x32_bf16 v[100:103], v[92:95], v[112:115], v[100:103]\n" \
"v_mfma_f32_16x16x32_bf16 v[104:107], v[96:99], v[112:115], v[104:107]\n" \
"v_mfma_f32_16x16x32_bf16 v[108:111], v[112:115], v[112:115], v[108:111]\n" \
"v_cndmask_b32_dpp v100, v102, v100, vcc quad_perm:[0,1,0,1] row_mask:0xf bank_mask:0xf\n" \
"v_cndmask_b32_dpp v101, v103, v101, vcc quad_perm:[0,1,0,1] row_mask:0xf bank_mask:0xf\n" \
"v_smfmac_f32_16x16x64_bf16 v[120:123], v[56:59], v[248:255], v6\n" \
"v_cndmask_b32_dpp v104, v106, v104, vcc quad_perm:[0,1,0,1] row_mask:0xf bank_mask:0xf\n" \
"v_cndmask_b32_dpp v105, v107, v105, vcc quad_perm:[0,1,0,1] row_mask:0xf bank_mask:0xf\n" \
"v_max3_f32 v92, v100, v101, v104\n" \
"s_nop 0\n" \
"v_cndmask_b32_dpp v110, v111, v110, vcc quad_perm:[0,1,0,1] row_mask:0xf bank_mask:0xf\n" \
"v_max3_f32 v92, v92, v105, v110\n" \
"v_smfmac_f32_16x16x64_bf16 v[116:119], v[60:63], v[128:135], v6\n" \
"s_nop 0\n" \
"v_max_f32_dpp v93, v92, v92 quad_perm:[2,3,0,1] row_mask:0xf bank_mask:0xf\n" \
"v_max_f32_dpp v92, v92, v92 quad_perm:[2,3,0,1] row_mask:0xf bank_mask:0xf\n" \
"v_smfmac_f32_16x16x64_bf16 v[120:123], v[60:63], v[192:199], v6\n" \
"s_nop 0\n" \
"v_permlane16_swap_b32 v92, v93\n" \
"v_max_f32 v94, v92, v93\n" \
"v_max_f32 v92, v92, v93\n" \
"v_smfmac_f32_16x16x64_bf16 v[116:119], v[64:67], v[136:143], v6\n" \
"s_nop 0\n" \
"v_permlane32_swap_b32 v92, v94\n" \
"v_max_f32 v92, v92, v94\n" \
"v_mul_f32 v94, s81, v92\n" \
"v_fma_f32 v110, v110, s80, v94\n" \
"v_pk_fma_f32 v[100:101], v[100:101], s[80:81], v[94:95] op_sel_hi:[1,0,0]\n" \
"v_pk_fma_f32 v[104:105], v[104:105], s[80:81], v[94:95] op_sel_hi:[1,0,0]\n" \
"v_smfmac_f32_16x16x64_bf16 v[120:123], v[64:67], v[200:207], v6\n" \
"v_exp_f32 v110, v110\n" \
"v_exp_f32 v100, v100\n" \
"v_exp_f32 v101, v101\n" \
"v_exp_f32 v104, v104\n" \
"v_exp_f32 v105, v105\n" \
"v_cvt_pk_bf16_f32 v91, v110, v110\n" \
"v_cvt_pk_bf16_f32 v88, v100, v101\n" \
"v_and_b32 v89, v12, v91\n" \
"v_and_b32 v91, v13, v91\n" \
"v_cvt_pk_bf16_f32 v90, v104, v105\n" \
"v_cvt_pk_bf16_f32 v22, v116, v118\n" \
"v_cvt_pk_bf16_f32 v23, v117, v119\n" \
"v_smfmac_f32_16x16x64_bf16 v[76:79], v[88:91], v[68:75], v6\n" \
"v_mov_b64 v[26:27], v[22:23]\n" \
"v_cvt_pk_bf16_f32 v30, v120, v122\n" \
"v_cvt_pk_bf16_f32 v31, v121, v123\n" \
"v_smfmac_f32_16x16x64_bf16 v[80:83], v[88:91], v[20:27], v6\n" \
"v_mov_b64 v[34:35], v[30:31]\n" \
"s_nop 1\n" \
"v_smfmac_f32_16x16x64_bf16 v[84:87], v[88:91], v[28:35], v6\n" \
"v_pk_add_f32 v[76:77], v[76:77], v[78:79]\n" \
"v_rcp_f32 v76, v76\n" \
"v_rcp_f32 v77, v77\n" \
"s_nop 0\n" \
"v_pk_add_f32 v[80:81], v[80:81], v[82:83]\n" \
"v_pk_mul_f32 v[80:81], v[80:81], v[76:77]\n" \
"v_cvt_pk_bf16_f32 v116, v80, v81\n" \
"v_min_u32 v116, -2, v116\n" \
"v_pk_add_f32 v[84:85], v[84:85], v[86:87]\n" \
"v_pk_mul_f32 v[84:85], v[84:85], v[76:77]\n" \
"v_cvt_pk_bf16_f32 v117, v84, v85\n" \
"v_min_u32 v117, -2, v117\n" \
"buffer_store_dwordx2 v[116:117], v2, s[40:43], s73 offen sc1\n" \
"s_add_u32 s71, s71, 65536\n" \
"s_add_u32 s70, s70, 1\n" \
"s_waitcnt vmcnt(1)\n" \
"s_mov_b32 s83, 0\n" \
"R1_fw%=:\n" \
"v_readfirstlane_b32 s75, v16\n" \
"s_nop 0\n" \
"s_cmp_eq_u32 s75, 0\n" \
"s_cbranch_scc1 R1_fww%=\n" \
"R1_fwx%=:\n" \
"s_add_u32 s31, s31, 2048\n" \
"s_min_u32 s31, s31, 126976\n" \
"s_nop 15\n" \
"s_nop 15\n" \
"s_nop 15\n" \
"s_nop 15\n" \
"s_nop 15\n" \
"s_nop 15\n" \
"s_nop 15\n" \
"buffer_load_dwordx4 v[44:47], v5, s[44:47], s74 offen offset:0 sc1\n" \
"buffer_load_dwordx4 v[48:51], v5, s[44:47], s74 offen offset:1024 sc1\n" \
"buffer_load_dwordx4 v[52:55], v5, s[44:47], s74 offen offset:2048 sc1\n" \
"buffer_load_dwordx4 v[56:59], v5, s[44:47], s74 offen offset:3072 sc1\n" \
"s_add_u32 s75, s74, 4096\n" \
"buffer_load_dwordx4 v[60:63], v5, s[44:47], s75 offen offset:0 sc1\n" \
"buffer_load_dwordx4 v[64:67], v5, s[44:47], s75 offen offset:1024 sc1\n" \
"buffer_load_dwordx4 v[68:71], v5, s[44:47], s75 offen offset:2048 sc1\n" \
"buffer_load_dwordx4 v[72:75], v5, s[44:47], s75 offen offset:3072 sc1\n" \
"s_add_u32 s74, s74, 524288\n" \
"s_min_u32 s74, s74, 32505856\n" \
"s_add_u32 s73, s73, 524288\n" \
"s_and_b32 s73, s73, 2097151\n" \
"s_lshr_b32 s75, s73, 7\n" \
"s_and_b32 s75, s75, 0x1000\n" \
"s_xor_b32 s75, s75, 0x5000\n" \
"v_xor_b32 v2, s75, v2\n" \
"s_sub_u32 s75, s70, 1\n" \
"s_xor_b32 s75, s75, s70\n" \
"s_bitcmp1_b32 s75, 5\n" \
"s_cselect_b32 s76, s72, 0\n" \
"s_lshl_b32 s38, s75, 11\n" \
"s_and_b32 s38, s38, 0x4000\n" \
"s_xor_b32 s76, s76, s38\n" \
"s_lshl_b32 s38, s75, 8\n" \
"s_and_b32 s38, s38, 0x1000\n" \
"s_xor_b32 s76, s76, s38\n" \
"v_xor_b32 v1, s76, v1\n" \
"s_nop 1\n" \
"s_cmp_ge_u32 s70, 63\n" \
"s_cbranch_scc1 R_done%=\n" \
"s_mov_b32 s83, 0\n" \
"R1_ck%=:\n" \
"ds_read_b64 v[100:101], v7 offset:16\n" \
"s_add_u32 s83, s83, 1\n" \
"v_mov_b32 v7, v7\n" \
"s_nop 2\n" \
"v_mov_b32 v7, v7\n" \
"s_nop 2\n" \
"v_mov_b32 v7, v7\n" \
"s_nop 2\n" \
"s_waitcnt lgkmcnt(0)\n" \
"v_readfirstlane_b32 s75, v100\n" \
"v_readfirstlane_b32 s34, v101\n" \
"s_cmp_eq_u32 s75, s70\n" \
"s_cbranch_scc1 R1_w2%=\n" \
"s_cmp_lt_u32 s83, 1048576\n" \
"s_cbranch_scc1 R1_ck%=\n" \
"s_branch R1_loop%=\n" \
"R1_fww%=:\n" \
"s_add_u32 s83, s83, 1\n" \
"s_cmp_gt_u32 s83, 65536\n" \
"s_cselect_b32 s77, 1, s77\n" \
"s_cmp_lg_u32 s77, 0\n" \
"s_cbranch_scc1 R1_fwx%=\n" \
"s_sleep 5\n" \
"buffer_load_dword v16, v14, s[44:47], s31 offen sc1\n" \
"s_waitcnt vmcnt(0)\n" \
"s_branch R1_fw%=\n" \
"R1_slowE%=:\n" \
"s_nop 7\n" \
"s_mov_b32 s83, 0\n" \
"R1_slowE1%=:\n" \
"v_max3_u32 v52, v36, v37, v38\n" \
"v_max3_u32 v52, v52, v39, v40\n" \
"v_max3_u32 v52, v52, v41, v42\n" \
"v_max_u32 v52, v52, v43\n" \
"v_cmp_eq_u32 vcc, -1, v52\n" \
"s_cbranch_vccnz R1_slowE2%=\n" \
"s_cmp_eq_u32 s83, 0\n" \
"s_cbranch_scc1 R1_okE%=\n" \
"ds_write_b128 v0, v[36:39] offset:2048\n" \
"ds_write_b128 v0, v[40:43] offset:3072\n" \
"v_mov_b64 v[100:101], 0\n" \
"v_mov_b64 v[102:103], 0\n" \
"v_mov_b64 v[104:105], 0\n" \
"v_mov_b64 v[106:107], 0\n" \
"v_mov_b64 v[108:109], 0\n" \
"v_mov_b64 v[110:111], 0\n" \
"v_mov_b64 v[112:113], 0\n" \
"v_mov_b64 v[114:115], 0\n" \
"v_smfmac_f32_16x16x64_bf16 v[100:103], v[36:39], a[16:23], v6\n" \
"v_smfmac_f32_16x16x64_bf16 v[100:103], v[40:43], a[24:31], v6\n" \
"v_smfmac_f32_16x16x64_bf16 v[108:111], v[36:39], a[144:151], v6\n" \
"v_smfmac_f32_16x16x64_bf16 v[104:107], v[36:39], a[80:87], v6\n" \
"v_smfmac_f32_16x16x64_bf16 v[112:115], v[36:39], a[208:215], v6\n" \
"s_branch R1_okE%=\n" \
"R1_slowE2%=:\n" \
"s_add_u32 s83, s83, 1\n" \
"s_add_u32 s78, s78, 1\n" \
"s_cmp_gt_u32 s83, 65536\n" \
"s_cselect_b32 s77, 1, s77\n" \
"s_cmp_lg_u32 s77, 0\n" \
"s_cbranch_scc1 R1_okE%=\n" \
"s_sleep 2\n" \
"s_mov_b64 exec, vcc\n" \
"buffer_load_dwordx4 v[36:39], v1, s[40:43], s71 offen offset:0 sc1\n" \
"buffer_load_dwordx4 v[40:43], v1, s[40:43], s71 offen offset:1024 sc1\n" \
"s_mov_b64 exec, -1\n" \
"s_waitcnt vmcnt(0)\n" \
"s_branch R1_slowE1%=\n" \
"R2_top%=:\n" \
"R2_w2%=:\n" \
"s_memtime s[14:15]\n" \
"s_getpc_b64 s[90:91]\n" \
"R2_w2pc%=:\n" \
"s_add_u32 s90, s90, R2_w2x%=-R2_w2pc%=\n" \
"s_addc_u32 s91, s91, 0\n" \
"v_mov_b32 v7, v7\n" \
"s_nop 0\n" \
"v_mov_b32 v7, v7\n" \
"s_nop 0\n" \
"v_mov_b32 v7, v7\n" \
"s_nop 0\n" \
"s_waitcnt lgkmcnt(0)\n" \
"s_sub_u32 s75, s34, s14\n" \
"v_mov_b32 v7, v7\n" \
"s_cmp_gt_i32 s75, 384\n" \
"s_cbranch_scc1 R2_w2%=\n" \
"s_max_i32 s75, s75, 0\n" \
"s_and_b32 s75, s75, -4\n" \
"s_sub_u32 s90, s90, s75\n" \
"s_subb_u32 s91, s91, 0\n" \
"v_mov_b32 v7, v7\n" \
"s_setpc_b64 s[90:91]\n" \
"v_mov_b32_e32 v7, v7\n" \
"v_mov_b32_e32 v7, v7\n" \
"v_mov_b32_e32 v7, v7\n" \
"v_mov_b32_e32 v7, v7\n" \
"v_mov_b32_e32 v7, v7\n" \
"v_mov_b32_e32 v7, v7\n" \
"v_mov_b32_e32 v7, v7\n" \
"v_mov_b32_e32 v7, v7\n" \
"v_mov_b32_e32 v7, v7\n" \
"v_mov_b32_e32 v7, v7\n" \
"v_mov_b32_e32 v7, v7\n" \
"v_mov_b32_e32 v7, v7\n" \
"v_mov_b32_e32 v7, v7\n" \
"v_mov_b32_e32 v7, v7\n" \
"v_mov_b32_e32 v7, v7\n" \
"v_mov_b32_e32 v7, v7\n" \
"v_mov_b32_e32 v7, v7\n" \
"v_mov_b32_e32 v7, v7\n" \
"v_mov_b32_e32 v7, v7\n" \
"v_mov_b32_e32 v7, v7\n" \
"v_mov_b32_e32 v7, v7\n" \
"v_mov_b32_e32 v7, v7\n" \
"v_mov_b32_e32 v7, v7\n" \
"v_mov_b32_e32 v7, v7\n" \
"v_mov_b32_e32 v7, v7\n" \
"v_mov_b32_e32 v7, v7\n" \
"v_mov_b32_e32 v7, v7\n" \
"v_mov_b32_e32 v7, v7\n" \
"v_mov_b32_e32 v7, v7\n" \
"v_mov_b32_e32 v7, v7\n" \
"v_mov_b32_e32 v7, v7\n" \
"v_mov_b32_e32 v7, v7\n" \
"v_mov_b32_e32 v7, v7\n" \
"v_mov_b32_e32 v7, v7\n" \
"v_mov_b32_e32 v7, v7\n" \
"v_mov_b32_e32 v7, v7\n" \
"v_mov_b32_e32 v7, v7\n" \
"v_mov_b32_e32 v7, v7\n" \
"v_mov_b32_e32 v7, v7\n" \
"v_mov_b32_e32 v7, v7\n" \
"v_mov_b32_e32 v7, v7\n" \
"v_mov_b32_e32 v7, v7\n" \
"v_mov_b32_e32 v7, v7\n" \
"v_mov_b32_e32 v7, v7\n" \
"v_mov_b32_e32 v7, v7\n" \
"v_mov_b32_e32 v7, v7\n" \
"v_mov_b32_e32 v7, v7\n" \
"v_mov_b32_e32 v7, v7\n" \
"v_mov_b32_e32 v7, v7\n" \
"v_mov_b32_e32 v7, v7\n" \
"v_mov_b32_e32 v7, v7\n" \
"v_mov_b32_e32 v7, v7\n" \
"v_mov_b32_e32 v7, v7\n" \
"v_mov_b32_e32 v7, v7\n" \
"v_mov_b32_e32 v7, v7\n" \
"v_mov_b32_e32 v7, v7\n" \
"v_mov_b32_e32 v7, v7\n" \
"v_mov_b32_e32 v7, v7\n" \
"v_mov_b32_e32 v7, v7\n" \
"v_mov_b32_e32 v7, v7\n" \
"v_mov_b32_e32 v7, v7\n" \
"v_mov_b32_e32 v7, v7\n" \
"v_mov_b32_e32 v7, v7\n" \
"v_mov_b32_e32 v7, v7\n" \
"v_mov_b32_e32 v7, v7\n" \
"v_mov_b32_e32 v7, v7\n" \
"v_mov_b32_e32 v7, v7\n" \
"v_mov_b32_e32 v7, v7\n" \
"v_mov_b32_e32 v7, v7\n" \
"v_mov_b32_e32 v7, v7\n" \
"v_mov_b32_e32 v7, v7\n" \
"v_mov_b32_e32 v7, v7\n" \
"v_mov_b32_e32 v7, v7\n" \
"v_mov_b32_e32 v7, v7\n" \
"v_mov_b32_e32 v7, v7\n" \
"v_mov_b32_e32 v7, v7\n" \
"v_mov_b32_e32 v7, v7\n" \
"v_mov_b32_e32 v7, v7\n" \
"v_mov_b32_e32 v7, v7\n" \
"v_mov_b32_e32 v7, v7\n" \
"v_mov_b32_e32 v7, v7\n" \
"v_mov_b32_e32 v7, v7\n" \
"v_mov_b32_e32 v7, v7\n" \
"v_mov_b32_e32 v7, v7\n" \
"v_mov_b32_e32 v7, v7\n" \
"v_mov_b32_e32 v7, v7\n" \
"v_mov_b32_e32 v7, v7\n" \
"v_mov_b32_e32 v7, v7\n" \
"v_mov_b32_e32 v7, v7\n" \
"v_mov_b32_e32 v7, v7\n" \
"v_mov_b32_e32 v7, v7\n" \
"v_mov_b32_e32 v7, v7\n" \
"v_mov_b32_e32 v7, v7\n" \
"v_mov_b32_e32 v7, v7\n" \
"v_mov_b32_e32 v7, v7\n" \
"v_mov_b32_e32 v7, v7\n" \
"R2_w2x%=:\n" \
"R2_loop%=:\n" \
"buffer_load_dwordx4 v[36:39], v1, s[40:43], s71 offen offset:0 sc1\n" \
"buffer_load_dwordx4 v[40:43], v1, s[40:43], s71 offen offset:1024 sc1\n" \
"s_waitcnt vmcnt(2)\n" \
"ds_write_b128 v4, v[116:119] offset:0\n" \
"ds_write_b128 v4, v[120:123] offset:1024\n" \
"ds_write_b128 v4, v[124:127] offset:2048\n" \
"ds_write_b128 v4, v[16:19] offset:3072\n" \
"v_xor_b32 v4, 16384, v4\n" \
"s_sub_u32 s76, s73, 524288\n" \
"s_and_b32 s76, s76, 2097151\n" \
"s_lshr_b32 s75, s76, 5\n" \
"s_and_b32 s75, s75, 0x4000\n" \
"s_lshr_b32 s38, s76, 8\n" \
"s_and_b32 s38, s38, 0x1000\n" \
"s_xor_b32 s75, s75, s38\n" \
"s_xor_b32 s75, s75, s5\n" \
"s_add_u32 s76, s76, s75\n" \
"s_sub_u32 s76, s76, 73728\n" \
"v_add_u32 v17, s76, v0\n" \
"v_mov_b64 v[124:125], -1\n" \
"v_mov_b64 v[126:127], -1\n" \
"v_mov_b64 v[100:101], 0\n" \
"v_mov_b64 v[102:103], 0\n" \
"v_mov_b64 v[104:105], 0\n" \
"v_mov_b64 v[106:107], 0\n" \
"v_mov_b64 v[108:109], 0\n" \
"v_mov_b64 v[110:111], 0\n" \
"v_mov_b64 v[112:113], 0\n" \
"v_mov_b64 v[114:115], 0\n" \
"v_mov_b64 v[116:117], 0\n" \
"v_mov_b64 v[118:119], 0\n" \
"v_mov_b64 v[120:121], 0\n" \
"v_mov_b64 v[122:123], 0\n" \
"s_waitcnt vmcnt(1)\n" \
"ds_write_b128 v0, v[36:39] offset:4096\n" \
"s_waitcnt vmcnt(0)\n" \
"ds_write_b128 v0, v[40:43] offset:5120\n" \
"v_smfmac_f32_16x16x64_bf16 v[100:103], v[36:39], a[32:39], v6\n" \
"v_smfmac_f32_16x16x64_bf16 v[100:103], v[40:43], a[40:47], v6\n" \
"v_smfmac_f32_16x16x64_bf16 v[108:111], v[36:39], a[160:167], v6\n" \
"v_smfmac_f32_16x16x64_bf16 v[104:107], v[36:39], a[96:103], v6\n" \
"v_cmp_u_f32 vcc, v100, v101\n" \
"v_cmp_u_f32 s[84:85], v102, v103\n" \
"v_smfmac_f32_16x16x64_bf16 v[112:115], v[36:39], a[224:231], v6\n" \
"s_or_b64 vcc, vcc, s[84:85]\n" \
"s_cbranch_vccnz R2_slowE%=\n" \
"R2_okE%=:\n" \
"s_waitcnt lgkmcnt(0)\n" \
"s_barrier \n" \
"ds_read_b128 v[44:47], v0 offset:6144\n" \
"ds_read_b128 v[48:51], v0 offset:7168\n" \
"v_smfmac_f32_16x16x64_bf16 v[108:111], v[40:43], a[168:175], v6\n" \
"ds_read_b128 v[52:55], v0 offset:0\n" \
"ds_read_b128 v[56:59], v0 offset:1024\n" \
"v_smfmac_f32_16x16x64_bf16 v[104:107], v[40:43], a[104:111], v6\n" \
"ds_read_b128 v[60:63], v0 offset:2048\n" \
"ds_read_b128 v[64:67], v0 offset:3072\n" \
"v_smfmac_f32_16x16x64_bf16 v[112:115], v[40:43], a[232:239], v6\n" \
"v_smfmac_f32_16x16x64_bf16 v[116:119], v[36:39], v[160:167], v6\n" \
"v_smfmac_f32_16x16x64_bf16 v[120:123], v[36:39], v[224:231], v6\n" \
"s_waitcnt lgkmcnt(5)\n" \
"v_smfmac_f32_16x16x64_bf16 v[100:103], v[44:47], a[48:55], v6\n" \
"ds_read_b128 v[68:71], v10 offset:0\n" \
"v_smfmac_f32_16x16x64_bf16 v[108:111], v[44:47], a[176:183], v6\n" \
"ds_read_b128 v[72:75], v10 offset:4096\n" \
"v_smfmac_f32_16x16x64_bf16 v[104:107], v[44:47], a[112:119], v6\n" \
"ds_read_b128 v[76:79], v10 offset:1024\n" \
"v_smfmac_f32_16x16x64_bf16 v[112:115], v[44:47], a[240:247], v6\n" \
"ds_read_b128 v[80:83], v10 offset:5120\n" \
"s_waitcnt lgkmcnt(8)\n" \
"v_smfmac_f32_16x16x64_bf16 v[100:103], v[48:51], a[56:63], v6\n" \
"ds_read_b128 v[84:87], v10 offset:2048\n" \
"v_smfmac_f32_16x16x64_bf16 v[108:111], v[48:51], a[184:191], v6\n" \
"ds_read_b128 v[88:91], v10 offset:6144\n" \
"v_smfmac_f32_16x16x64_bf16 v[104:107], v[48:51], a[120:127], v6\n" \
"ds_read_b128 v[92:95], v10 offset:3072\n" \
"v_smfmac_f32_16x16x64_bf16 v[112:115], v[48:51], a[248:255], v6\n" \
"ds_read_b128 v[96:99], v10 offset:7168\n" \
"s_waitcnt lgkmcnt(11)\n" \
"v_smfmac_f32_16x16x64_bf16 v[100:103], v[52:55], a[0:7], v6\n" \
"v_xor_b32 v10, 16384, v10\n" \
"v_smfmac_f32_16x16x64_bf16 v[108:111], v[52:55], a[128:135], v6\n" \
"s_mov_b32 vcc_lo, 0x33333333\n" \
"v_smfmac_f32_16x16x64_bf16 v[104:107], v[52:55], a[64:71], v6\n" \
"s_mov_b32 vcc_hi, 0x33333333\n" \
"v_smfmac_f32_16x16x64_bf16 v[112:115], v[52:55], a[192:199], v6\n" \
"buffer_store_dwordx4 v[124:127], v17, s[40:43], 0 offen sc1\n" \
"s_waitcnt lgkmcnt(10)\n" \
"v_smfmac_f32_16x16x64_bf16 v[100:103], v[56:59], a[8:15], v6\n" \
"buffer_load_dword v16, v14, s[44:47], s31 offen sc1\n" \
"v_smfmac_f32_16x16x64_bf16 v[108:111], v[56:59], a[136:143], v6\n" \
"v_smfmac_f32_16x16x64_bf16 v[104:107], v[56:59], a[72:79], v6\n" \
"v_smfmac_f32_16x16x64_bf16 v[112:115], v[56:59], a[200:207], v6\n" \
"s_waitcnt lgkmcnt(9)\n" \
"v_smfmac_f32_16x16x64_bf16 v[100:103], v[60:63], a[16:23], v6\n" \
"v_smfmac_f32_16x16x64_bf16 v[108:111], v[60:63], a[144:151], v6\n" \
"v_smfmac_f32_16x16x64_bf16 v[104:107], v[60:63], a[80:87], v6\n" \
"v_smfmac_f32_16x16x64_bf16 v[112:115], v[60:63], a[208:215], v6\n" \
"s_waitcnt lgkmcnt(8)\n" \
"v_smfmac_f32_16x16x64_bf16 v[100:103], v[64:67], a[24:31], v6\n" \
"v_smfmac_f32_16x16x64_bf16 v[108:111], v[64:67], a[152:159], v6\n" \
"v_smfmac_f32_16x16x64_bf16 v[104:107], v[64:67], a[88:95], v6\n" \
"v_smfmac_f32_16x16x64_bf16 v[112:115], v[64:67], a[216:223], v6\n" \
"v_add_f32 v100, v100, v102\n" \
"v_add_f32 v101, v101, v103\n" \
"v_cvt_pk_bf16_f32 v124, v100, v101\n" \
"v_add_f32 v108, v108, v110\n" \
"v_add_f32 v109, v109, v111\n" \
"v_cvt_pk_bf16_f32 v125, v108, v109\n" \
"v_pk_add_f32 v[104:105], v[104:105], v[106:107]\n" \
"v_cvt_pk_bf16_f32 v126, v104, v105\n" \
"v_pk_add_f32 v[112:113], v[112:113], v[114:115]\n" \
"v_cvt_pk_bf16_f32 v127, v112, v113\n" \
"v_smfmac_f32_16x16x64_bf16 v[116:119], v[40:43], v[168:175], v6\n" \
"ds_write_b128 v8, v[124:127]\n" \
"v_smfmac_f32_16x16x64_bf16 v[120:123], v[40:43], v[232:239], v6\n" \
"v_smfmac_f32_16x16x64_bf16 v[116:119], v[44:47], v[176:183], v6\n" \
"v_smfmac_f32_16x16x64_bf16 v[120:123], v[44:47], v[240:247], v6\n" \
"v_smfmac_f32_16x16x64_bf16 v[116:119], v[48:51], v[184:191], v6\n" \
"s_waitcnt lgkmcnt(0)\n" \
"s_barrier \n" \
"ds_read_b64_tr_b16 v[36:37], v9 offset:0\n" \
"ds_read_b64_tr_b16 v[38:39], v9 offset:64\n" \
"v_smfmac_f32_16x16x64_bf16 v[120:123], v[48:51], v[248:255], v6\n" \
"ds_read_b64_tr_b16 v[40:41], v9 offset:256\n" \
"ds_read_b64_tr_b16 v[42:43], v9 offset:320\n" \
"v_smfmac_f32_16x16x64_bf16 v[116:119], v[52:55], v[128:135], v6\n" \
"ds_read_b64_tr_b16 v[124:125], v9 offset:512\n" \
"ds_read_b64_tr_b16 v[126:127], v9 offset:576\n" \
"v_smfmac_f32_16x16x64_bf16 v[120:123], v[52:55], v[192:199], v6\n" \
"ds_read_b64_tr_b16 v[112:113], v9 offset:768\n" \
"ds_read_b64_tr_b16 v[114:115], v9 offset:832\n" \
"v_smfmac_f32_16x16x64_bf16 v[116:119], v[56:59], v[136:143], v6\n" \
"s_waitcnt lgkmcnt(6)\n" \
"v_mfma_f32_16x16x32_bf16 v[100:103], v[68:71], v[36:39], 0\n" \
"v_mfma_f32_16x16x32_bf16 v[104:107], v[72:75], v[36:39], 0\n" \
"v_mov_b32 v68, 0x3f803f80\n" \
"v_mov_b32 v69, 0x3f803f80\n" \
"v_mfma_f32_16x16x32_bf16 v[108:111], v[36:39], v[36:39], 0\n" \
"v_mov_b32 v70, 0x3f80\n" \
"v_mov_b32 v71, 0x3f80\n" \
"s_waitcnt lgkmcnt(4)\n" \
"v_mfma_f32_16x16x32_bf16 v[100:103], v[76:79], v[40:43], v[100:103]\n" \
"v_mov_b32 v72, 0x3f803f80\n" \
"v_mov_b32 v73, 0x3f803f80\n" \
"v_mfma_f32_16x16x32_bf16 v[104:107], v[80:83], v[40:43], v[104:107]\n" \
"v_mov_b32 v74, 0x3f80\n" \
"v_mov_b32 v75, 0x3f80\n" \
"v_mfma_f32_16x16x32_bf16 v[108:111], v[40:43], v[40:43], v[108:111]\n" \
"v_mov_b64 v[76:77], 0\n" \
"v_mov_b64 v[78:79], 0\n" \
"s_waitcnt lgkmcnt(2)\n" \
"v_mfma_f32_16x16x32_bf16 v[100:103], v[84:87], v[124:127], v[100:103]\n" \
"v_mov_b64 v[80:81], 0\n" \
"v_mov_b64 v[82:83], 0\n" \
"v_mfma_f32_16x16x32_bf16 v[104:107], v[88:91], v[124:127], v[104:107]\n" \
"v_mov_b64 v[84:85], 0\n" \
"v_mov_b64 v[86:87], 0\n" \
"v_mfma_f32_16x16x32_bf16 v[108:111], v[124:127], v[124:127], v[108:111]\n" \
"s_waitcnt lgkmcnt(0)\n" \
"v_mfma_f32_16x16x32_bf16 v[100:103], v[92:95], v[112:115], v[100:103]\n" \
"v_mfma_f32_16x16x32_bf16 v[104:107], v[96:99], v[112:115], v[104:107]\n" \
"v_mfma_f32_16x16x32_bf16 v[108:111], v[112:115], v[112:115], v[108:111]\n" \
"v_cndmask_b32_dpp v100, v102, v100, vcc quad_perm:[0,1,0,1] row_mask:0xf bank_mask:0xf\n" \
"v_cndmask_b32_dpp v101, v103, v101, vcc quad_perm:[0,1,0,1] row_mask:0xf bank_mask:0xf\n" \
"v_smfmac_f32_16x16x64_bf16 v[120:123], v[56:59], v[200:207], v6\n" \
"v_cndmask_b32_dpp v104, v106, v104, vcc quad_perm:[0,1,0,1] row_mask:0xf bank_mask:0xf\n" \
"v_cndmask_b32_dpp v105, v107, v105, vcc quad_perm:[0,1,0,1] row_mask:0xf bank_mask:0xf\n" \
"v_max3_f32 v92, v100, v101, v104\n" \
"s_nop 0\n" \
"v_cndmask_b32_dpp v110, v111, v110, vcc quad_perm:[0,1,0,1] row_mask:0xf bank_mask:0xf\n" \
"v_max3_f32 v92, v92, v105, v110\n" \
"v_smfmac_f32_16x16x64_bf16 v[116:119], v[60:63], v[144:151], v6\n" \
"s_nop 0\n" \
"v_max_f32_dpp v93, v92, v92 quad_perm:[2,3,0,1] row_mask:0xf bank_mask:0xf\n" \
"v_max_f32_dpp v92, v92, v92 quad_perm:[2,3,0,1] row_mask:0xf bank_mask:0xf\n" \
"v_smfmac_f32_16x16x64_bf16 v[120:123], v[60:63], v[208:215], v6\n" \
"s_nop 0\n" \
"v_permlane16_swap_b32 v92, v93\n" \
"v_max_f32 v94, v92, v93\n" \
"v_max_f32 v92, v92, v93\n" \
"v_smfmac_f32_16x16x64_bf16 v[116:119], v[64:67], v[152:159], v6\n" \
"s_nop 0\n" \
"v_permlane32_swap_b32 v92, v94\n" \
"v_max_f32 v92, v92, v94\n" \
"v_mul_f32 v94, s81, v92\n" \
"v_fma_f32 v110, v110, s80, v94\n" \
"v_pk_fma_f32 v[100:101], v[100:101], s[80:81], v[94:95] op_sel_hi:[1,0,0]\n" \
"v_pk_fma_f32 v[104:105], v[104:105], s[80:81], v[94:95] op_sel_hi:[1,0,0]\n" \
"v_smfmac_f32_16x16x64_bf16 v[120:123], v[64:67], v[216:223], v6\n" \
"v_exp_f32 v110, v110\n" \
"v_exp_f32 v100, v100\n" \
"v_exp_f32 v101, v101\n" \
"v_exp_f32 v104, v104\n" \
"v_exp_f32 v105, v105\n" \
"v_cvt_pk_bf16_f32 v91, v110, v110\n" \
"v_cvt_pk_bf16_f32 v88, v100, v101\n" \
"v_and_b32 v89, v12, v91\n" \
"v_and_b32 v91, v13, v91\n" \
"v_cvt_pk_bf16_f32 v90, v104, v105\n" \
"v_cvt_pk_bf16_f32 v22, v116, v118\n" \
"v_cvt_pk_bf16_f32 v23, v117, v119\n" \
"v_smfmac_f32_16x16x64_bf16 v[76:79], v[88:91], v[68:75], v6\n" \
"v_mov_b64 v[26:27], v[22:23]\n" \
"v_cvt_pk_bf16_f32 v30, v120, v122\n" \
"v_cvt_pk_bf16_f32 v31, v121, v123\n" \
"v_smfmac_f32_16x16x64_bf16 v[80:83], v[88:91], v[20:27], v6\n" \
"v_mov_b64 v[34:35], v[30:31]\n" \
"s_nop 1\n" \
"v_smfmac_f32_16x16x64_bf16 v[84:87], v[88:91], v[28:35], v6\n" \
"v_pk_add_f32 v[76:77], v[76:77], v[78:79]\n" \
"v_rcp_f32 v76, v76\n" \
"v_rcp_f32 v77, v77\n" \
"s_nop 0\n" \
"v_pk_add_f32 v[80:81], v[80:81], v[82:83]\n" \
"v_pk_mul_f32 v[80:81], v[80:81], v[76:77]\n" \
"v_cvt_pk_bf16_f32 v116, v80, v81\n" \
"v_min_u32 v116, -2, v116\n" \
"v_pk_add_f32 v[84:85], v[84:85], v[86:87]\n" \
"v_pk_mul_f32 v[84:85], v[84:85], v[76:77]\n" \
"v_cvt_pk_bf16_f32 v117, v84, v85\n" \
"v_min_u32 v117, -2, v117\n" \
"buffer_store_dwordx2 v[116:117], v2, s[40:43], s73 offen sc1\n" \
"s_add_u32 s71, s71, 65536\n" \
"s_add_u32 s70, s70, 1\n" \
"s_waitcnt vmcnt(1)\n" \
"s_mov_b32 s83, 0\n" \
"R2_fw%=:\n" \
"v_readfirstlane_b32 s75, v16\n" \
"s_nop 0\n" \
"s_cmp_eq_u32 s75, 0\n" \
"s_cbranch_scc1 R2_fww%=\n" \
"R2_fwx%=:\n" \
"s_add_u32 s31, s31, 2048\n" \
"s_min_u32 s31, s31, 126976\n" \
"ds_read_b128 v[68:71], v11 offset:0\n" \
"ds_read_b128 v[76:79], v15 offset:0\n" \
"ds_read_b128 v[80:83], v15 offset:4096\n" \
"ds_read_b128 v[72:75], v11 offset:128\n" \
"ds_read_b128 v[84:87], v15 offset:16384\n" \
"ds_read_b128 v[88:91], v15 offset:20480\n" \
"ds_read_b128 v[44:47], v11 offset:1024\n" \
"ds_read_b128 v[52:55], v15 offset:1024\n" \
"ds_read_b128 v[56:59], v15 offset:5120\n" \
"ds_read_b128 v[48:51], v11 offset:1152\n" \
"ds_read_b128 v[60:63], v15 offset:17408\n" \
"ds_read_b128 v[64:67], v15 offset:21504\n" \
"s_waitcnt lgkmcnt(10)\n" \
"v_mfma_f32_16x16x32_bf16 v[36:39], v[68:71], v[76:79], 0\n" \
"s_waitcnt lgkmcnt(9)\n" \
"v_mfma_f32_16x16x32_bf16 v[40:43], v[68:71], v[80:83], 0\n" \
"s_waitcnt lgkmcnt(8)\n" \
"v_mfma_f32_16x16x32_bf16 v[92:95], v[72:75], v[76:79], 0\n" \
"v_mfma_f32_16x16x32_bf16 v[96:99], v[72:75], v[80:83], 0\n" \
"s_waitcnt lgkmcnt(7)\n" \
"v_mfma_f32_16x16x32_bf16 v[100:103], v[68:71], v[84:87], 0\n" \
"s_waitcnt lgkmcnt(6)\n" \
"v_mfma_f32_16x16x32_bf16 v[104:107], v[68:71], v[88:91], 0\n" \
"v_mfma_f32_16x16x32_bf16 v[108:111], v[72:75], v[84:87], 0\n" \
"v_mfma_f32_16x16x32_bf16 v[112:115], v[72:75], v[88:91], 0\n" \
"ds_read_b128 v[68:71], v11 offset:2048\n" \
"ds_read_b128 v[76:79], v15 offset:2048\n" \
"ds_read_b128 v[80:83], v15 offset:6144\n" \
"ds_read_b128 v[72:75], v11 offset:2176\n" \
"ds_read_b128 v[84:87], v15 offset:18432\n" \
"ds_read_b128 v[88:91], v15 offset:22528\n" \
"s_waitcnt lgkmcnt(10)\n" \
"v_mfma_f32_16x16x32_bf16 v[36:39], v[44:47], v[52:55], v[36:39]\n" \
"s_waitcnt lgkmcnt(9)\n" \
"v_mfma_f32_16x16x32_bf16 v[40:43], v[44:47], v[56:59], v[40:43]\n" \
"s_waitcnt lgkmcnt(8)\n" \
"v_mfma_f32_16x16x32_bf16 v[92:95], v[48:51], v[52:55], v[92:95]\n" \
"v_mfma_f32_16x16x32_bf16 v[96:99], v[48:51], v[56:59], v[96:99]\n" \
"s_waitcnt lgkmcnt(7)\n" \
"v_mfma_f32_16x16x32_bf16 v[100:103], v[44:47], v[60:63], v[100:103]\n" \
"s_waitcnt lgkmcnt(6)\n" \
"v_mfma_f32_16x16x32_bf16 v[104:107], v[44:47], v[64:67], v[104:107]\n" \
"v_mfma_f32_16x16x32_bf16 v[108:111], v[48:51], v[60:63], v[108:111]\n" \
"v_mfma_f32_16x16x32_bf16 v[112:115], v[48:51], v[64:67], v[112:115]\n" \
"ds_read_b128 v[44:47], v11 offset:3072\n" \
"ds_read_b128 v[52:55], v15 offset:3072\n" \
"ds_read_b128 v[56:59], v15 offset:7168\n" \
"ds_read_b128 v[48:51], v11 offset:3200\n" \
"ds_read_b128 v[60:63], v15 offset:19456\n" \
"ds_read_b128 v[64:67], v15 offset:23552\n" \
"s_waitcnt lgkmcnt(10)\n" \
"v_mfma_f32_16x16x32_bf16 v[36:39], v[68:71], v[76:79], v[36:39]\n" \
"s_waitcnt lgkmcnt(9)\n" \
"v_mfma_f32_16x16x32_bf16 v[40:43], v[68:71], v[80:83], v[40:43]\n" \
"s_waitcnt lgkmcnt(8)\n" \
"v_mfma_f32_16x16x32_bf16 v[92:95], v[72:75], v[76:79], v[92:95]\n" \
"v_mfma_f32_16x16x32_bf16 v[96:99], v[72:75], v[80:83], v[96:99]\n" \
"s_waitcnt lgkmcnt(7)\n" \
"v_mfma_f32_16x16x32_bf16 v[100:103], v[68:71], v[84:87], v[100:103]\n" \
"s_waitcnt lgkmcnt(6)\n" \
"v_mfma_f32_16x16x32_bf16 v[104:107], v[68:71], v[88:91], v[104:107]\n" \
"v_mfma_f32_16x16x32_bf16 v[108:111], v[72:75], v[84:87], v[108:111]\n" \
"v_mfma_f32_16x16x32_bf16 v[112:115], v[72:75], v[88:91], v[112:115]\n" \
"buffer_load_dwordx4 v[116:119], v5, s[44:47], s74 offen offset:0 sc1\n" \
"buffer_load_dwordx4 v[120:123], v5, s[44:47], s74 offen offset:1024 sc1\n" \
"buffer_load_dwordx4 v[124:127], v5, s[44:47], s74 offen offset:2048 sc1\n" \
"buffer_load_dwordx4 v[16:19], v5, s[44:47], s74 offen offset:3072 sc1\n" \
"s_add_u32 s74, s74, 524288\n" \
"s_min_u32 s74, s74, 32505856\n" \
"s_waitcnt lgkmcnt(4)\n" \
"v_mfma_f32_16x16x32_bf16 v[36:39], v[44:47], v[52:55], v[36:39]\n" \
"s_waitcnt lgkmcnt(3)\n" \
"v_mfma_f32_16x16x32_bf16 v[40:43], v[44:47], v[56:59], v[40:43]\n" \
"s_waitcnt lgkmcnt(2)\n" \
"v_mfma_f32_16x16x32_bf16 v[92:95], v[48:51], v[52:55], v[92:95]\n" \
"v_mfma_f32_16x16x32_bf16 v[96:99], v[48:51], v[56:59], v[96:99]\n" \
"s_waitcnt lgkmcnt(1)\n" \
"v_mfma_f32_16x16x32_bf16 v[100:103], v[44:47], v[60:63], v[100:103]\n" \
"s_waitcnt lgkmcnt(0)\n" \
"v_mfma_f32_16x16x32_bf16 v[104:107], v[44:47], v[64:67], v[104:107]\n" \
"v_mfma_f32_16x16x32_bf16 v[108:111], v[48:51], v[60:63], v[108:111]\n" \
"v_mfma_f32_16x16x32_bf16 v[112:115], v[48:51], v[64:67], v[112:115]\n" \
"v_xor_b32 v11, 16384, v11\n" \
"v_cvt_pk_bf16_f32 v68, v36, v37\n" \
"v_cvt_pk_bf16_f32 v69, v38, v39\n" \
"v_cvt_pk_bf16_f32 v70, v92, v93\n" \
"v_cvt_pk_bf16_f32 v71, v94, v95\n" \
"ds_write_b128 v3, v[68:71] offset:0\n" \
"v_cvt_pk_bf16_f32 v72, v40, v41\n" \
"v_cvt_pk_bf16_f32 v73, v42, v43\n" \
"v_cvt_pk_bf16_f32 v74, v96, v97\n" \
"v_cvt_pk_bf16_f32 v75, v98, v99\n" \
"ds_write_b128 v3, v[72:75] offset:1024\n" \
"v_cvt_pk_bf16_f32 v20, v100, v101\n" \
"v_cvt_pk_bf16_f32 v21, v102, v103\n" \
"v_cvt_pk_bf16_f32 v28, v104, v105\n" \
"v_cvt_pk_bf16_f32 v29, v106, v107\n" \
"v_cvt_pk_bf16_f32 v24, v108, v109\n" \
"v_cvt_pk_bf16_f32 v25, v110, v111\n" \
"v_cvt_pk_bf16_f32 v32, v112, v113\n" \
"v_cvt_pk_bf16_f32 v33, v114, v115\n" \
"s_add_u32 s73, s73, 524288\n" \
"s_and_b32 s73, s73, 2097151\n" \
"s_lshr_b32 s75, s73, 7\n" \
"s_and_b32 s75, s75, 0x1000\n" \
"s_xor_b32 s75, s75, 0x5000\n" \
"v_xor_b32 v2, s75, v2\n" \
"s_sub_u32 s75, s70, 1\n" \
"s_xor_b32 s75, s75, s70\n" \
"s_bitcmp1_b32 s75, 5\n" \
"s_cselect_b32 s76, s72, 0\n" \
"s_lshl_b32 s38, s75, 11\n" \
"s_and_b32 s38, s38, 0x4000\n" \
"s_xor_b32 s76, s76, s38\n" \
"s_lshl_b32 s38, s75, 8\n" \
"s_and_b32 s38, s38, 0x1000\n" \
"s_xor_b32 s76, s76, s38\n" \
"v_xor_b32 v1, s76, v1\n" \
"s_nop 1\n" \
"s_cmp_ge_u32 s70, 63\n" \
"s_cbranch_scc1 R_done%=\n" \
"s_mov_b32 s83, 0\n" \
"R2_ck%=:\n" \
"ds_read_b64 v[100:101], v7 offset:16\n" \
"s_add_u32 s83, s83, 1\n" \
"v_mov_b32 v7, v7\n" \
"s_nop 2\n" \
"v_mov_b32 v7, v7\n" \
"s_nop 2\n" \
"v_mov_b32 v7, v7\n" \
"s_nop 2\n" \
"s_waitcnt lgkmcnt(0)\n" \
"v_readfirstlane_b32 s75, v100\n" \
"v_readfirstlane_b32 s34, v101\n" \
"s_cmp_eq_u32 s75, s70\n" \
"s_cbranch_scc1 R2_w2%=\n" \
"s_cmp_lt_u32 s83, 1048576\n" \
"s_cbranch_scc1 R2_ck%=\n" \
"s_branch R2_loop%=\n" \
"R2_fww%=:\n" \
"s_add_u32 s83, s83, 1\n" \
"s_cmp_gt_u32 s83, 65536\n" \
"s_cselect_b32 s77, 1, s77\n" \
"s_cmp_lg_u32 s77, 0\n" \
"s_cbranch_scc1 R2_fwx%=\n" \
"s_sleep 5\n" \
"s_waitcnt vmcnt(0)\n" \
"buffer_load_dword v16, v14, s[44:47], s31 offen sc1\n" \
"s_waitcnt vmcnt(0)\n" \
"s_branch R2_fw%=\n" \
"R2_slowE%=:\n" \
"s_nop 7\n" \
"s_mov_b32 s83, 0\n" \
"R2_slowE1%=:\n" \
"v_max3_u32 v52, v36, v37, v38\n" \
"v_max3_u32 v52, v52, v39, v40\n" \
"v_max3_u32 v52, v52, v41, v42\n" \
"v_max_u32 v52, v52, v43\n" \
"v_cmp_eq_u32 vcc, -1, v52\n" \
"s_cbranch_vccnz R2_slowE2%=\n" \
"s_cmp_eq_u32 s83, 0\n" \
"s_cbranch_scc1 R2_okE%=\n" \
"ds_write_b128 v0, v[36:39] offset:4096\n" \
"ds_write_b128 v0, v[40:43] offset:5120\n" \
"v_mov_b64 v[100:101], 0\n" \
"v_mov_b64 v[102:103], 0\n" \
"v_mov_b64 v[104:105], 0\n" \
"v_mov_b64 v[106:107], 0\n" \
"v_mov_b64 v[108:109], 0\n" \
"v_mov_b64 v[110:111], 0\n" \
"v_mov_b64 v[112:113], 0\n" \
"v_mov_b64 v[114:115], 0\n" \
"v_smfmac_f32_16x16x64_bf16 v[100:103], v[36:39], a[32:39], v6\n" \
"v_smfmac_f32_16x16x64_bf16 v[100:103], v[40:43], a[40:47], v6\n" \
"v_smfmac_f32_16x16x64_bf16 v[108:111], v[36:39], a[160:167], v6\n" \
"v_smfmac_f32_16x16x64_bf16 v[104:107], v[36:39], a[96:103], v6\n" \
"v_smfmac_f32_16x16x64_bf16 v[112:115], v[36:39], a[224:231], v6\n" \
"s_branch R2_okE%=\n" \
"R2_slowE2%=:\n" \
"s_add_u32 s83, s83, 1\n" \
"s_add_u32 s78, s78, 1\n" \
"s_cmp_gt_u32 s83, 65536\n" \
"s_cselect_b32 s77, 1, s77\n" \
"s_cmp_lg_u32 s77, 0\n" \
"s_cbranch_scc1 R2_okE%=\n" \
"s_sleep 2\n" \
"s_mov_b64 exec, vcc\n" \
"buffer_load_dwordx4 v[36:39], v1, s[40:43], s71 offen offset:0 sc1\n" \
"buffer_load_dwordx4 v[40:43], v1, s[40:43], s71 offen offset:1024 sc1\n" \
"s_mov_b64 exec, -1\n" \
"s_waitcnt vmcnt(0)\n" \
"s_branch R2_slowE1%=\n" \
"R3_top%=:\n" \
"R3_w2%=:\n" \
"s_memtime s[14:15]\n" \
"s_getpc_b64 s[90:91]\n" \
"R3_w2pc%=:\n" \
"s_add_u32 s90, s90, R3_w2x%=-R3_w2pc%=\n" \
"s_addc_u32 s91, s91, 0\n" \
"v_mov_b32 v7, v7\n" \
"s_nop 0\n" \
"v_mov_b32 v7, v7\n" \
"s_nop 0\n" \
"v_mov_b32 v7, v7\n" \
"s_nop 0\n" \
"s_waitcnt lgkmcnt(0)\n" \
"s_sub_u32 s75, s34, s14\n" \
"v_mov_b32 v7, v7\n" \
"s_cmp_gt_i32 s75, 384\n" \
"s_cbranch_scc1 R3_w2%=\n" \
"s_max_i32 s75, s75, 0\n" \
"s_and_b32 s75, s75, -4\n" \
"s_sub_u32 s90, s90, s75\n" \
"s_subb_u32 s91, s91, 0\n" \
"v_mov_b32 v7, v7\n" \
"s_setpc_b64 s[90:91]\n" \
"v_mov_b32_e32 v7, v7\n" \
"v_mov_b32_e32 v7, v7\n" \
"v_mov_b32_e32 v7, v7\n" \
"v_mov_b32_e32 v7, v7\n" \
"v_mov_b32_e32 v7, v7\n" \
"v_mov_b32_e32 v7, v7\n" \
"v_mov_b32_e32 v7, v7\n" \
"v_mov_b32_e32 v7, v7\n" \
"v_mov_b32_e32 v7, v7\n" \
"v_mov_b32_e32 v7, v7\n" \
"v_mov_b32_e32 v7, v7\n" \
"v_mov_b32_e32 v7, v7\n" \
"v_mov_b32_e32 v7, v7\n" \
"v_mov_b32_e32 v7, v7\n" \
"v_mov_b32_e32 v7, v7\n" \
"v_mov_b32_e32 v7, v7\n" \
"v_mov_b32_e32 v7, v7\n" \
"v_mov_b32_e32 v7, v7\n" \
"v_mov_b32_e32 v7, v7\n" \
"v_mov_b32_e32 v7, v7\n" \
"v_mov_b32_e32 v7, v7\n" \
"v_mov_b32_e32 v7, v7\n" \
"v_mov_b32_e32 v7, v7\n" \
"v_mov_b32_e32 v7, v7\n" \
"v_mov_b32_e32 v7, v7\n" \
"v_mov_b32_e32 v7, v7\n" \
"v_mov_b32_e32 v7, v7\n" \
"v_mov_b32_e32 v7, v7\n" \
"v_mov_b32_e32 v7, v7\n" \
"v_mov_b32_e32 v7, v7\n" \
"v_mov_b32_e32 v7, v7\n" \
"v_mov_b32_e32 v7, v7\n" \
"v_mov_b32_e32 v7, v7\n" \
"v_mov_b32_e32 v7, v7\n" \
"v_mov_b32_e32 v7, v7\n" \
"v_mov_b32_e32 v7, v7\n" \
"v_mov_b32_e32 v7, v7\n" \
"v_mov_b32_e32 v7, v7\n" \
"v_mov_b32_e32 v7, v7\n" \
"v_mov_b32_e32 v7, v7\n" \
"v_mov_b32_e32 v7, v7\n" \
"v_mov_b32_e32 v7, v7\n" \
"v_mov_b32_e32 v7, v7\n" \
"v_mov_b32_e32 v7, v7\n" \
"v_mov_b32_e32 v7, v7\n" \
"v_mov_b32_e32 v7, v7\n" \
"v_mov_b32_e32 v7, v7\n" \
"v_mov_b32_e32 v7, v7\n" \
"v_mov_b32_e32 v7, v7\n" \
"v_mov_b32_e32 v7, v7\n" \
"v_mov_b32_e32 v7, v7\n" \
"v_mov_b32_e32 v7, v7\n" \
"v_mov_b32_e32 v7, v7\n" \
"v_mov_b32_e32 v7, v7\n" \
"v_mov_b32_e32 v7, v7\n" \
"v_mov_b32_e32 v7, v7\n" \
"v_mov_b32_e32 v7, v7\n" \
"v_mov_b32_e32 v7, v7\n" \
"v_mov_b32_e32 v7, v7\n" \
"v_mov_b32_e32 v7, v7\n" \
"v_mov_b32_e32 v7, v7\n" \
"v_mov_b32_e32 v7, v7\n" \
"v_mov_b32_e32 v7, v7\n" \
"v_mov_b32_e32 v7, v7\n" \
"v_mov_b32_e32 v7, v7\n" \
"v_mov_b32_e32 v7, v7\n" \
"v_mov_b32_e32 v7, v7\n" \
"v_mov_b32_e32 v7, v7\n" \
"v_mov_b32_e32 v7, v7\n" \
"v_mov_b32_e32 v7, v7\n" \
"v_mov_b32_e32 v7, v7\n" \
"v_mov_b32_e32 v7, v7\n" \
"v_mov_b32_e32 v7, v7\n" \
"v_mov_b32_e32 v7, v7\n" \
"v_mov_b32_e32 v7, v7\n" \
"v_mov_b32_e32 v7, v7\n" \
"v_mov_b32_e32 v7, v7\n" \
"v_mov_b32_e32 v7, v7\n" \
"v_mov_b32_e32 v7, v7\n" \
"v_mov_b32_e32 v7, v7\n" \
"v_mov_b32_e32 v7, v7\n" \
"v_mov_b32_e32 v7, v7\n" \
"v_mov_b32_e32 v7, v7\n" \
"v_mov_b32_e32 v7, v7\n" \
"v_mov_b32_e32 v7, v7\n" \
"v_mov_b32_e32 v7, v7\n" \
"v_mov_b32_e32 v7, v7\n" \
"v_mov_b32_e32 v7, v7\n" \
"v_mov_b32_e32 v7, v7\n" \
"v_mov_b32_e32 v7, v7\n" \
"v_mov_b32_e32 v7, v7\n" \
"v_mov_b32_e32 v7, v7\n" \
"v_mov_b32_e32 v7, v7\n" \
"v_mov_b32_e32 v7, v7\n" \
"v_mov_b32_e32 v7, v7\n" \
"v_mov_b32_e32 v7, v7\n" \
"R3_w2x%=:\n" \
"R3_loop%=:\n" \
"buffer_load_dwordx4 v[36:39], v1, s[40:43], s71 offen offset:0 sc1\n" \
"buffer_load_dwordx4 v[40:43], v1, s[40:43], s71 offen offset:1024 sc1\n" \
"s_waitcnt vmcnt(2)\n" \
"ds_write_b128 v4, v[116:119] offset:0\n" \
"ds_write_b128 v4, v[120:123] offset:1024\n" \
"ds_write_b128 v4, v[124:127] offset:2048\n" \
"ds_write_b128 v4, v[16:19] offset:3072\n" \
"v_xor_b32 v4, 16384, v4\n" \
"s_sub_u32 s76, s73, 524288\n" \
"s_and_b32 s76, s76, 2097151\n" \
"s_lshr_b32 s75, s76, 5\n" \
"s_and_b32 s75, s75, 0x4000\n" \
"s_lshr_b32 s38, s76, 8\n" \
"s_and_b32 s38, s38, 0x1000\n" \
"s_xor_b32 s75, s75, s38\n" \
"s_xor_b32 s75, s75, s5\n" \
"s_add_u32 s76, s76, s75\n" \
"s_sub_u32 s76, s76, 73728\n" \
"v_add_u32 v17, s76, v0\n" \
"v_mov_b64 v[124:125], -1\n" \
"v_mov_b64 v[126:127], -1\n" \
"v_mov_b32 v18, s6\n" \
"v_mov_b64 v[100:101], 0\n" \
"v_mov_b64 v[102:103], 0\n" \
"v_mov_b64 v[104:105], 0\n" \
"v_mov_b64 v[106:107], 0\n" \
"v_mov_b64 v[108:109], 0\n" \
"v_mov_b64 v[110:111], 0\n" \
"v_mov_b64 v[112:113], 0\n" \
"v_mov_b64 v[114:115], 0\n" \
"v_mov_b64 v[116:117], 0\n" \
"v_mov_b64 v[118:119], 0\n" \
"v_mov_b64 v[120:121], 0\n" \
"v_mov_b64 v[122:123], 0\n" \
"s_waitcnt vmcnt(1)\n" \
"ds_write_b128 v0, v[36:39] offset:6144\n" \
"s_waitcnt vmcnt(0)\n" \
"ds_write_b128 v0, v[40:43] offset:7168\n" \
"v_smfmac_f32_16x16x64_bf16 v[100:103], v[36:39], a[48:55], v6\n" \
"v_smfmac_f32_16x16x64_bf16 v[100:103], v[40:43], a[56:63], v6\n" \
"v_smfmac_f32_16x16x64_bf16 v[108:111], v[36:39], a[176:183], v6\n" \
"v_smfmac_f32_16x16x64_bf16 v[104:107], v[36:39], a[112:119], v6\n" \
"v_cmp_u_f32 vcc, v100, v101\n" \
"v_cmp_u_f32 s[84:85], v102, v103\n" \
"v_smfmac_f32_16x16x64_bf16 v[112:115], v[36:39], a[240:247], v6\n" \
"s_or_b64 vcc, vcc, s[84:85]\n" \
"s_cbranch_vccnz R3_slowE%=\n" \
"R3_okE%=:\n" \
"s_waitcnt lgkmcnt(0)\n" \
"s_barrier \n" \
"ds_read_b128 v[44:47], v0 offset:0\n" \
"ds_read_b128 v[48:51], v0 offset:1024\n" \
"v_smfmac_f32_16x16x64_bf16 v[108:111], v[40:43], a[184:191], v6\n" \
"ds_read_b128 v[52:55], v0 offset:2048\n" \
"ds_read_b128 v[56:59], v0 offset:3072\n" \
"v_smfmac_f32_16x16x64_bf16 v[104:107], v[40:43], a[120:127], v6\n" \
"ds_read_b128 v[60:63], v0 offset:4096\n" \
"ds_read_b128 v[64:67], v0 offset:5120\n" \
"v_smfmac_f32_16x16x64_bf16 v[112:115], v[40:43], a[248:255], v6\n" \
"v_smfmac_f32_16x16x64_bf16 v[116:119], v[36:39], v[176:183], v6\n" \
"v_smfmac_f32_16x16x64_bf16 v[120:123], v[36:39], v[240:247], v6\n" \
"s_waitcnt lgkmcnt(5)\n" \
"v_smfmac_f32_16x16x64_bf16 v[100:103], v[44:47], a[0:7], v6\n" \
"ds_read_b128 v[68:71], v10 offset:0\n" \
"v_smfmac_f32_16x16x64_bf16 v[108:111], v[44:47], a[128:135], v6\n" \
"ds_read_b128 v[72:75], v10 offset:4096\n" \
"v_smfmac_f32_16x16x64_bf16 v[104:107], v[44:47], a[64:71], v6\n" \
"ds_read_b128 v[76:79], v10 offset:1024\n" \
"v_smfmac_f32_16x16x64_bf16 v[112:115], v[44:47], a[192:199], v6\n" \
"ds_read_b128 v[80:83], v10 offset:5120\n" \
"s_waitcnt lgkmcnt(8)\n" \
"v_smfmac_f32_16x16x64_bf16 v[100:103], v[48:51], a[8:15], v6\n" \
"ds_read_b128 v[84:87], v10 offset:2048\n" \
"v_smfmac_f32_16x16x64_bf16 v[108:111], v[48:51], a[136:143], v6\n" \
"ds_read_b128 v[88:91], v10 offset:6144\n" \
"v_smfmac_f32_16x16x64_bf16 v[104:107], v[48:51], a[72:79], v6\n" \
"ds_read_b128 v[92:95], v10 offset:3072\n" \
"v_smfmac_f32_16x16x64_bf16 v[112:115], v[48:51], a[200:207], v6\n" \
"ds_read_b128 v[96:99], v10 offset:7168\n" \
"s_waitcnt lgkmcnt(11)\n" \
"v_smfmac_f32_16x16x64_bf16 v[100:103], v[52:55], a[16:23], v6\n" \
"v_xor_b32 v10, 16384, v10\n" \
"v_smfmac_f32_16x16x64_bf16 v[108:111], v[52:55], a[144:151], v6\n" \
"s_mov_b32 vcc_lo, 0x33333333\n" \
"v_smfmac_f32_16x16x64_bf16 v[104:107], v[52:55], a[80:87], v6\n" \
"s_mov_b32 vcc_hi, 0x33333333\n" \
"v_smfmac_f32_16x16x64_bf16 v[112:115], v[52:55], a[208:215], v6\n" \
"buffer_store_dwordx4 v[124:127], v17, s[40:43], 0 offen sc1\n" \
"s_waitcnt lgkmcnt(10)\n" \
"v_smfmac_f32_16x16x64_bf16 v[100:103], v[56:59], a[24:31], v6\n" \
"buffer_load_dword v19, v18, s[40:43], 0 offen sc1\n" \
"v_smfmac_f32_16x16x64_bf16 v[108:111], v[56:59], a[152:159], v6\n" \
"buffer_load_dword v16, v14, s[44:47], s31 offen sc1\n" \
"v_smfmac_f32_16x16x64_bf16 v[104:107], v[56:59], a[88:95], v6\n" \
"v_smfmac_f32_16x16x64_bf16 v[112:115], v[56:59], a[216:223], v6\n" \
"s_waitcnt lgkmcnt(9)\n" \
"v_smfmac_f32_16x16x64_bf16 v[100:103], v[60:63], a[32:39], v6\n" \
"v_smfmac_f32_16x16x64_bf16 v[108:111], v[60:63], a[160:167], v6\n" \
"v_smfmac_f32_16x16x64_bf16 v[104:107], v[60:63], a[96:103], v6\n" \
"v_smfmac_f32_16x16x64_bf16 v[112:115], v[60:63], a[224:231], v6\n" \
"s_waitcnt lgkmcnt(8)\n" \
"v_smfmac_f32_16x16x64_bf16 v[100:103], v[64:67], a[40:47], v6\n" \
"v_smfmac_f32_16x16x64_bf16 v[108:111], v[64:67], a[168:175], v6\n" \
"v_smfmac_f32_16x16x64_bf16 v[104:107], v[64:67], a[104:111], v6\n" \
"v_smfmac_f32_16x16x64_bf16 v[112:115], v[64:67], a[232:239], v6\n" \
"v_add_f32 v100, v100, v102\n" \
"v_add_f32 v101, v101, v103\n" \
"v_cvt_pk_bf16_f32 v124, v100, v101\n" \
"v_add_f32 v108, v108, v110\n" \
"v_add_f32 v109, v109, v111\n" \
"v_cvt_pk_bf16_f32 v125, v108, v109\n" \
"v_pk_add_f32 v[104:105], v[104:105], v[106:107]\n" \
"v_cvt_pk_bf16_f32 v126, v104, v105\n" \
"v_pk_add_f32 v[112:113], v[112:113], v[114:115]\n" \
"v_cvt_pk_bf16_f32 v127, v112, v113\n" \
"v_smfmac_f32_16x16x64_bf16 v[116:119], v[40:43], v[184:191], v6\n" \
"ds_write_b128 v8, v[124:127]\n" \
"v_smfmac_f32_16x16x64_bf16 v[120:123], v[40:43], v[248:255], v6\n" \
"v_smfmac_f32_16x16x64_bf16 v[116:119], v[44:47], v[128:135], v6\n" \
"v_smfmac_f32_16x16x64_bf16 v[120:123], v[44:47], v[192:199], v6\n" \
"v_smfmac_f32_16x16x64_bf16 v[116:119], v[48:51], v[136:143], v6\n" \
"s_waitcnt lgkmcnt(0)\n" \
"s_barrier \n" \
"ds_read_b64_tr_b16 v[36:37], v9 offset:0\n" \
"ds_read_b64_tr_b16 v[38:39], v9 offset:64\n" \
"v_smfmac_f32_16x16x64_bf16 v[120:123], v[48:51], v[200:207], v6\n" \
"ds_read_b64_tr_b16 v[40:41], v9 offset:256\n" \
"ds_read_b64_tr_b16 v[42:43], v9 offset:320\n" \
"v_smfmac_f32_16x16x64_bf16 v[116:119], v[52:55], v[144:151], v6\n" \
"ds_read_b64_tr_b16 v[124:125], v9 offset:512\n" \
"ds_read_b64_tr_b16 v[126:127], v9 offset:576\n" \
"v_smfmac_f32_16x16x64_bf16 v[120:123], v[52:55], v[208:215], v6\n" \
"ds_read_b64_tr_b16 v[112:113], v9 offset:768\n" \
"ds_read_b64_tr_b16 v[114:115], v9 offset:832\n" \
"v_smfmac_f32_16x16x64_bf16 v[116:119], v[56:59], v[152:159], v6\n" \
"s_waitcnt lgkmcnt(6)\n" \
"v_mfma_f32_16x16x32_bf16 v[100:103], v[68:71], v[36:39], 0\n" \
"v_mfma_f32_16x16x32_bf16 v[104:107], v[72:75], v[36:39], 0\n" \
"v_mov_b32 v68, 0x3f803f80\n" \
"v_mov_b32 v69, 0x3f803f80\n" \
"v_mfma_f32_16x16x32_bf16 v[108:111], v[36:39], v[36:39], 0\n" \
"v_mov_b32 v70, 0x3f80\n" \
"v_mov_b32 v71, 0x3f80\n" \
"s_waitcnt lgkmcnt(4)\n" \
"v_mfma_f32_16x16x32_bf16 v[100:103], v[76:79], v[40:43], v[100:103]\n" \
"v_mov_b32 v72, 0x3f803f80\n" \
"v_mov_b32 v73, 0x3f803f80\n" \
"v_mfma_f32_16x16x32_bf16 v[104:107], v[80:83], v[40:43], v[104:107]\n" \
"v_mov_b32 v74, 0x3f80\n" \
"v_mov_b32 v75, 0x3f80\n" \
"v_mfma_f32_16x16x32_bf16 v[108:111], v[40:43], v[40:43], v[108:111]\n" \
"v_mov_b64 v[76:77], 0\n" \
"v_mov_b64 v[78:79], 0\n" \
"s_waitcnt lgkmcnt(2)\n" \
"v_mfma_f32_16x16x32_bf16 v[100:103], v[84:87], v[124:127], v[100:103]\n" \
"v_mov_b64 v[80:81], 0\n" \
"v_mov_b64 v[82:83], 0\n" \
"v_mfma_f32_16x16x32_bf16 v[104:107], v[88:91], v[124:127], v[104:107]\n" \
"v_mov_b64 v[84:85], 0\n" \
"v_mov_b64 v[86:87], 0\n" \
"v_mfma_f32_16x16x32_bf16 v[108:111], v[124:127], v[124:127], v[108:111]\n" \
"s_waitcnt lgkmcnt(0)\n" \
"v_mfma_f32_16x16x32_bf16 v[100:103], v[92:95], v[112:115], v[100:103]\n" \
"v_mfma_f32_16x16x32_bf16 v[104:107], v[96:99], v[112:115], v[104:107]\n" \
"v_mfma_f32_16x16x32_bf16 v[108:111], v[112:115], v[112:115], v[108:111]\n" \
"v_cndmask_b32_dpp v100, v102, v100, vcc quad_perm:[0,1,0,1] row_mask:0xf bank_mask:0xf\n" \
"v_cndmask_b32_dpp v101, v103, v101, vcc quad_perm:[0,1,0,1] row_mask:0xf bank_mask:0xf\n" \
"v_smfmac_f32_16x16x64_bf16 v[120:123], v[56:59], v[216:223], v6\n" \
"v_cndmask_b32_dpp v104, v106, v104, vcc quad_perm:[0,1,0,1] row_mask:0xf bank_mask:0xf\n" \
"v_cndmask_b32_dpp v105, v107, v105, vcc quad_perm:[0,1,0,1] row_mask:0xf bank_mask:0xf\n" \
"v_max3_f32 v92, v100, v101, v104\n" \
"s_nop 0\n" \
"v_cndmask_b32_dpp v110, v111, v110, vcc quad_perm:[0,1,0,1] row_mask:0xf bank_mask:0xf\n" \
"v_max3_f32 v92, v92, v105, v110\n" \
"v_smfmac_f32_16x16x64_bf16 v[116:119], v[60:63], v[160:167], v6\n" \
"s_nop 0\n" \
"v_max_f32_dpp v93, v92, v92 quad_perm:[2,3,0,1] row_mask:0xf bank_mask:0xf\n" \
"v_max_f32_dpp v92, v92, v92 quad_perm:[2,3,0,1] row_mask:0xf bank_mask:0xf\n" \
"v_smfmac_f32_16x16x64_bf16 v[120:123], v[60:63], v[224:231], v6\n" \
"s_nop 0\n" \
"v_permlane16_swap_b32 v92, v93\n" \
"v_max_f32 v94, v92, v93\n" \
"v_max_f32 v92, v92, v93\n" \
"v_smfmac_f32_16x16x64_bf16 v[116:119], v[64:67], v[168:175], v6\n" \
"s_nop 0\n" \
"v_permlane32_swap_b32 v92, v94\n" \
"v_max_f32 v92, v92, v94\n" \
"v_mul_f32 v94, s81, v92\n" \
"v_fma_f32 v110, v110, s80, v94\n" \
"v_pk_fma_f32 v[100:101], v[100:101], s[80:81], v[94:95] op_sel_hi:[1,0,0]\n" \
"v_pk_fma_f32 v[104:105], v[104:105], s[80:81], v[94:95] op_sel_hi:[1,0,0]\n" \
"v_smfmac_f32_16x16x64_bf16 v[120:123], v[64:67], v[232:239], v6\n" \
"v_exp_f32 v110, v110\n" \
"v_exp_f32 v100, v100\n" \
"v_exp_f32 v101, v101\n" \
"v_exp_f32 v104, v104\n" \
"v_exp_f32 v105, v105\n" \
"v_cvt_pk_bf16_f32 v91, v110, v110\n" \
"v_cvt_pk_bf16_f32 v88, v100, v101\n" \
"v_and_b32 v89, v12, v91\n" \
"v_and_b32 v91, v13, v91\n" \
"v_cvt_pk_bf16_f32 v90, v104, v105\n" \
"v_cvt_pk_bf16_f32 v22, v116, v118\n" \
"v_cvt_pk_bf16_f32 v23, v117, v119\n" \
"v_smfmac_f32_16x16x64_bf16 v[76:79], v[88:91], v[68:75], v6\n" \
"v_mov_b64 v[26:27], v[22:23]\n" \
"v_cvt_pk_bf16_f32 v30, v120, v122\n" \
"v_cvt_pk_bf16_f32 v31, v121, v123\n" \
"v_smfmac_f32_16x16x64_bf16 v[80:83], v[88:91], v[20:27], v6\n" \
"v_mov_b64 v[34:35], v[30:31]\n" \
"s_nop 1\n" \
"v_smfmac_f32_16x16x64_bf16 v[84:87], v[88:91], v[28:35], v6\n" \
"v_pk_add_f32 v[76:77], v[76:77], v[78:79]\n" \
"v_rcp_f32 v76, v76\n" \
"v_rcp_f32 v77, v77\n" \
"s_nop 0\n" \
"v_pk_add_f32 v[80:81], v[80:81], v[82:83]\n" \
"v_pk_mul_f32 v[80:81], v[80:81], v[76:77]\n" \
"v_cvt_pk_bf16_f32 v116, v80, v81\n" \
"v_min_u32 v116, -2, v116\n" \
"v_pk_add_f32 v[84:85], v[84:85], v[86:87]\n" \
"v_pk_mul_f32 v[84:85], v[84:85], v[76:77]\n" \
"v_cvt_pk_bf16_f32 v117, v84, v85\n" \
"v_min_u32 v117, -2, v117\n" \
"buffer_store_dwordx2 v[116:117], v2, s[40:43], s73 offen sc1\n" \
"s_add_u32 s71, s71, 65536\n" \
"s_add_u32 s70, s70, 1\n" \
"s_waitcnt vmcnt(1)\n" \
"s_mov_b32 s83, 0\n" \
"R3_fw%=:\n" \
"v_readfirstlane_b32 s75, v16\n" \
"s_nop 0\n" \
"s_cmp_eq_u32 s75, 0\n" \
"s_cbranch_scc1 R3_fww%=\n" \
"R3_fwx%=:\n" \
"s_add_u32 s31, s31, 2048\n" \
"s_min_u32 s31, s31, 126976\n" \
"ds_read_b128 v[68:71], v11 offset:0\n" \
"ds_read_b128 v[76:79], v15 offset:0\n" \
"ds_read_b128 v[80:83], v15 offset:4096\n" \
"ds_read_b128 v[72:75], v11 offset:128\n" \
"ds_read_b128 v[84:87], v15 offset:16384\n" \
"ds_read_b128 v[88:91], v15 offset:20480\n" \
"ds_read_b128 v[44:47], v11 offset:1024\n" \
"ds_read_b128 v[52:55], v15 offset:1024\n" \
"ds_read_b128 v[56:59], v15 offset:5120\n" \
"ds_read_b128 v[48:51], v11 offset:1152\n" \
"ds_read_b128 v[60:63], v15 offset:17408\n" \
"ds_read_b128 v[64:67], v15 offset:21504\n" \
"s_waitcnt lgkmcnt(10)\n" \
"v_mfma_f32_16x16x32_bf16 v[36:39], v[68:71], v[76:79], 0\n" \
"s_waitcnt lgkmcnt(9)\n" \
"v_mfma_f32_16x16x32_bf16 v[40:43], v[68:71], v[80:83], 0\n" \
"s_waitcnt lgkmcnt(8)\n" \
"v_mfma_f32_16x16x32_bf16 v[92:95], v[72:75], v[76:79], 0\n" \
"v_mfma_f32_16x16x32_bf16 v[96:99], v[72:75], v[80:83], 0\n" \
"s_waitcnt lgkmcnt(7)\n" \
"v_mfma_f32_16x16x32_bf16 v[100:103], v[68:71], v[84:87], 0\n" \
"s_waitcnt lgkmcnt(6)\n" \
"v_mfma_f32_16x16x32_bf16 v[104:107], v[68:71], v[88:91], 0\n" \
"v_mfma_f32_16x16x32_bf16 v[108:111], v[72:75], v[84:87], 0\n" \
"v_mfma_f32_16x16x32_bf16 v[112:115], v[72:75], v[88:91], 0\n" \
"ds_read_b128 v[68:71], v11 offset:2048\n" \
"ds_read_b128 v[76:79], v15 offset:2048\n" \
"ds_read_b128 v[80:83], v15 offset:6144\n" \
"ds_read_b128 v[72:75], v11 offset:2176\n" \
"ds_read_b128 v[84:87], v15 offset:18432\n" \
"ds_read_b128 v[88:91], v15 offset:22528\n" \
"s_waitcnt lgkmcnt(10)\n" \
"v_mfma_f32_16x16x32_bf16 v[36:39], v[44:47], v[52:55], v[36:39]\n" \
"s_waitcnt lgkmcnt(9)\n" \
"v_mfma_f32_16x16x32_bf16 v[40:43], v[44:47], v[56:59], v[40:43]\n" \
"s_waitcnt lgkmcnt(8)\n" \
"v_mfma_f32_16x16x32_bf16 v[92:95], v[48:51], v[52:55], v[92:95]\n" \
"v_mfma_f32_16x16x32_bf16 v[96:99], v[48:51], v[56:59], v[96:99]\n" \
"s_waitcnt lgkmcnt(7)\n" \
"v_mfma_f32_16x16x32_bf16 v[100:103], v[44:47], v[60:63], v[100:103]\n" \
"s_waitcnt lgkmcnt(6)\n" \
"v_mfma_f32_16x16x32_bf16 v[104:107], v[44:47], v[64:67], v[104:107]\n" \
"v_mfma_f32_16x16x32_bf16 v[108:111], v[48:51], v[60:63], v[108:111]\n" \
"v_mfma_f32_16x16x32_bf16 v[112:115], v[48:51], v[64:67], v[112:115]\n" \
"ds_read_b128 v[44:47], v11 offset:3072\n" \
"ds_read_b128 v[52:55], v15 offset:3072\n" \
"ds_read_b128 v[56:59], v15 offset:7168\n" \
"ds_read_b128 v[48:51], v11 offset:3200\n" \
"ds_read_b128 v[60:63], v15 offset:19456\n" \
"ds_read_b128 v[64:67], v15 offset:23552\n" \
"s_waitcnt lgkmcnt(10)\n" \
"v_mfma_f32_16x16x32_bf16 v[36:39], v[68:71], v[76:79], v[36:39]\n" \
"s_waitcnt lgkmcnt(9)\n" \
"v_mfma_f32_16x16x32_bf16 v[40:43], v[68:71], v[80:83], v[40:43]\n" \
"s_waitcnt lgkmcnt(8)\n" \
"v_mfma_f32_16x16x32_bf16 v[92:95], v[72:75], v[76:79], v[92:95]\n" \
"v_mfma_f32_16x16x32_bf16 v[96:99], v[72:75], v[80:83], v[96:99]\n" \
"s_waitcnt lgkmcnt(7)\n" \
"v_mfma_f32_16x16x32_bf16 v[100:103], v[68:71], v[84:87], v[100:103]\n" \
"s_waitcnt lgkmcnt(6)\n" \
"v_mfma_f32_16x16x32_bf16 v[104:107], v[68:71], v[88:91], v[104:107]\n" \
"v_mfma_f32_16x16x32_bf16 v[108:111], v[72:75], v[84:87], v[108:111]\n" \
"v_mfma_f32_16x16x32_bf16 v[112:115], v[72:75], v[88:91], v[112:115]\n" \
"buffer_load_dwordx4 v[116:119], v5, s[44:47], s74 offen offset:0 sc1\n" \
"buffer_load_dwordx4 v[120:123], v5, s[44:47], s74 offen offset:1024 sc1\n" \
"buffer_load_dwordx4 v[124:127], v5, s[44:47], s74 offen offset:2048 sc1\n" \
"buffer_load_dwordx4 v[16:19], v5, s[44:47], s74 offen offset:3072 sc1\n" \
"s_add_u32 s74, s74, 524288\n" \
"s_min_u32 s74, s74, 32505856\n" \
"s_waitcnt lgkmcnt(4)\n" \
"v_mfma_f32_16x16x32_bf16 v[36:39], v[44:47], v[52:55], v[36:39]\n" \
"s_waitcnt lgkmcnt(3)\n" \
"v_mfma_f32_16x16x32_bf16 v[40:43], v[44:47], v[56:59], v[40:43]\n" \
"s_waitcnt lgkmcnt(2)\n" \
"v_mfma_f32_16x16x32_bf16 v[92:95], v[48:51], v[52:55], v[92:95]\n" \
"v_mfma_f32_16x16x32_bf16 v[96:99], v[48:51], v[56:59], v[96:99]\n" \
"s_waitcnt lgkmcnt(1)\n" \
"v_mfma_f32_16x16x32_bf16 v[100:103], v[44:47], v[60:63], v[100:103]\n" \
"s_waitcnt lgkmcnt(0)\n" \
"v_mfma_f32_16x16x32_bf16 v[104:107], v[44:47], v[64:67], v[104:107]\n" \
"v_mfma_f32_16x16x32_bf16 v[108:111], v[48:51], v[60:63], v[108:111]\n" \
"v_mfma_f32_16x16x32_bf16 v[112:115], v[48:51], v[64:67], v[112:115]\n" \
"v_xor_b32 v11, 16384, v11\n" \
"v_cvt_pk_bf16_f32 v68, v36, v37\n" \
"v_cvt_pk_bf16_f32 v69, v38, v39\n" \
"v_cvt_pk_bf16_f32 v70, v92, v93\n" \
"v_cvt_pk_bf16_f32 v71, v94, v95\n" \
"ds_write_b128 v3, v[68:71] offset:0\n" \
"v_cvt_pk_bf16_f32 v72, v40, v41\n" \
"v_cvt_pk_bf16_f32 v73, v42, v43\n" \
"v_cvt_pk_bf16_f32 v74, v96, v97\n" \
"v_cvt_pk_bf16_f32 v75, v98, v99\n" \
"ds_write_b128 v3, v[72:75] offset:1024\n" \
"v_cvt_pk_bf16_f32 v20, v100, v101\n" \
"v_cvt_pk_bf16_f32 v21, v102, v103\n" \
"v_cvt_pk_bf16_f32 v28, v104, v105\n" \
"v_cvt_pk_bf16_f32 v29, v106, v107\n" \
"v_cvt_pk_bf16_f32 v24, v108, v109\n" \
"v_cvt_pk_bf16_f32 v25, v110, v111\n" \
"v_cvt_pk_bf16_f32 v32, v112, v113\n" \
"v_cvt_pk_bf16_f32 v33, v114, v115\n" \
"s_add_u32 s73, s73, 524288\n" \
"s_and_b32 s73, s73, 2097151\n" \
"s_lshr_b32 s75, s73, 7\n" \
"s_and_b32 s75, s75, 0x1000\n" \
"s_xor_b32 s75, s75, 0x5000\n" \
"v_xor_b32 v2, s75, v2\n" \
"s_sub_u32 s75, s70, 1\n" \
"s_xor_b32 s75, s75, s70\n" \
"s_bitcmp1_b32 s75, 5\n" \
"s_cselect_b32 s76, s72, 0\n" \
"s_lshl_b32 s38, s75, 11\n" \
"s_and_b32 s38, s38, 0x4000\n" \
"s_xor_b32 s76, s76, s38\n" \
"s_lshl_b32 s38, s75, 8\n" \
"s_and_b32 s38, s38, 0x1000\n" \
"s_xor_b32 s76, s76, s38\n" \
"v_xor_b32 v1, s76, v1\n" \
"s_nop 1\n" \
"s_cmp_ge_u32 s70, 63\n" \
"s_cbranch_scc1 R_done%=\n" \
"s_mov_b32 s83, 0\n" \
"R3_ck%=:\n" \
"ds_read_b64 v[100:101], v7 offset:16\n" \
"s_add_u32 s83, s83, 1\n" \
"v_mov_b32 v7, v7\n" \
"s_nop 2\n" \
"v_mov_b32 v7, v7\n" \
"s_nop 2\n" \
"v_mov_b32 v7, v7\n" \
"s_nop 2\n" \
"s_waitcnt lgkmcnt(0)\n" \
"v_readfirstlane_b32 s75, v100\n" \
"v_readfirstlane_b32 s34, v101\n" \
"s_cmp_eq_u32 s75, s70\n" \
"s_cbranch_scc1 R3_w2%=\n" \
"s_cmp_lt_u32 s83, 1048576\n" \
"s_cbranch_scc1 R3_ck%=\n" \
"s_branch R3_loop%=\n" \
"R3_fww%=:\n" \
"s_add_u32 s83, s83, 1\n" \
"s_cmp_gt_u32 s83, 65536\n" \
"s_cselect_b32 s77, 1, s77\n" \
"s_cmp_lg_u32 s77, 0\n" \
"s_cbranch_scc1 R3_fwx%=\n" \
"s_sleep 5\n" \
"s_waitcnt vmcnt(0)\n" \
"buffer_load_dword v16, v14, s[44:47], s31 offen sc1\n" \
"s_waitcnt vmcnt(0)\n" \
"s_branch R3_fw%=\n" \
"R3_slowE%=:\n" \
"s_nop 7\n" \
"s_mov_b32 s83, 0\n" \
"R3_slowE1%=:\n" \
"v_max3_u32 v52, v36, v37, v38\n" \
"v_max3_u32 v52, v52, v39, v40\n" \
"v_max3_u32 v52, v52, v41, v42\n" \
"v_max_u32 v52, v52, v43\n" \
"v_cmp_eq_u32 vcc, -1, v52\n" \
"s_cbranch_vccnz R3_slowE2%=\n" \
"s_cmp_eq_u32 s83, 0\n" \
"s_cbranch_scc1 R3_okE%=\n" \
"ds_write_b128 v0, v[36:39] offset:6144\n" \
"ds_write_b128 v0, v[40:43] offset:7168\n" \
"v_mov_b64 v[100:101], 0\n" \
"v_mov_b64 v[102:103], 0\n" \
"v_mov_b64 v[104:105], 0\n" \
"v_mov_b64 v[106:107], 0\n" \
"v_mov_b64 v[108:109], 0\n" \
"v_mov_b64 v[110:111], 0\n" \
"v_mov_b64 v[112:113], 0\n" \
"v_mov_b64 v[114:115], 0\n" \
"v_smfmac_f32_16x16x64_bf16 v[100:103], v[36:39], a[48:55], v6\n" \
"v_smfmac_f32_16x16x64_bf16 v[100:103], v[40:43], a[56:63], v6\n" \
"v_smfmac_f32_16x16x64_bf16 v[108:111], v[36:39], a[176:183], v6\n" \
"v_smfmac_f32_16x16x64_bf16 v[104:107], v[36:39], a[112:119], v6\n" \
"v_smfmac_f32_16x16x64_bf16 v[112:115], v[36:39], a[240:247], v6\n" \
"s_branch R3_okE%=\n" \
"R3_slowE2%=:\n" \
"s_add_u32 s83, s83, 1\n" \
"s_add_u32 s78, s78, 1\n" \
"s_cmp_gt_u32 s83, 65536\n" \
"s_cselect_b32 s77, 1, s77\n" \
"s_cmp_lg_u32 s77, 0\n" \
"s_cbranch_scc1 R3_okE%=\n" \
"s_sleep 2\n" \
"s_mov_b64 exec, vcc\n" \
"buffer_load_dwordx4 v[36:39], v1, s[40:43], s71 offen offset:0 sc1\n" \
"buffer_load_dwordx4 v[40:43], v1, s[40:43], s71 offen offset:1024 sc1\n" \
"s_mov_b64 exec, -1\n" \
"s_waitcnt vmcnt(0)\n" \
"s_branch R3_slowE1%=\n" \
"R_done%=:\n" \
"s_waitcnt lgkmcnt(0)\n" \
"ROLE_END%=:\n" \
"RSCAN_CODE_END:\n"
#define ROLE_CLOB "v1", "v2", "v3", "v4", "v5", "v6", "v7", "v8", "v9", "v10", "v11", "v12", "v13", "v14", "v15", "v16", "v17", "v18", "v19", "v20", "v21", "v22", "v23", "v24", "v25", "v26", "v27", "v28", "v29", "v30", "v31", "v32", "v33", "v34", "v35", "v36", "v37", "v38", "v39", "v40", "v41", "v42", "v43", "v44", "v45", "v46", "v47", "v48", "v49", "v50", "v51", "v52", "v53", "v54", "v55", "v56", "v57", "v58", "v59", "v60", "v61", "v62", "v63", "v64", "v65", "v66", "v67", "v68", "v69", "v70", "v71", "v72", "v73", "v74", "v75", "v76", "v77", "v78", "v79", "v80", "v81", "v82", "v83", "v84", "v85", "v86", "v87", "v88", "v89", "v90", "v91", "v92", "v93", "v94", "v95", "v96", "v97", "v98", "v99", "v100", "v101", "v102", "v103", "v104", "v105", "v106", "v107", "v108", "v109", "v110", "v111", "v112", "v113", "v114", "v115", "v116", "v117", "v118", "v119", "v120", "v121", "v122", "v123", "v124", "v125", "v126", "v127", "v128", "v129", "v130", "v131", "v132", "v133", "v134", "v135", "v136", "v137", "v138", "v139", "v140", "v141", "v142", "v143", "v144", "v145", "v146", "v147", "v148", "v149", "v150", "v151", "v152", "v153", "v154", "v155", "v156", "v157", "v158", "v159", "v160", "v161", "v162", "v163", "v164", "v165", "v166", "v167", "v168", "v169", "v170", "v171", "v172", "v173", "v174", "v175", "v176", "v177", "v178", "v179", "v180", "v181", "v182", "v183", "v184", "v185", "v186", "v187", "v188", "v189", "v190", "v191", "v192", "v193", "v194", "v195", "v196", "v197", "v198", "v199", "v200", "v201", "v202", "v203", "v204", "v205", "v206", "v207", "v208", "v209", "v210", "v211", "v212", "v213", "v214", "v215", "v216", "v217", "v218", "v219", "v220", "v221", "v222", "v223", "v224", "v225", "v226", "v227", "v228", "v229", "v230", "v231", "v232", "v233", "v234", "v235", "v236", "v237", "v238", "v239", "v240", "v241", "v242", "v243", "v244", "v245", "v246", "v247", "v248", "v249", "v250", "v251", "v252", "v253", "v254", "v255", "a0", "a1", "a2", "a3", "a4", "a5", "a6", "a7", "a8", "a9", "a10", "a11", "a12", "a13", "a14", "a15", "a16", "a17", "a18", "a19", "a20", "a21", "a22", "a23", "a24", "a25", "a26", "a27", "a28", "a29", "a30", "a31", "a32", "a33", "a34", "a35", "a36", "a37", "a38", "a39", "a40", "a41", "a42", "a43", "a44", "a45", "a46", "a47", "a48", "a49", "a50", "a51", "a52", "a53", "a54", "a55", "a56", "a57", "a58", "a59", "a60", "a61", "a62", "a63", "a64", "a65", "a66", "a67", "a68", "a69", "a70", "a71", "a72", "a73", "a74", "a75", "a76", "a77", "a78", "a79", "a80", "a81", "a82", "a83", "a84", "a85", "a86", "a87", "a88", "a89", "a90", "a91", "a92", "a93", "a94", "a95", "a96", "a97", "a98", "a99", "a100", "a101", "a102", "a103", "a104", "a105", "a106", "a107", "a108", "a109", "a110", "a111", "a112", "a113", "a114", "a115", "a116", "a117", "a118", "a119", "a120", "a121", "a122", "a123", "a124", "a125", "a126", "a127", "a128", "a129", "a130", "a131", "a132", "a133", "a134", "a135", "a136", "a137", "a138", "a139", "a140", "a141", "a142", "a143", "a144", "a145", "a146", "a147", "a148", "a149", "a150", "a151", "a152", "a153", "a154", "a155", "a156", "a157", "a158", "a159", "a160", "a161", "a162", "a163", "a164", "a165", "a166", "a167", "a168", "a169", "a170", "a171", "a172", "a173", "a174", "a175", "a176", "a177", "a178", "a179", "a180", "a181", "a182", "a183", "a184", "a185", "a186", "a187", "a188", "a189", "a190", "a191", "a192", "a193", "a194", "a195", "a196", "a197", "a198", "a199", "a200", "a201", "a202", "a203", "a204", "a205", "a206", "a207", "a208", "a209", "a210", "a211", "a212", "a213", "a214", "a215", "a216", "a217", "a218", "a219", "a220", "a221", "a222", "a223", "a224", "a225", "a226", "a227", "a228", "a229", "a230", "a231", "a232", "a233", "a234", "a235", "a236", "a237", "a238", "a239", "a240", "a241", "a242", "a243", "a244", "a245", "a246", "a247", "a248", "a249", "a250", "a251", "a252", "a253", "a254", "a255", "s0", "s1", "s2", "s3", "s5", "s6", "s8", "s9", "s10", "s11", "s12", "s13", "s14", "s15", "s16", "s17", "s18", "s19", "s20", "s21", "s22", "s23", "s24", "s25", "s26", "s27", "s28", "s29", "s30", "s31", "s34", "s35", "s36", "s37", "s38", "s39", "s70", "s71", "s72", "s73", "s74", "s75", "s76", "s77", "s78", "s79", "s80", "s81", "s82", "s83", "s84", "s85", "s86", "s87", "vcc", "scc", "memory"
#define S1R0 "s_add_u32 m0, %[base], 24576\n" "s_mov_b32 %[t], 0\n" "buffer_load_dwordx4 %[va], %[da], %[t] offen lds\n" "s_add_u32 m0, %[base], 25600\n" "s_mov_b32 %[t], 16384\n" "buffer_load_dwordx4 %[va], %[da], %[t] offen lds\n" "s_add_u32 m0, %[base], 26624\n" "s_mov_b32 %[t], 32768\n" "buffer_load_dwordx4 %[va], %[da], %[t] offen lds\n" "s_add_u32 m0, %[base], 27648\n" "s_mov_b32 %[t], 49152\n" "buffer_load_dwordx4 %[va], %[da], %[t] offen lds\n"
#define S1R1 "s_add_u32 m0, %[base], 28672\n" "s_mov_b32 %[t], 65536\n" "buffer_load_dwordx4 %[va], %[da], %[t] offen lds\n" "s_add_u32 m0, %[base], 29696\n" "s_mov_b32 %[t], 81920\n" "buffer_load_dwordx4 %[va], %[da], %[t] offen lds\n" "s_add_u32 m0, %[base], 30720\n" "s_mov_b32 %[t], 98304\n" "buffer_load_dwordx4 %[va], %[da], %[t] offen lds\n" "s_add_u32 m0, %[base], 31744\n" "s_mov_b32 %[t], 114688\n" "buffer_load_dwordx4 %[va], %[da], %[t] offen lds\n"
#define S1R2 "s_add_u32 m0, %[base], 0\n" "s_mov_b32 %[t], 0\n" "buffer_load_dwordx4 %[va], %[da], %[t] offen lds\n" "s_add_u32 m0, %[base], 1024\n" "s_mov_b32 %[t], 64\n" "buffer_load_dwordx4 %[va], %[da], %[t] offen lds\n" "s_add_u32 m0, %[base], 2048\n" "s_mov_b32 %[t], 262144\n" "buffer_load_dwordx4 %[va], %[da], %[t] offen lds\n" "s_add_u32 m0, %[base], 3072\n" "s_mov_b32 %[t], 262208\n" "buffer_load_dwordx4 %[va], %[da], %[t] offen lds\n"
#define S1R3 "s_add_u32 m0, %[base], 4096\n" "s_mov_b32 %[t], 524288\n" "buffer_load_dwordx4 %[va], %[da], %[t] offen lds\n" "s_add_u32 m0, %[base], 5120\n" "s_mov_b32 %[t], 524352\n" "buffer_load_dwordx4 %[va], %[da], %[t] offen lds\n" "s_add_u32 m0, %[base], 6144\n" "s_mov_b32 %[t], 786432\n" "buffer_load_dwordx4 %[va], %[da], %[t] offen lds\n" "s_add_u32 m0, %[base], 7168\n" "s_mov_b32 %[t], 786496\n" "buffer_load_dwordx4 %[va], %[da], %[t] offen lds\n"
#define S1R4 "s_add_u32 m0, %[base], 8192\n" "s_mov_b32 %[t], 1048576\n" "buffer_load_dwordx4 %[va], %[da], %[t] offen lds\n" "s_add_u32 m0, %[base], 9216\n" "s_mov_b32 %[t], 1048640\n" "buffer_load_dwordx4 %[va], %[da], %[t] offen lds\n" "s_add_u32 m0, %[base], 10240\n" "s_mov_b32 %[t], 1310720\n" "buffer_load_dwordx4 %[va], %[da], %[t] offen lds\n" "s_add_u32 m0, %[base], 11264\n" "s_mov_b32 %[t], 1310784\n" "buffer_load_dwordx4 %[va], %[da], %[t] offen lds\n"
#define S1R5 "s_add_u32 m0, %[base], 12288\n" "s_mov_b32 %[t], 1572864\n" "buffer_load_dwordx4 %[va], %[da], %[t] offen lds\n" "s_add_u32 m0, %[base], 13312\n" "s_mov_b32 %[t], 1572928\n" "buffer_load_dwordx4 %[va], %[da], %[t] offen lds\n" "s_add_u32 m0, %[base], 14336\n" "s_mov_b32 %[t], 1835008\n" "buffer_load_dwordx4 %[va], %[da], %[t] offen lds\n" "s_add_u32 m0, %[base], 15360\n" "s_mov_b32 %[t], 1835072\n" "buffer_load_dwordx4 %[va], %[da], %[t] offen lds\n"
#define S1R6 "s_add_u32 m0, %[base], 16384\n" "s_mov_b32 %[t], 2097152\n" "buffer_load_dwordx4 %[va], %[da], %[t] offen lds\n" "s_add_u32 m0, %[base], 17408\n" "s_mov_b32 %[t], 2097216\n" "buffer_load_dwordx4 %[va], %[da], %[t] offen lds\n" "s_add_u32 m0, %[base], 18432\n" "s_mov_b32 %[t], 2359296\n" "buffer_load_dwordx4 %[va], %[da], %[t] offen lds\n" "s_add_u32 m0, %[base], 19456\n" "s_mov_b32 %[t], 2359360\n" "buffer_load_dwordx4 %[va], %[da], %[t] offen lds\n"
#define S1R7 "s_add_u32 m0, %[base], 20480\n" "s_mov_b32 %[t], 2621440\n" "buffer_load_dwordx4 %[va], %[da], %[t] offen lds\n" "s_add_u32 m0, %[base], 21504\n" "s_mov_b32 %[t], 2621504\n" "buffer_load_dwordx4 %[va], %[da], %[t] offen lds\n" "s_add_u32 m0, %[base], 22528\n" "s_mov_b32 %[t], 2883584\n" "buffer_load_dwordx4 %[va], %[da], %[t] offen lds\n" "s_add_u32 m0, %[base], 23552\n" "s_mov_b32 %[t], 2883648\n" "buffer_load_dwordx4 %[va], %[da], %[t] offen lds\n"
#define S1G "s_mov_b32 m0, %[base]\n" "s_add_u32 %[base], %[base], 1024\n" "buffer_load_dwordx4 %[va], %[da], %[off] offen lds\n" "s_add_u32 %[off], %[off], %[step]\n" "s_mov_b32 m0, %[base]\n" "s_add_u32 %[base], %[base], 1024\n" "buffer_load_dwordx4 %[va], %[da], %[off] offen lds\n" "s_add_u32 %[off], %[off], %[step]\n" "s_mov_b32 m0, %[base]\n" "s_add_u32 %[base], %[base], 1024\n" "buffer_load_dwordx4 %[va], %[da], %[off] offen lds\n" "s_add_u32 %[off], %[off], %[step]\n" "s_mov_b32 m0, %[base]\n" "s_add_u32 %[base], %[base], 1024\n" "buffer_load_dwordx4 %[va], %[da], %[off] offen lds\n" "s_add_u32 %[off], %[off], %[step]\n"

constexpr unsigned MB = 1u << 20;
constexpr unsigned OFF_XF = 6 * MB, OFF_KV = 8 * MB, OFF_OF = 42 * MB, OFF_HF = 58 * MB, OFF_G = 64 * MB, OFF_CTL = 66 * MB, WS_BYTES = 68 * MB;
constexpr unsigned OFF_FLAG = OFF_CTL, OFF_CNT = OFF_CTL + 131072, OFF_CLAIM = OFF_CNT + 16384, OFF_PAR = OFF_CLAIM + 20480, CTL_ZERO = 131072 + 16384 + 20480;
#define WDELAY 7
constexpr unsigned PROBE_TICKS = 330, LB = 65536, OOB = 0x7fffff00u, SENT = 0xFFFFFFFFu;
struct Args { const u16 *x, *mem0, *Wq, *Wk, *Wv, *Wo, *w1, *b1, *w2, *b2; u16* out; unsigned char* ws; unsigned* dbg; int g1, g2, rho, tol; };
DEV u32x4 srd(const void* p, unsigned bytes) { const unsigned long long a = (unsigned long long)p; return (u32x4){(unsigned)a, (unsigned)(a >> 32) & 0xffffu, bytes, 0x00020000u}; }
DEV unsigned pk(float a, float b) { f32x2 v = {a, b}; return __builtin_bit_cast(unsigned, __builtin_convertvector(v, bf16x2)); }
DEV float rb(float f) { return __uint_as_float(pk(f, f) << 16); }
DEV unsigned xcc_id() { return (unsigned)__builtin_amdgcn_s_getreg(20 | (3 << 11)) & 7u; }

__global__ void __launch_bounds__(1024) prep_kernel(Args p) {
  extern __shared__ __attribute__((aligned(16))) u32x4 xt[];
  const int b = blockIdx.x - 48, tid = threadIdx.x, lane = tid & 63, wv = tid >> 6;
  const __amdgpu_buffer_rsrc_t rs = __builtin_amdgcn_make_buffer_rsrc(p.ws, 0, WS_BYTES, 0x00020000);
  if (blockIdx.x < 48) {
    if (wv == 0) {
      const unsigned task = blockIdx.x >> 3, bit = (task & 1) ? 0x4000u : 0x1000u, xc = xcc_id();
      const unsigned base = (task >> 1) * 2 * MB + 7 * 32768 + blockIdx.x % 16 * 128;
      unsigned l0 = ~0u, l1 = ~0u, l2 = ~0u, l3 = ~0u;
      const u32x4 sv = {SENT, SENT, SENT, SENT};
      const unsigned long long tend = __builtin_amdgcn_s_memrealtime() + PROBE_TICKS;
      auto once = [&](unsigned pg) {
        const unsigned long long t0 = __builtin_amdgcn_s_memtime();
        __builtin_amdgcn_raw_buffer_store_b128(sv, rs, lane < 8 ? base + pg + lane * 16 : OOB, 0, 16);
        asm volatile("s_waitcnt vmcnt(0)" ::: "memory");
        return (unsigned)(__builtin_amdgcn_s_memtime() - t0); };
      __builtin_amdgcn_raw_buffer_store_b128(sv, rs, lane < 32 ? base + ((lane >> 3) & 1) * 0x2000u + (lane >> 4) * bit + (lane & 7) * 16 : OOB, 0, 16);
      asm volatile("s_waitcnt vmcnt(0)" ::: "memory");
      l0 = once(0); l1 = once(0x2000u); l2 = once(bit); l3 = once(bit | 0x2000u);
#pragma unroll 1
      for (int t = 0; t < 16 && __builtin_amdgcn_s_memrealtime() < tend; ++t) {
        if (min(l0, l2) < min(l1, l3) + 100) { l0 = min(l0, once(0)); l2 = min(l2, once(bit)); }
        if (min(l1, l3) < min(l0, l2) + 100) { l1 = min(l1, once(0x2000u)); l3 = min(l3, once(bit | 0x2000u)); }
      }
      const unsigned near = min(l1, l3) < min(l0, l2), lat[2] = {near ? l1 : l0, near ? l3 : l2};
      const int dv = max(-60, min(60, (int)(lat[0] - lat[1])));
      const unsigned side = (task & 1) ? (xc & 1) : ((xc >> 1) & 1);
      if (lane == 0) ((unsigned*)(p.ws + OFF_PAR))[blockIdx.x] = (unsigned)(64 + (side ? -dv : dv)) | ((near ^ (xc >> 2)) << 7)
          ;
    }
    return;
  }
  u32x4 xv = {0, 0, 0, 0};
  if (b < 128) xv = *(const u32x4*)(p.x + (b * 16 + wv) * 512 + lane * 8);
  const unsigned gt = b < 128 ? ~0u >> 5 : (b - 128) * 1024 + tid;
  if (gt < 45056) {
    const unsigned off = gt < 32768 ? gt * 16 : 2 * MB + (gt - 32768) * 16;
    u32x4 val = {SENT, SENT, SENT, SENT};
    if (gt >= 32768 && gt < 32768 + 4096) {
      const int l = gt & 63, ks = ((gt >> 6) & 3) + 4 * ((gt >> 11) & 1), tok = 2 * ((l >> 2) & 3) + (l & 1);
      const unsigned* src = (const unsigned*)(p.mem0 + tok * 512 + 64 * ks + 16 * (l >> 4) + 2 * ((l >> 1) & 1));
#pragma unroll
      for (int j = 0; j < 4; ++j) val[j] = min(src[2 * j], SENT - 1);
    }
    __builtin_amdgcn_raw_buffer_store_b128(val, rs, off, 0, 16);
  } else if ((gt - 45056) * 16 < CTL_ZERO) __builtin_amdgcn_raw_buffer_store_b128((u32x4){0, 0, 0, 0}, rs, OFF_CTL + (gt - 45056) * 16, 0, 16);
  __builtin_amdgcn_s_barrier();
  unsigned pf = 0;
  {
    const unsigned xc = xcc_id(), n = (b >> 3) * 800 + tid, r = (n >> 4) & 511, c = (xc & 3) * 1024 + (n & 15) * 64;
    const u16* s0 = n < 8192 ? p.Wv + r * 4096 + c : n < 16384 ? (xc < 4 ? p.Wo + (xc * 8192 + (n & 8191)) * 64 : p.Wk + r * 4096 + c) : p.Wq + (r + (xc >> 2) * 256) * 4096 + c;
    pf = *(const unsigned*)(tid < 800 && n < 20480 ? s0 : p.Wv);
  }
  if (b >= 200) {
    unsigned o = tid * 128;
    asm volatile("s_getpc_b64 vcc\nRSCAN_PF:\n\tv_min_u32 %0, RSCAN_CODE_END-RSCAN_PF-4, %0\n\tglobal_load_dword %0, %0, vcc\n\ts_waitcnt vmcnt(0)" : "+v"(o) :: "vcc", "scc", "memory");
  }
  if (b < 128) {
    float v[8], s = 0;
#pragma unroll
    for (int j = 0; j < 4; ++j) { v[2 * j] = __uint_as_float(xv[j] << 16); v[2 * j + 1] = __uint_as_float(xv[j] & 0xffff0000u); s += v[2 * j] + v[2 * j + 1]; }
#pragma unroll
    for (int o = 32; o; o >>= 1) s += __shfl_xor(s, o);
    const float mf = s * (1.f / 512.f); float q = 0;
#pragma unroll
    for (int j = 0; j < 8; ++j) q += (v[j] - mf) * (v[j] - mf);
#pragma unroll
    for (int o = 32; o; o >>= 1) q += __shfl_xor(q, o);
    const float m = rb(mf), sd = rb(sqrtf(rb(rb(q * (1.f / 512.f)) + 1e-5f)));
    u32x4 o4;
#pragma unroll
    for (int j = 0; j < 4; ++j) o4[j] = pk(rb(v[2 * j] - m) / sd, rb(v[2 * j + 1] - m) / sd);
    xt[lane * 16 + (wv ^ (lane & 7))] = o4;
    __syncthreads();
    *(u32x4*)(p.ws + OFF_XF + b * 16384 + tid * 16) = xt[tid ^ ((tid >> 4) & 7)];
  }
  asm volatile("" :: "v"(pf));
}

__global__ void __launch_bounds__(256, 1) main_kernel(Args p) {
  extern __shared__ __attribute__((aligned(16))) unsigned char smem[];
  const int tid = threadIdx.x, wv0 = __builtin_amdgcn_readfirstlane(tid >> 6);
  LAS unsigned* tab = (LAS unsigned*)smem + tid;
  const u16 *px = p.x, *pWq = p.Wq, *pWk = p.Wk, *pWv = p.Wv, *pWo = p.Wo, *pw1 = p.w1, *pb1 = p.b1, *pw2 = p.w2, *pb2 = p.b2, *pout = p.out;
  int pg1 = p.g1, i66 = p.g2, i67 = p.rho, i7 = p.tol;
  asm volatile("" : "+s"(px), "+s"(pWq), "+s"(pWk), "+s"(pWv), "+s"(pWo), "+s"(pw1), "+s"(pb1), "+s"(pw2), "+s"(pb2), "+s"(pout), "+s"(pg1), "+s"(i66), "+s"(i67), "+s"(i7));
  GAS unsigned* claim = (GAS unsigned*)(p.ws + OFF_CLAIM);
  int slot = __builtin_amdgcn_readfirstlane(xcc_id() * 32 + (blockIdx.x >> 3));
  asm volatile("global_load_dword v253, %0, off" :: "v"(p.ws + OFF_PAR + min(tid & 63, 47) * 4) : "v253", "memory");
  if (tid == 0) asm volatile("v_mov_b32 v252, 1\n\tglobal_atomic_swap v252, %0, v252, off sc0" :: "v"(claim + slot * 16) : "v252", "memory");
  unsigned kap[3] = {0, 0, 0};
  auto getkap = [&]() {
    unsigned vote;
    asm volatile("s_waitcnt vmcnt(16)\n\tv_mov_b32 %0, v253" : "=v"(vote) :: "memory");
    unsigned sv = vote & 127u;
    sv += __builtin_amdgcn_update_dpp(0u, sv, 0xb1, 0xf, 0xf, true); sv += __builtin_amdgcn_update_dpp(0u, sv, 0x4e, 0xf, 0xf, true); sv += __builtin_amdgcn_update_dpp(0u, sv, 0x141, 0xf, 0xf, true);
    const unsigned long long b0 = __builtin_amdgcn_ballot_w64(sv > 512u), b1 = __builtin_amdgcn_ballot_w64(vote & 128u);
#pragma unroll
    for (int w = 0; w < 3; ++w) kap[w] = (((b0 >> (16 * w)) & 1) ? 0x1000u : 0u) | (((b0 >> (16 * w + 8)) & 1) ? 0x4000u : 0u) |
                                         (__builtin_popcountll((b1 >> (16 * w)) & 0xffff) > 8 ? 0x2000u : 0u); };
  auto page = [&](int w, int x) { return kap[w] ^ (unsigned)((x & 1) << 14) ^ (unsigned)((x & 2) << 11); };
  auto pblock = [&](int pi) { return (unsigned)(pi >> 3) * 32768 + page(0, pi & 3) + ((pi >> 2) & 1) * 2048; };
#define FILLR(k, va_, da_) do { unsigned t_; asm volatile(S1R##k : [t] "=&s"(t_) : [base] "s"(rbase), [va] "v"(va_), [da] "s"(da_) : "scc", "memory"); __builtin_amdgcn_sched_barrier(0); } while (0)
#define FILLG(va_, da_) do { asm volatile(S1G : [base] "+s"(rbase), [off] "+s"(roff) : [step] "s"(rstep), [va] "v"(va_), [da] "s"(da_) : "scc", "memory"); __builtin_amdgcn_sched_barrier(0); } while (0)
  for (int pass = 0;; ++pass) {
  int lane = tid & 63, wv = tid >> 6;
  asm volatile("" : "+v"(lane), "+v"(wv));
  const int m = lane & 15, g4 = lane >> 4;
  const int rw = lane >> 2, ch = (lane & 3) ^ (2 * (lane >> 5));
  if (slot < 128) {
    const int xs = slot >> 5, ri = slot & 31, h = 8 * xs + (ri >> 2), g = ri & 3, piece = 4 * ri + xs, hpos = 4 * (h & 7) + (h >> 3);
    const unsigned rbase = 32768 + wv0 * 32768;
    const u32x4 dO = srd(pWo, 4 * MB), dV = srd(pWv, 4 * MB);
    const unsigned aO = ((h * 128 + rw) * 512 + 128 * g + 32 * wv + 8 * ch) * 2;
    FILLR(0, aO, dO);
    const unsigned aV = ((4 * (wv >> 1) + 8 * (rw >> 2) + (rw & 3)) * 4096 + h * 128 + 64 * (wv & 1) + 8 * ((lane & 3) ^ ((4 - (rw >> 2)) & 3))) * 2;
    tab[19 * 256] = (rw * 4096 + h * 128 + 32 * wv + 8 * ch) * 2;
    tab[20 * 256] = aV;
    FILLR(1, aO, dO);
    const int pg = ((g4 & 1) << 1) | (g4 >> 1);
    tab[5 * 256] = (lane & 2) ? 0xEEEEu : 0x4444u;
    tab[6 * 256] = 32768 + wv * 32768 + (8 * g4 + (m >> 2)) * 64 + (((m & 3) ^ (2 * (g4 & 1))) * 16);
    tab[7 * 256] = LB + pg * 1088 + wv * 256 + m * 16;
    tab[8 * 256] = LB + (m & 3) * 1088 + (8 * (g4 >> 1) + (m >> 2)) * 16 + (g4 & 1) * 8;
    tab[9 * 256] = 32768 + lane * 16;
    FILLR(2, aV, dV);
    const unsigned vrd = 32768 + 8192 + ((m >> 2) & 1) * 4096 + (4 * (m >> 3) + (m & 3) + 16 * g4) * 16;
    tab[10 * 256] = wv < 2 ? LB + 16384 + wv * 2048 + lane * 16 : vrd;
    tab[21 * 256] = vrd;
    tab[15 * 256] = ((lane >> 1) * 16 + g * 4 + wv) * 8192 + h * 256 + (lane & 1) * 128;
    tab[11 * 256] = lane < 32 ? ~0u : 0u;
    tab[12 * 256] = lane < 32 ? 0u : ~0u;
    tab[13 * 256] = OFF_FLAG + h * 64;
    tab[14 * 256] = wv * 8192 + lane * 16;
    tab[29 * 256] = LB + 8192 + lane * 16;
    FILLR(3, aV, dV);
    getkap();
    FILLR(4, aV, dV);
    const unsigned inpiece = 2 * MB + (piece >> 6) * 32768 + (piece & 63) * 64 + (2 * (m & 1) + (g4 & 1)) * 16 + ((m >> 1) & 1) * 8;
    const unsigned kvo = (wv == 1 ? 8192 : (wv & 1) * 4096) + lane * 16;
    tab[0 * 256] = 2 * MB + (wv >> 1) * 32768 + page(1, xs) + (wv & 1) * 2048 + lane * 16;
    tab[1 * 256] = pblock((2 * g + (wv >> 1)) * 16 + (2 * (wv & 1) + (m >> 3)) * 4 + pg) + hpos * 64 + (m & 7) * 8;
    tab[2 * 256] = wv == 0 ? pblock(piece) + lane * 16 : wv == 1 ? OFF_KV + h * 16384 + lane * 128 : LB + 16384 + (wv - 2) * 2048 + lane * 16;
    FILLR(5, aV, dV);
    tab[3 * 256] = wv == 0 ? (lane >= 32 ? OOB : page(1, m >> 2) + inpiece) : 32768 + kvo;
    tab[4 * 256] = wv == 0 ? (lane < 32 && m < 4 ? (kap[1] ^ 8192u) + inpiece : OOB) : OFF_KV + h * 16384 + kvo;
    tab[17 * 256] = (~kap[0] & 8192u) + lane * 32;
    tab[18 * 256] = lane == 0 ? (~kap[0] & 8192u) + piece * 32 : OOB;
    FILLR(6, aV, dV);
#pragma unroll
    for (int t = 0; t < 2; ++t) tab[(22 + t) * 256] = 32768 + wv * 32768 + (8 * g4 + (m >> 2)) * 64 + (((2 * ((m >> 1) & 1) + t) ^ (2 * (g4 & 1))) * 16) + (m & 1) * 8;
    tab[24 * 256] = 32768 + m * 64 + ((g4 ^ ((4 - (m >> 2)) & 3)) * 16);
    const int jm = 4 * (m & 1) + (m & 2) + ((m >> 2) & 1);
#pragma unroll
    for (int j = 0; j < 4; ++j) tab[(25 + j) * 256] = ((2 * j == jm) ? 0x3F80u : 0u) | ((2 * j + 1 == jm) ? 0x3F800000u : 0u);
    FILLR(7, aV, dV);
  } else if ((slot & 31) < 16) {
    const int y = (slot >> 5) - 4, i = slot & 15, h = 8 * y + (i >> 1);
    unsigned rbase = 49152 + wv0 * 28672, roff = 0; const unsigned rstep = 16 * 8192;
    const u32x4 dK = srd(pWk, 4 * MB);
    const unsigned aK = (rw * 4096 + h * 128 + 32 * wv + 8 * ch) * 2;
    FILLG(aK, dK);
    tab[0 * 256] = lane * 16;
    tab[1 * 256] = OFF_XF + wv * 8192 + lane * 16;
    tab[2 * 256] = OFF_KV + h * 16384 + wv * 1024 + lane * 16;
    tab[3 * 256] = ((4 * g4 + (m >> 2)) + 16 * (m & 3)) * 16;
    FILLG(aK, dK);
    tab[5 * 256] = (lane & 2) ? 0xEEEEu : 0x4444u;
    tab[6 * 256] = 8192 + (32 * wv + 8 * (m >> 2) + (m & 3)) * 32 + g4 * 8;
    tab[7 * 256] = 8192 + (8 * g4 + (m >> 2)) * 32 + (m & 3) * 8;
    FILLG(aK, dK);
    tab[8 * 256] = OFF_OF + (4 * h + wv) * 1024 + lane * 16;
    tab[9 * 256] = OFF_FLAG + h * 64;
    tab[11 * 256] = aK;
    tab[12 * 256] = 49152 + wv * 28672 + (8 * g4 + (m >> 2)) * 64 + (((m & 3) ^ (2 * (g4 & 1))) * 16);
    FILLG(aK, dK);
    getkap();
    FILLG(aK, dK);
    tab[4 * 256] = 2 * MB + (wv >> 1) * 32768 + (kap[1] ^ 8192u) + (wv & 1) * 2048 + lane * 16;
    FILLG(aK, dK);
#pragma unroll
    for (int r = 0; r < 4; ++r) tab[(13 + r) * 256] = __float_as_uint(4 * g4 + r <= m ? 0.f : -1e30f);
    FILLG(aK, dK);
  } else {
    const int y = (slot >> 5) - 4, i = slot & 15, tb = 4 * y + (i >> 2), cq = i & 3, g0 = (cq * 4 + wv) * 3, kb0 = min(11 * wv, 32);
    unsigned rbase = 49152 + wv0 * 28672, roff = min((cq * 4 + wv0) * 3, 42) * 64; const unsigned rstep = 16 * 2752;
    const u32x4 d1 = srd(pw1, 512 * 1376 * 2);
    const unsigned a1 = rw * 2752 + ch * 16;
    {
      const unsigned n = min(i * 256 + tid, 2751) + y * 2752;
      asm volatile("global_load_dword v250, %0, off\n\tglobal_load_dword v251, %1, off" :: "v"(pw1 + n * 64), "v"(pw2 + n * 64) : "v250", "v251", "memory"); }
    for (int k = 0; k < WDELAY; ++k) __builtin_amdgcn_s_sleep(16);
#pragma unroll 1
    for (int k = 0; k < 7; ++k) FILLG(a1, d1);
    tab[0 * 256] = lane * 16;
    tab[1 * 256] = OFF_XF + wv * 8192 + lane * 16;
    tab[2 * 256] = a1;
    tab[3 * 256] = 49152 + wv * 28672 + (8 * g4 + (m >> 2)) * 64 + (((m & 3) ^ (2 * (g4 & 1))) * 16);
#pragma unroll
    for (int k = 0; k < 3; ++k) tab[(4 + k) * 256] = g0 + k < 43 ? OFF_HF + (g0 + k) * 1024 + lane * 16 : OOB;
    tab[7 * 256] = g4 * 16;
    tab[8 * 256] = OFF_HF + kb0 * 1024 + lane * 16;
    tab[9 * 256] = wv < 3 ? OFF_HF + kb0 * 1024 + lane * 16 : OOB;
    tab[10 * 256] = (kb0 * 32 + rw) * 1024 + cq * 256 + ch * 16;
    tab[11 * 256] = cq * 256 + wv * 64 + g4 * 16;
    tab[12 * 256] = OFF_G + m * 1024 + cq * 256 + wv * 64 + g4 * 16;
    tab[13 * 256] = (wv * 1024 + rw) * 1024 + i * 64 + ch * 16;
    tab[14 * 256] = OFF_OF + (y >> 1) * 131072 + wv * 32768 + (m >> 3) * 1024 + (8 * (y & 1) + (m & 7) + 16 * g4) * 16;
    tab[15 * 256] = wv < 2 && m < 8 ? (8 * y + m) * 1024 + i * 64 + g4 * 16 + wv * 8 : OOB;
    tab[19 * 256] = wv < 2 ? (16 * y + m) * 1024 + i * 64 + g4 * 16 + wv * 8 : OOB;
    tab[16 * 256] = OFF_CNT + 64 * 128 + tb * 128;
    tab[17 * 256] = OFF_CNT + 96 * 128;
    tab[18 * 256] = OFF_CNT;
  }
  if (pass) break;
  if (tid == 0) {
    unsigned s2 = slot, old;
    asm volatile("s_waitcnt vmcnt(28)\n\tv_mov_b32 %0, v252" : "=v"(old) :: "memory");
    __hip_atomic_store(claim + blockIdx.x * 16 + 8, 1u, RLX_AGENT);
    if (old) {
      unsigned k = __hip_atomic_fetch_add(claim + 4096, 1u, RLX_AGENT), sp = 0;
#pragma unroll 1
      for (unsigned i = 0; i < 256u; ++i) while (__hip_atomic_load(claim + i * 16 + 8, RLX_AGENT) == 0u && ++sp < (1u << 20)) __builtin_amdgcn_s_sleep(2);
#pragma unroll 1
      for (s2 = 0; s2 < 255u; ++s2) if (__hip_atomic_load(claim + s2 * 16, RLX_AGENT) == 0u && k-- == 0u) break;
    }
    ((LAS unsigned*)smem)[8191] = s2;
  }
  __syncthreads();
  const int s3 = __builtin_amdgcn_readfirstlane(((LAS unsigned*)smem)[8191]);
  if (s3 == slot) break;
  slot = s3;
  asm volatile("s_waitcnt vmcnt(0)" ::: "memory");
  __syncthreads();
  }
  const unsigned pbase = pblock(4 * (slot & 31) + ((slot >> 5) & 3));
  if (slot < 128) {
    const __amdgpu_buffer_rsrc_t rs = __builtin_amdgcn_make_buffer_rsrc(p.ws, 0, WS_BYTES, 0x00020000);
    const u32x4 sv = {SENT, SENT, SENT, SENT};
    if (slot >= 6) {
      const unsigned k0 = (slot < 64 ? kap[1] : kap[2]) ^ (unsigned)((slot & 16) << 10) ^ (unsigned)((slot & 32) << 7), gb = 2 * MB + slot * 32768 + tid * 16;
#pragma unroll
      for (int k = 0; k < 5; ++k) __builtin_amdgcn_raw_buffer_store_b128(sv, rs, gb + (k0 ^ (k == 4 ? 0x2000u : (unsigned)(((k & 1) << 14) | ((k & 2) << 11)))), 0, 16);
    }
    __builtin_amdgcn_raw_buffer_store_b128(sv, rs, (pbase ^ (tid < 128 ? 0x4000u : 0x1000u)) + (1 + (tid >> 7)) * (MB / 2) + (tid & 127) * 16, 0, 16);
  }
  const int wv = wv0;
  int v0 = tid * 4, role = slot < 128 ? 0 : (slot & 16) ? 2 : 1, i65 = role == 0 ? pg1 : role == 1 ? (slot & 1) : 4 * ((slot >> 5) - 4) + ((slot & 15) >> 2);
  const bool isw = role == 2;
  const int gw = ((slot & 3) * 4 + wv) * 3;
  u32x4 d40 = srd(p.ws, WS_BYTES), d44 = srd(isw ? (const void*)pw1 : (const void*)p.ws, isw ? 512 * 1376 * 2 : WS_BYTES), d48 = srd(isw ? pw2 : pWq, isw ? 1376 * 512 * 2 : 4 * MB),
        d52 = srd(isw ? pWo : pWk, 4 * MB), d56 = srd(isw ? pb1 : pWv, isw ? 1376 * 2 : 4 * MB), d60 = srd(isw ? px : pWo, isw ? 2 * MB : 4 * MB),
        i96 = {(unsigned)min(gw, 42) * 64, isw ? (unsigned)min(gw + 1, 42) * 64 : 4 * MB + 7 * 32768 + (~kap[2] & 0x2000u) + ((kap[2] & 0x5000u) ? 0u : 0x1000u), isw ? (unsigned)min(gw + 2, 42) * 64 : kap[1] ^ kap[2], isw ? (unsigned)((slot >> 5) - 4) : pbase + (wv & 1) * 1024};
  int i64 = wv;
  u32x4 d88 = srd(pout, 2 * MB), d92 = srd(pb2, 1024);
  unsigned* dbg = p.dbg + (slot * 4 + wv) * 1024;
  asm volatile("s_waitcnt lgkmcnt(0)" ::: "memory");
  asm volatile(ROLE_ASM : "+{v0}"(v0), "+{s4}"(role), "+{s7}"(i7), "+{s[40:43]}"(d40), "+{s[44:47]}"(d44), "+{s[48:51]}"(d48), "+{s[52:55]}"(d52), "+{s[56:59]}"(d56), "+{s[60:63]}"(d60),
               "+{s64}"(i64), "+{s65}"(i65), "+{s66}"(i66), "+{s67}"(i67), "+{s[68:69]}"(dbg), "+{s[88:91]}"(d88), "+{s[92:95]}"(d92), "+{s[96:99]}"(i96) :: ROLE_CLOB);
}

extern "C" void kernel_launch(void* const* d_in, const int* in_sizes, int n_in, void* d_out, int out_size, void* d_ws, size_t ws_size, hipStream_t stream) {
  Args a;
  a.x = (const u16*)d_in[0]; a.mem0 = (const u16*)d_in[1]; a.Wq = (const u16*)d_in[2]; a.Wk = (const u16*)d_in[3]; a.Wv = (const u16*)d_in[4]; a.Wo = (const u16*)d_in[5];
  a.w1 = (const u16*)d_in[8]; a.b1 = (const u16*)d_in[9]; a.w2 = (const u16*)d_in[10]; a.b2 = (const u16*)d_in[11];
  a.out = (u16*)d_out;
  a.ws = (unsigned char*)(((size_t)d_ws + (2 * MB - 1)) & ~(size_t)(2 * MB - 1));
  a.dbg = (unsigned*)a.ws; a.g1 = 44; a.g2 = 28; a.rho = 300; a.tol = 32;
  (void)hipFuncSetAttribute((const void*)main_kernel, hipFuncAttributeMaxDynamicSharedMemorySize, 163840);
  (void)hipFuncSetAttribute((const void*)prep_kernel, hipFuncAttributeMaxDynamicSharedMemorySize, 98304);
  prep_kernel<<<256, 1024, 98304, stream>>>(a);
  main_kernel<<<256, 256, 163840, stream>>>(a);
}
```
